```python
import torch

def _softmax(x, axis=-1):
    return torch.softmax(x, dim=axis)
def _gelu(x):
    return torch.nn.functional.gelu(x, approximate="tanh")
def _silu(x):
    return torch.nn.functional.silu(x)
def _sigmoid(x):
    return torch.sigmoid(x)
def _layernorm(x, axis=-1, eps=1e-5):
    m = x.mean(dim=axis, keepdim=True)
    v = x.var(dim=axis, keepdim=True, unbiased=False)
    return (x - m) / torch.sqrt(v + eps)
def _scatter_add(tgt, idx, src, axis=0):
    return tgt.clone().index_add_(axis, idx, src)
def _segment_sum(src, seg, n):
    out = torch.zeros((n,) + src.shape[1:], dtype=src.dtype)
    return out.index_add_(0, seg, src)
def _pad(x, pw):
    flat = tuple(v for pair in reversed(pw) for v in pair)
    return torch.nn.functional.pad(x, flat)

kernel_name = 'sequential_preLN__sliding_block_cached__glu_gated_glu__decode'

def setup_inputs():

    _w = (lambda a: (lambda t: t.to(torch.bfloat16) if t.is_floating_point() else t)(torch.as_tensor(a)))
    def _A_setup():
        B, Sq, D = 4, 1, 512
        Hq, Hkv, Sk, Dh = 32, 8, 8192, 128
        assert Hq % Hkv == 0, (Hq, Hkv)
        return {
            "x": _w((lambda *s: torch.randn(*s, dtype=torch.bfloat16) * 0.05)(B, Sq, D)),
            "k_cache": _w((lambda *s: torch.randn(*s, dtype=torch.bfloat16) * 3.2)(B, Hkv, Sk, Dh)),
            "v_cache": _w((lambda *s: torch.randn(*s, dtype=torch.bfloat16) * 0.4)(B, Hkv, Sk, Dh)),
            "wq": _w((lambda *s: torch.randn(*s, dtype=torch.bfloat16) * 3.2)(D, Hq * Dh) * (D ** -0.5)),
            "wo": _w((lambda *s: torch.randn(*s, dtype=torch.bfloat16) * 0.4)(Hq * Dh, D) * ((Hq * Dh) ** -0.5)),
        }
    _A_in = _A_setup()

    def _B_setup():
        B, S, D, F = 4, 1, 512, 1792
        return {
            "x": _w((lambda *s: torch.randn(*s, dtype=torch.bfloat16) * 0.05)(B, S, D)),
            "w_gate": _w((lambda *s: torch.randn(*s, dtype=torch.bfloat16) * 0.1)(D, F)),
            "w_up": _w((lambda *s: torch.randn(*s, dtype=torch.bfloat16) * 0.05)(D, F)),
            "w_down": _w((lambda *s: torch.randn(*s, dtype=torch.bfloat16) * 0.05)(F, D)),
        }
    _B_in = _B_setup()

    return {
        "A_x": _A_in["x"],
        "A_k_cache": _A_in["k_cache"],
        "A_v_cache": _A_in["v_cache"],
        "A_wq": _A_in["wq"],
        "A_wo": _A_in["wo"],
        "B_x": _B_in["x"],
        "B_w_gate": _B_in["w_gate"],
        "B_w_up": _B_in["w_up"],
        "B_w_down": _B_in["w_down"],
    }

def reference(A_x, A_k_cache, A_v_cache, A_wq, A_wo, B_x, B_w_gate, B_w_up, B_w_down):
    def _A_ref(x, k_cache, v_cache, wq, wo):
        B, Sq, D = x.shape
        Hq, Dh = 32, 128
        Hkv, Sk = k_cache.shape[1], k_cache.shape[2]
        W = min(4096, max(256, Sk // 4))
        rep = Hq // Hkv
        q = (lambda x, *d: x.permute(*d))((x @ wq).reshape(B, Sq, Hq, Dh), 0, 2, 1, 3)
        k = (lambda x, n, axis: torch.repeat_interleave(x, n, dim=axis))(k_cache[:, :, Sk - W:, :], rep, axis=1)
        v = (lambda x, n, axis: torch.repeat_interleave(x, n, dim=axis))(v_cache[:, :, Sk - W:, :], rep, axis=1)
        scores = torch.einsum("bhqd,bhkd->bhqk", q, k) * (Dh ** -0.5)
        p = _softmax(scores, axis=-1)
        o = torch.einsum("bhqk,bhkd->bhqd", p, v)
        return (lambda x, *d: x.permute(*d))(o, 0, 2, 1, 3).reshape(B, Sq, Hq * Dh) @ wo

    def _B_ref(x, w_gate, w_up, w_down):
        g = torch.einsum("bsd,df->bsf", x, w_gate)
        u = torch.einsum("bsd,df->bsf", x, w_up)
        h = _sigmoid(g) * u
        return torch.einsum("bsf,fd->bsd", h, w_down)

    x = A_x
    h = x + _A_ref(_layernorm(x, axis=-1), A_k_cache, A_v_cache, A_wq, A_wo)
    h2 = h + _B_ref(_layernorm(h, axis=-1), B_w_gate, B_w_up, B_w_down)
    return h2
```

```cpp
#include <hip/hip_runtime.h>
#include <hip/hip_bf16.h>
#include <stdint.h>

typedef unsigned short u16;
__device__ __forceinline__ float bf2f(u16 v){ return __uint_as_float(((unsigned)v)<<16); }
__device__ __forceinline__ u16 f2bf(float f){ unsigned u=__float_as_uint(f); u += 0x7fffu + ((u>>16)&1u); return (u16)(u>>16); }
__device__ __forceinline__ float rbf(float f){ return bf2f(f2bf(f)); }

constexpr int B=4, D=512, HQ=32, HKV=8, SK=8192, DH=128, W=2048, F=1792;

__device__ float block_sum(float v, float* red){
  for(int m=32;m>=1;m>>=1) v += __shfl_xor(v,m);
  int w = threadIdx.x>>6; int nw = blockDim.x>>6;
  __syncthreads();
  if((threadIdx.x&63)==0) red[w]=v;
  __syncthreads();
  float s=0; for(int i=0;i<nw;i++) s+=red[i];
  return s;
}
__device__ void ln_row(const float* hrow_f, float* xs, float* red){
  int t=threadIdx.x;
  float a=hrow_f[t], b=hrow_f[t+256];
  float s=block_sum(a+b,red); float m=s/512.f;
  float d0=a-m,d1=b-m;
  float v=block_sum(d0*d0+d1*d1,red)/512.f;
  float r=1.f/sqrtf(v+1e-5f);
  xs[t]=rbf(d0*r); xs[t+256]=rbf(d1*r);
  __syncthreads();
}
__global__ void k_q(const u16* x,const u16* wq,float* q){
  __shared__ float xf[512], xs[512], red[8];
  int b=blockIdx.y,t=threadIdx.x;
  xf[t]=bf2f(x[b*D+t]); xf[t+256]=bf2f(x[b*D+t+256]); __syncthreads();
  ln_row(xf,xs,red);
  int c=blockIdx.x*256+t; float acc=0;
  for(int k=0;k<D;k++) acc+=xs[k]*bf2f(wq[(size_t)k*4096+c]);
  q[b*4096+c]=rbf(acc);
}
__global__ void k_attn(const float* q,const u16* kc,const u16* vc,float* o){
  __shared__ float qs[128], sc[W], red[8];
  int h=blockIdx.x,b=blockIdx.y,t=threadIdx.x,kv=h/4;
  if(t<128) qs[t]=q[b*4096+h*128+t];
  __syncthreads();
  const u16* K=kc+((size_t)(b*HKV+kv)*SK+(SK-W))*DH;
  const u16* V=vc+((size_t)(b*HKV+kv)*SK+(SK-W))*DH;
  float mx=-1e30f;
  for(int j=t;j<W;j+=256){ float a=0; for(int d=0;d<128;d++) a+=qs[d]*bf2f(K[(size_t)j*128+d]); a*=0.08838834764831845f; sc[j]=a; mx=fmaxf(mx,a);}
  for(int m=32;m>=1;m>>=1) mx=fmaxf(mx,__shfl_xor(mx,m));
  __syncthreads(); if((t&63)==0) red[t>>6]=mx; __syncthreads();
  mx=fmaxf(fmaxf(red[0],red[1]),fmaxf(red[2],red[3]));
  float s=0; for(int j=t;j<W;j+=256){ float p=__expf(sc[j]-mx); sc[j]=p; s+=p; }
  s=block_sum(s,red);
  if(t<128){ float a=0; for(int j=0;j<W;j++) a+=sc[j]*bf2f(V[(size_t)j*128+t]); o[b*4096+h*128+t]=rbf(a/s);}
}
__global__ void k_wo(const float* o,const u16* wo,const u16* x,float* h){
  int b=blockIdx.y,c=blockIdx.x*256+threadIdx.x; float acc=0;
  for(int k=0;k<4096;k++) acc+=o[b*4096+k]*bf2f(wo[(size_t)k*512+c]);
  h[b*512+c]=rbf(bf2f(x[b*512+c])+rbf(acc));
}
__global__ void k_gu(const float* h,const u16* wg,const u16* wu,float* hh){
  __shared__ float xs[512], red[8];
  int b=blockIdx.y,t=threadIdx.x;
  ln_row(h+b*512,xs,red);
  int c=blockIdx.x*256+t; float g=0,u=0;
  for(int k=0;k<D;k++){ g+=xs[k]*bf2f(wg[(size_t)k*F+c]); u+=xs[k]*bf2f(wu[(size_t)k*F+c]); }
  g=rbf(g);u=rbf(u);
  float s=rbf(1.f/(1.f+__expf(-g)));
  hh[b*F+c]=rbf(s*u);
}
__global__ void k_dn(const float* hh,const u16* wd,const float* h,u16* out){
  int b=blockIdx.y,c=blockIdx.x*256+threadIdx.x; float acc=0;
  for(int k=0;k<F;k++) acc+=hh[b*F+k]*bf2f(wd[(size_t)k*512+c]);
  out[b*512+c]=f2bf(h[b*512+c]+rbf(acc));
}

extern "C" void kernel_launch(void* const* d_in, const int* in_sizes, int n_in,
                              void* d_out, int out_size, void* d_ws, size_t ws_size,
                              hipStream_t stream) {
  const u16* x=(const u16*)d_in[0]; const u16* kc=(const u16*)d_in[1]; const u16* vc=(const u16*)d_in[2];
  const u16* wq=(const u16*)d_in[3]; const u16* wo=(const u16*)d_in[4];
  const u16* wg=(const u16*)d_in[6]; const u16* wu=(const u16*)d_in[7]; const u16* wd=(const u16*)d_in[8];
  float* ws=(float*)d_ws; float* q=ws; float* o=ws+16384; float* h=ws+32768; float* hh=ws+40960;
  k_q<<<dim3(16,B),256,0,stream>>>(x,wq,q);
  k_attn<<<dim3(HQ,B),256,0,stream>>>(q,kc,vc,o);
  k_wo<<<dim3(2,B),256,0,stream>>>(o,wo,x,h);
  k_gu<<<dim3(7,B),256,0,stream>>>(h,wg,wu,hh);
  k_dn<<<dim3(2,B),256,0,stream>>>(hh,wd,h,(u16*)d_out);
}
```

```cpp
#include <hip/hip_runtime.h>
#include <stdint.h>

#define PROF 0
#define DDELAY 200
#define PACE 16
#define HPDELAY 800
#define QDELAY 300
#define KLIN 0
#define HELLODELAY 600
#define WO_EARLY 0
#define RDELAY 100

namespace {
typedef unsigned short u16;
typedef unsigned long long u64;
typedef unsigned v4u __attribute__((ext_vector_type(4)));
typedef unsigned v2u __attribute__((ext_vector_type(2)));
typedef float f32x4 __attribute__((ext_vector_type(4)));
typedef float f32x2 __attribute__((ext_vector_type(2)));
typedef short bf16x8 __attribute__((ext_vector_type(8)));
typedef __bf16 bf16x2 __attribute__((ext_vector_type(2)));
#define GAS __attribute__((address_space(1)))
#define LAS __attribute__((address_space(3)))
typedef __amdgpu_buffer_rsrc_t rsrc_t;

constexpr int NB = 4, DM = 512, HKV = 8, SK = 8192, WIN = 2048, DH = 128, FF = 1792, NQ = 4096;
constexpr int NSPL = 6, NDCU = 56;
constexpr int AUX_NT = 2, AUX_SC1 = 16;
constexpr unsigned SENT = 0xFFFFFFFFu;
constexpr int WS_EPOCH = 0, WS_HELLO = 4096, WS_QG = 8192, WS_OREC = WS_QG + 8 * 4 * 4 * 512 * 8, OREC_SZ = 1152;
constexpr int WS_HP = WS_OREC + 8 * 4 * NSPL * OREC_SZ, WS_OP = WS_HP + 8 * 4 * 512 * 2, WS_END = WS_OP + NDCU * 8192;
constexpr int WS_PROF = 2 << 20;
constexpr int L_FLAG = 0, L_QRED = 256, L_QS = L_QRED + 4096, L_MW = L_QS + 1024, L_LW = L_MW + 256, L_OPART = L_LW + 256;
constexpr int L_OA = L_OPART + 12 * 2048, L_CRED = L_OA + 4096, L_SRED = L_CRED + 4096, L_STAT = L_SRED + 1024, L_USED = L_STAT + 64;
constexpr int L_TOTAL = 84 * 1024;
static_assert(L_USED <= L_TOTAL, "lds");
enum { F_QCNT = 0, F_CCNT = 1, F_DONE = 2, F_QS = 4, F_OA = 8, F_MW = 16, F_OPF = 32, F_VIS = 48 };
constexpr unsigned SPIN_G = 1u << 17, SPIN_L = 1u << 21;

__device__ __forceinline__ u64 rt() { return __builtin_amdgcn_s_memrealtime(); }
__device__ __forceinline__ rsrc_t mk(const void* p, unsigned bytes) { return __builtin_amdgcn_make_buffer_rsrc((void*)p, 0, bytes, 0x00020000); }
__device__ __forceinline__ f32x4 mfma(v4u a, v4u b, f32x4 c) { return __builtin_amdgcn_mfma_f32_16x16x32_bf16(__builtin_bit_cast(bf16x8, a), __builtin_bit_cast(bf16x8, b), c, 0, 0, 0); }
__device__ __forceinline__ unsigned pk(float lo, float hi) { return __builtin_bit_cast(unsigned, __builtin_convertvector(f32x2{lo, hi}, bf16x2)); }
__device__ __forceinline__ float blo(unsigned v) { return __builtin_bit_cast(float, v << 16); }
__device__ __forceinline__ float bhi(unsigned v) { return __builtin_bit_cast(float, v & 0xffff0000u); }
__device__ __forceinline__ unsigned plo(unsigned a, unsigned b) { return __builtin_amdgcn_perm(b, a, 0x05040100u); }
__device__ __forceinline__ unsigned phi(unsigned a, unsigned b) { return __builtin_amdgcn_perm(b, a, 0x07060302u); }
__device__ __forceinline__ unsigned nosent(unsigned v) { return v == SENT ? 0xFFFEFFFFu : v; }
template <int C> __device__ __forceinline__ float dpp(float x) { return __builtin_bit_cast(float, __builtin_amdgcn_mov_dpp(__builtin_bit_cast(int, x), C, 0xf, 0xf, true)); }
template <int C> __device__ __forceinline__ unsigned dppu(unsigned x) { return (unsigned)__builtin_amdgcn_mov_dpp((int)x, C, 0xf, 0xf, true); }
constexpr int XOR1 = 0xB1, XOR2 = 0x4E, ROR4 = 0x124, ROR8 = 0x128;
__device__ __forceinline__ float x16_sum(float x) { auto s = __builtin_amdgcn_permlane16_swap(__float_as_uint(x), __float_as_uint(x), false, false); return __uint_as_float(s[0]) + __uint_as_float(s[1]); }
__device__ __forceinline__ float x32_sum(float x) { auto s = __builtin_amdgcn_permlane32_swap(__float_as_uint(x), __float_as_uint(x), false, false); return __uint_as_float(s[0]) + __uint_as_float(s[1]); }
__device__ __forceinline__ float x16_max(float x) { auto s = __builtin_amdgcn_permlane16_swap(__float_as_uint(x), __float_as_uint(x), false, false); return fmaxf(__uint_as_float(s[0]), __uint_as_float(s[1])); }
__device__ __forceinline__ float x32_max(float x) { auto s = __builtin_amdgcn_permlane32_swap(__float_as_uint(x), __float_as_uint(x), false, false); return fmaxf(__uint_as_float(s[0]), __uint_as_float(s[1])); }
__device__ __forceinline__ float dot2(unsigned a, unsigned b, float c) { return __builtin_amdgcn_fdot2_f32_bf16(__builtin_bit_cast(bf16x2, a), __builtin_bit_cast(bf16x2, b), c, false); }
#define CBAR() asm volatile("" ::: "memory")
#define SCHED() __builtin_amdgcn_sched_barrier(0)
#define DRAIN() asm volatile("s_waitcnt vmcnt(0)" ::: "memory")
#define WGBAR() do { __builtin_amdgcn_sched_barrier(0); asm volatile("s_waitcnt lgkmcnt(0)" ::: "memory"); __builtin_amdgcn_s_barrier(); asm volatile("" ::: "memory"); __builtin_amdgcn_sched_barrier(0); } while (0)
__device__ __forceinline__ bool okv(v4u r) { return (r.x != SENT) & (r.y != SENT) & (r.z != SENT) & (r.w != SENT); }
__device__ __forceinline__ void lds_wait(volatile LAS unsigned* f, int n, int lane) {
  const int j = lane < n ? lane : 0;
  _Pragma("nounroll") for (unsigned s = 0; s < SPIN_L; ++s) { if (__all(f[j] != 0u)) break; }
  CBAR();
}
#define STAMP(k) do {} while (0)
#define PDUMP() do {} while (0)

__device__ __forceinline__ void tr4(const v2u (&s)[8], v4u (&o)[4]) {
#pragma unroll
  for (int m = 0; m < 4; ++m) {
    o[0][m] = plo(s[2 * m].x, s[2 * m + 1].x); o[1][m] = phi(s[2 * m].x, s[2 * m + 1].x);
    o[2][m] = plo(s[2 * m].y, s[2 * m + 1].y); o[3][m] = phi(s[2 * m].y, s[2 * m + 1].y);
  }
}
__device__ __forceinline__ void tr8(const v4u (&s)[8], v4u (&o)[8]) {
#pragma unroll
  for (int m = 0; m < 4; ++m)
#pragma unroll
    for (int d = 0; d < 4; ++d) { o[2 * d][m] = plo(s[2 * m][d], s[2 * m + 1][d]); o[2 * d + 1][m] = phi(s[2 * m][d], s[2 * m + 1][d]); }
}

__global__ void __launch_bounds__(1024) fused_decode(const u16* __restrict__ x, const u16* __restrict__ kc, const u16* __restrict__ vc,
                                                     const u16* __restrict__ wq, const u16* __restrict__ wo, const u16* __restrict__ wg,
                                                     const u16* __restrict__ wu, const u16* __restrict__ wd, u16* __restrict__ out, unsigned char* ws) {
  const u64 tstart = rt();
  SCHED();
  __shared__ __attribute__((aligned(16))) unsigned char smem[L_TOTAL];
  LAS unsigned char* L = (LAS unsigned char*)smem;
  volatile LAS unsigned* FL = (volatile LAS unsigned*)(L + L_FLAG);
  const int lane = threadIdx.x & 63, w = (__builtin_amdgcn_readfirstlane(threadIdx.x >> 6) + 12) & 15, tid = w * 64 + lane;
  asm volatile("" :: "s"(x), "s"(kc), "s"(vc), "s"(wq), "s"(wo), "s"(wg), "s"(wu), "s"(wd), "s"(out), "s"(ws));
  const int c = blockIdx.x, g = c & 7, i = c >> 3;
  const int gq = lane >> 4, r16 = lane & 15;
  const bool isAtt = i < 24;
  const int b = i / NSPL, s = i - b * NSPL;
  const bool isC = isAtt && s >= 4;
  const int cc = 2 * b + (s - 4);
  const int dI = (i - 24) * 8 + g;
  const bool isD = !isAtt && dI < NDCU;
  const int ntile = s < 4 ? 11 : 10, tile0 = s < 4 ? 11 * s : 44 + 10 * (s - 4);
  const rsrc_t rws = mk(ws, WS_END);

  if (w < 12) {
    if (isAtt) {
      const bool act = w < ntile;
      v4u kf[8], vf[8];
      const unsigned kvbase = ((unsigned)(b * HKV + g) * SK + (SK - WIN) + 32u * (tile0 + w)) * 256u;
      const rsrc_t rk = mk(kc, NB * HKV * SK * 256u), rv = mk(vc, NB * HKV * SK * 256u);
      const unsigned ko = KLIN ? kvbase + lane * 16 : kvbase + (r16 >> 2) * 256 + (r16 & 3) * 64 + gq * 16;
      const unsigned vo = kvbase + lane * 16;
      if (act) {
#pragma unroll
        for (int t = 0; t < (PACE < 2 ? PACE : 2); ++t) kf[t] = __builtin_amdgcn_raw_buffer_load_b128(rk, ko + t * 1024, 0, AUX_NT);
      }
      WGBAR();
      if (act) {
#pragma unroll
        for (int t = (PACE < 2 ? PACE : 2); t < 16; ++t) {
          SCHED();
          if (t >= PACE) asm volatile("s_waitcnt vmcnt(%0)" :: "i"(PACE - 1) : "memory");
          if (t < 8) kf[t] = __builtin_amdgcn_raw_buffer_load_b128(rk, ko + t * 1024, 0, AUX_NT);
          else vf[t - 8] = __builtin_amdgcn_raw_buffer_load_b128(rv, vo + (t - 8) * 1024, 0, AUX_NT);
        }
        SCHED();
        FL[F_VIS + w] = 1u;
        STAMP(1);
        lds_wait(FL + F_QS, 4, lane);
        STAMP(2);
        const v4u qf = *(const LAS v4u*)(L + L_QS + (r16 >> 2) * 256 + (r16 & 3) * 64 + gq * 16);
        float sc[8];
#pragma unroll
        for (int t = 0; t < 8; ++t) {
          const f32x4 d = mfma(kf[t], qf, f32x4{0.f, 0.f, 0.f, 0.f});
          sc[t] = (dpp<0x00>(d[0]) + dpp<0x55>(d[1])) + (dpp<0xAA>(d[2]) + dpp<0xFF>(d[3]));
        }
        float m = fmaxf(fmaxf(fmaxf(sc[0], sc[1]), fmaxf(sc[2], sc[3])), fmaxf(fmaxf(sc[4], sc[5]), fmaxf(sc[6], sc[7])));
        m = x32_max(x16_max(m));
        if ((lane & 0x33) == 0) ((LAS float*)(L + L_MW))[w * 4 + (lane >> 2)] = m;
        STAMP(3);
        CBAR();
        FL[F_MW + w] = 1u;
        lds_wait(FL + F_MW, ntile, lane);
        STAMP(4);
        {
          const int j = gq * 4 + (lane & 3);
          m = ((const LAS float*)(L + L_MW))[(j < ntile ? j : 0) * 4 + (r16 >> 2)];
          m = fmaxf(m, dpp<XOR1>(m)); m = fmaxf(m, dpp<XOR2>(m));
          m = x32_max(x16_max(m));
        }
        constexpr float C2 = 0.08838834764831845f * 1.4426950408889634f;
        const float ms = m * C2;
        float p[8], l = 0.f;
#pragma unroll
        for (int t = 0; t < 8; ++t) { p[t] = __builtin_amdgcn_exp2f(fmaf(sc[t], C2, -ms)); l += p[t]; }
        l = x32_sum(x16_sum(l));
        if ((lane & 0x33) == 0) ((LAS float*)(L + L_LW))[w * 4 + (lane >> 2)] = l;
        const v4u pf = {pk(p[0], p[1]), pk(p[2], p[3]), pk(p[4], p[5]), pk(p[6], p[7])};
        STAMP(5);
        v4u vt[8];
        tr8(vf, vt);
        STAMP(6);
        float o[8];
#pragma unroll
        for (int e = 0; e < 8; ++e) o[e] = mfma(pf, vt[e], f32x4{0.f, 0.f, 0.f, 0.f})[0];
        LAS f32x4* op = (LAS f32x4*)(L + L_OPART + w * 2048 + gq * 512 + r16 * 32);
        op[0] = f32x4{o[0], o[1], o[2], o[3]};
        op[1] = f32x4{o[4], o[5], o[6], o[7]};
        CBAR();
        FL[F_OPF + w] = 1u;
        STAMP(7);
        if (w < 4) {
          lds_wait(FL + F_OPF, ntile, lane);
          STAMP(8);
          f32x2 a = {0.f, 0.f};
#pragma unroll
          for (int j = 0; j < 11; ++j) if (j < 10 || ntile == 11) a += *(const LAS f32x2*)(L + L_OPART + j * 2048 + tid * 8);
          const unsigned v = nosent(pk(a.x, a.y));
          const v4u o4 = {dppu<0x00>(v), dppu<0x55>(v), dppu<0xAA>(v), dppu<0xFF>(v)};
          const unsigned rec = WS_OREC + ((g * NB + b) * NSPL + s) * OREC_SZ;
          if ((lane & 3) == 0) __builtin_amdgcn_raw_buffer_store_b128(o4, rws, rec + tid * 4, 0, AUX_SC1);
          if (w == 0) {
            float lt = (r16 < ntile) ? ((const LAS float*)(L + L_LW))[r16 * 4 + gq] : 0.f;
            lt += dpp<XOR1>(lt); lt += dpp<XOR2>(lt); lt += dpp<ROR4>(lt); lt += dpp<ROR8>(lt);
            const float mh = __builtin_bit_cast(float, __builtin_amdgcn_ds_bpermute(gq << 4, __builtin_bit_cast(int, ms)));
            if (r16 == 0) __builtin_amdgcn_raw_buffer_store_b64(v2u{nosent(__float_as_uint(mh)), nosent(__float_as_uint(lt))}, rws, rec + 1024 + gq * 8, 0, AUX_SC1);
            FL[F_DONE] = 1u;
          }
          STAMP(9);
        }
      }
      if (isC && w < 8) {
        const int bb = w >> 1, head = 2 * (w & 1) + (lane >> 5), sg = gq & 1;
        const unsigned rec = WS_OREC + ((g * NB + bb) * NSPL + sg) * OREC_SZ;
        v4u od[3]; v2u st[3];
        _Pragma("nounroll") for (unsigned sp = 0; sp < SPIN_G; ++sp) {
          CBAR();
          bool ok = true;
#pragma unroll
          for (int u = 0; u < 3; ++u) {
            od[u] = __builtin_amdgcn_raw_buffer_load_b128(rws, rec + 2 * u * OREC_SZ + head * 256 + r16 * 16, 0, AUX_SC1);
            st[u] = __builtin_amdgcn_raw_buffer_load_b64(rws, rec + 2 * u * OREC_SZ + 1024 + head * 8, 0, AUX_SC1);
          }
#pragma unroll
          for (int u = 0; u < 3; ++u) ok &= okv(od[u]) & (st[u].x != SENT) & (st[u].y != SENT);
          if (__all(ok)) break;
        }
        STAMP(10);
        const float m0 = __uint_as_float(st[0].x), m1 = __uint_as_float(st[1].x), m2 = __uint_as_float(st[2].x);
        const float mg = x16_max(fmaxf(fmaxf(m0, m1), m2));
        float wt[3] = {__builtin_amdgcn_exp2f(m0 - mg), __builtin_amdgcn_exp2f(m1 - mg), __builtin_amdgcn_exp2f(m2 - mg)};
        const float lsum = x16_sum(wt[0] * __uint_as_float(st[0].y) + wt[1] * __uint_as_float(st[1].y) + wt[2] * __uint_as_float(st[2].y));
        const float inv = __builtin_amdgcn_rcpf(lsum);
        float a[8];
#pragma unroll
        for (int u = 0; u < 3; ++u) {
          const float ww = wt[u] * inv;
#pragma unroll
          for (int d = 0; d < 4; ++d) {
            a[2 * d] = u == 0 ? ww * blo(od[u][d]) : fmaf(ww, blo(od[u][d]), a[2 * d]);
            a[2 * d + 1] = u == 0 ? ww * bhi(od[u][d]) : fmaf(ww, bhi(od[u][d]), a[2 * d + 1]);
          }
        }
#pragma unroll
        for (int d = 0; d < 8; ++d) a[d] = x16_sum(a[d]);
        if (sg == 0) *(LAS v4u*)(L + L_OA + bb * 1024 + head * 256 + r16 * 16) = v4u{pk(a[0], a[1]), pk(a[2], a[3]), pk(a[4], a[5]), pk(a[6], a[7])};
        CBAR();
        FL[F_OA + w] = 1u;
        STAMP(11);
      }
      PDUMP();
      return;
    } else {
      WGBAR();
    }
  } else {
    const int cw = w - 12;
    __builtin_amdgcn_s_setprio(3);
    {
      const v4u sv = {SENT, SENT, SENT, SENT};
      if (isAtt) {
        const unsigned rec = WS_OREC + ((g * NB + b) * NSPL + s) * OREC_SZ;
        if (cw == 0) __builtin_amdgcn_raw_buffer_store_b128(sv, rws, rec + lane * 16, 0, AUX_SC1);
        if (cw == 1 && lane < 8) __builtin_amdgcn_raw_buffer_store_b128(sv, rws, rec + 1024 + lane * 16, 0, AUX_SC1);
        if (cw == 2 && isC && lane < 32) __builtin_amdgcn_raw_buffer_store_b128(sv, rws, WS_HP + (g * NB + (lane >> 3)) * 1024 + cc * 128 + (lane & 7) * 16, 0, AUX_SC1);
      } else if (isD) {
#pragma unroll
        for (int u = 0; u < 2; ++u) __builtin_amdgcn_raw_buffer_store_b128(sv, rws, WS_OP + dI * 8192 + (cw * 2 + u) * 1024 + lane * 16, 0, AUX_SC1);
      }
    }
    const v2u ep = __builtin_amdgcn_raw_buffer_load_b64(rws, WS_EPOCH, 0, AUX_SC1);
    const int rq = i & 3, cq = i >> 2, R0 = 128 * rq + 32 * cw;
    v2u wsrc[8];
    {
      const rsrc_t rw = mk(wq, DM * NQ * 2u);
      const unsigned o = (unsigned)(R0 + 8 * gq) * (NQ * 2) + (512 * g + 64 * cq + 4 * r16) * 2;
#pragma unroll
      for (int j = 0; j < 8; ++j) wsrc[j] = __builtin_amdgcn_raw_buffer_load_b64(rw, o + j * (NQ * 2), 0, AUX_NT);
    }
    const rsrc_t rx = mk(x, NB * DM * 2u);
    v4u xs[4];
    const int xb = r16 >> 2;
#pragma unroll
    for (int m = 0; m < 4; ++m) xs[m] = __builtin_amdgcn_raw_buffer_load_b128(rx, xb * 1024 + m * 256 + ((lane & 3) + 4 * gq) * 16, 0, 0);
    const v4u xa = __builtin_amdgcn_raw_buffer_load_b128(rx, xb * 1024 + (R0 + 8 * gq) * 2, 0, 0);
    if (cw == 3) FL[lane] = 0u;
    STAMP(1);
    WGBAR();
    STAMP(2);
    float sum = 0.f;
#pragma unroll
    for (int m = 0; m < 4; ++m)
#pragma unroll
      for (int d = 0; d < 4; ++d) sum += blo(xs[m][d]) + bhi(xs[m][d]);
    sum += dpp<XOR1>(sum); sum += dpp<XOR2>(sum); sum = x32_sum(x16_sum(sum));
    STAMP(3);
    const float mean = sum * (1.f / DM);
    float var = 0.f;
#pragma unroll
    for (int m = 0; m < 4; ++m)
#pragma unroll
      for (int d = 0; d < 4; ++d) { const float t0 = blo(xs[m][d]) - mean, t1 = bhi(xs[m][d]) - mean; var = fmaf(t0, t0, var); var = fmaf(t1, t1, var); }
    var += dpp<XOR1>(var); var += dpp<XOR2>(var); var = x32_sum(x16_sum(var));
    const float rstd = 1.f / sqrtf(var * (1.f / DM) + 1e-5f);
    v4u af;
#pragma unroll
    for (int d = 0; d < 4; ++d) af[d] = pk((blo(xa[d]) - mean) * rstd, (bhi(xa[d]) - mean) * rstd);
    STAMP(4);
    {
      v4u bt[4];
      tr4(wsrc, bt);
      f32x4 qp;
#pragma unroll
      for (int e = 0; e < 4; ++e) qp[e] = mfma(af, bt[e], f32x4{0.f, 0.f, 0.f, 0.f})[0];
      *(LAS f32x4*)(L + L_QRED + cw * 1024 + lane * 16) = qp;
    }
    STAMP(5);
    DRAIN();
    const u64 tag = (((((u64)ep.y) << 32) | ep.x) + 1ull) & 0xFFFFFFFFFFull;
    const unsigned taghi = (unsigned)(tag >> 8), taglo = (unsigned)(tag << 24);
    {
      unsigned old = 0;
      if (lane == 0) old = __hip_atomic_fetch_add((LAS unsigned*)(L + L_FLAG) + F_QCNT, 1u, __ATOMIC_RELAXED, __HIP_MEMORY_SCOPE_WORKGROUP);
      old = __builtin_amdgcn_readfirstlane(old);
      CBAR();
      if (old == 3u) {
#pragma unroll
        for (int u = 0; u < 2; ++u) {
          f32x2 a = {0.f, 0.f};
#pragma unroll
          for (int j = 0; j < 4; ++j) a += *(const LAS f32x2*)(L + L_QRED + j * 1024 + gq * 256 + u * 128 + r16 * 8);
          const v4u gr = {taglo | ((__float_as_uint(a.x) + 0x80u) >> 8), taghi, taglo | ((__float_as_uint(a.y) + 0x80u) >> 8), taghi};
          __builtin_amdgcn_raw_buffer_store_b128(gr, rws, WS_QG + (((g * NB + gq) * 4 + rq) * 512 + 64 * cq + 32 * u + 2 * r16) * 8, 0, AUX_SC1);
        }
        if (lane == 0) __builtin_amdgcn_raw_buffer_store_b64(v2u{taglo, taghi}, rws, WS_HELLO + c * 8, 0, AUX_SC1);
      }
    }
    STAMP(6);
    if (isAtt) {
      const unsigned qo = WS_QG + ((g * NB + b) * 4 * 512 + 2 * (cw * 64 + lane)) * 8;
      v4u qg[4];
      while (rt() - tstart < QDELAY) __builtin_amdgcn_s_sleep(2);
      _Pragma("nounroll") for (unsigned sp = 0; sp < SPIN_G; ++sp) {
        CBAR();
        bool ok = true;
#pragma unroll
        for (int j = 0; j < 4; ++j) qg[j] = __builtin_amdgcn_raw_buffer_load_b128(rws, qo + j * 4096, 0, AUX_SC1);
#pragma unroll
        for (int j = 0; j < 4; ++j) ok &= (qg[j].y == taghi) & (qg[j].w == taghi) & ((qg[j].x >> 24) == (taglo >> 24)) & ((qg[j].z >> 24) == (taglo >> 24));
        if (__all(ok)) break;
      }
      const float q0 = (__uint_as_float(qg[0].x << 8) + __uint_as_float(qg[1].x << 8)) + (__uint_as_float(qg[2].x << 8) + __uint_as_float(qg[3].x << 8));
      const float q1 = (__uint_as_float(qg[0].z << 8) + __uint_as_float(qg[1].z << 8)) + (__uint_as_float(qg[2].z << 8) + __uint_as_float(qg[3].z << 8));
      *(LAS unsigned*)(L + L_QS + (cw * 64 + lane) * 4) = pk(q0, q1);
      CBAR();
      FL[F_QS + cw] = 1u;
      STAMP(7);
      if (isC) {
        const rsrc_t rw = mk(wo, NQ * DM * 2u);
        const unsigned o = (unsigned)(512 * g + 128 * cw + 8 * gq) * 1024 + (64 * cc + 4 * r16) * 2;
        v2u ws4[4][8];
#pragma unroll
        for (int ks = 0; ks < WO_EARLY; ++ks)
#pragma unroll
          for (int j = 0; j < 8; ++j) ws4[ks][j] = __builtin_amdgcn_raw_buffer_load_b64(rw, o + (ks * 32 + j) * 1024, 0, AUX_NT);
        SCHED();
        lds_wait(FL + F_VIS, ntile, lane);
#pragma unroll
        for (int ks = WO_EARLY; ks < 4; ++ks)
#pragma unroll
          for (int j = 0; j < 8; ++j) ws4[ks][j] = __builtin_amdgcn_raw_buffer_load_b64(rw, o + (ks * 32 + j) * 1024, 0, AUX_NT);
        SCHED();
        v4u bt[4][4];
#pragma unroll
        for (int ks = 0; ks < 4; ++ks) tr4(ws4[ks], bt[ks]);
        SCHED();
        STAMP(8);
        {
          const int j = F_OA + 2 * (lane & 3) + (cw >> 1);
          _Pragma("nounroll") for (unsigned sp = 0; sp < SPIN_L; ++sp) { if (__all(FL[j] != 0u)) break; }
          CBAR();
        }
        STAMP(9);
        f32x4 acc[4];
#pragma unroll
        for (int ks = 0; ks < 4; ++ks) {
          const v4u a = *(const LAS v4u*)(L + L_OA + (r16 >> 2) * 1024 + cw * 256 + ks * 64 + gq * 16);
#pragma unroll
          for (int e = 0; e < 4; ++e) acc[e] = mfma(a, bt[ks][e], ks == 0 ? f32x4{0.f, 0.f, 0.f, 0.f} : acc[e]);
        }
        *(LAS f32x4*)(L + L_CRED + cw * 1024 + lane * 16) = f32x4{acc[0][0], acc[1][0], acc[2][0], acc[3][0]};
        CBAR();
        STAMP(10);
        unsigned old = 0;
        if (lane == 0) old = __hip_atomic_fetch_add((LAS unsigned*)(L + L_FLAG) + F_CCNT, 1u, __ATOMIC_RELAXED, __HIP_MEMORY_SCOPE_WORKGROUP);
        old = __builtin_amdgcn_readfirstlane(old);
        CBAR();
        if (old == 3u) {
          f32x4 a = *(const LAS f32x4*)(L + L_CRED + lane * 16);
#pragma unroll
          for (int j = 1; j < 4; ++j) a += *(const LAS f32x4*)(L + L_CRED + j * 1024 + lane * 16);
          __builtin_amdgcn_raw_buffer_store_b64(v2u{nosent(pk(a[0], a[1])), nosent(pk(a[2], a[3]))}, rws, WS_HP + (g * NB + gq) * 1024 + cc * 128 + r16 * 8, 0, AUX_SC1);
        }
        STAMP(11);
      } else if (s < 2 && cw == 0) {
        const int ln = g * 8 + b * 2 + s, rb = ln >> 4, pg = lane >> 3, q4 = lane & 7;
        const int col = 128 * ((ln >> 2) & 3) + 8 * (8 * (ln & 1) + q4) + 4 * ((ln >> 1) & 1);
        const v2u xv = __builtin_amdgcn_raw_buffer_load_b64(rx, (rb * DM + col) * 2, 0, 0);
        while (rt() - tstart < HELLODELAY) __builtin_amdgcn_s_sleep(2);
        _Pragma("nounroll") for (unsigned sp = 0; sp < SPIN_G; ++sp) {
          CBAR();
          const v4u h0 = __builtin_amdgcn_raw_buffer_load_b128(rws, WS_HELLO + lane * 32, 0, AUX_SC1), h1 = __builtin_amdgcn_raw_buffer_load_b128(rws, WS_HELLO + lane * 32 + 16, 0, AUX_SC1);
          if (__all((h0.x == taglo) & (h0.y == taghi) & (h0.z == taglo) & (h0.w == taghi) & (h1.x == taglo) & (h1.y == taghi) & (h1.z == taglo) & (h1.w == taghi))) break;
          __builtin_amdgcn_s_sleep(8);
        }
        lds_wait(FL + F_DONE, 1, lane);
        STAMP(8);
        { const u64 t0 = rt(); while (rt() - t0 < RDELAY) __builtin_amdgcn_s_sleep(1); }
        v2u hv;
        _Pragma("nounroll") for (unsigned sp = 0; sp < SPIN_G; ++sp) {
          CBAR();
          hv = __builtin_amdgcn_raw_buffer_load_b64(rws, WS_HP + (pg * NB + rb) * 1024 + col * 2, 0, AUX_SC1);
          if (__all((hv.x != SENT) & (hv.y != SENT))) break;
        }
        STAMP(9);
        float h[4] = {blo(hv.x), bhi(hv.x), blo(hv.y), bhi(hv.y)};
#pragma unroll
        for (int d = 0; d < 4; ++d) { h[d] += dpp<ROR8>(h[d]); h[d] = x32_sum(x16_sum(h[d])); }
        {
          const unsigned a0 = pk(h[0], h[1]), a1 = pk(h[2], h[3]);
          const unsigned h0 = pk(blo(xv.x) + blo(a0), bhi(xv.x) + bhi(a0)), h1 = pk(blo(xv.y) + blo(a1), bhi(xv.y) + bhi(a1));
          h[0] = blo(h0); h[1] = bhi(h0); h[2] = blo(h1); h[3] = bhi(h1);
        }
        v4u pv[7];
        _Pragma("nounroll") for (unsigned sp = 0; sp < SPIN_G; ++sp) {
          CBAR();
          bool ok = true;
#pragma unroll
          for (int t = 0; t < 7; ++t) pv[t] = __builtin_amdgcn_raw_buffer_load_b128(rws, WS_OP + (pg + 8 * t) * 8192 + ln * 128 + q4 * 16, 0, AUX_SC1);
#pragma unroll
          for (int t = 0; t < 7; ++t) ok &= okv(pv[t]);
          if (__all(ok)) break;
        }
        STAMP(10);
        float y[4];
#pragma unroll
        for (int d = 0; d < 4; ++d) {
          float t = ((__uint_as_float(pv[0][d]) + __uint_as_float(pv[1][d])) + (__uint_as_float(pv[2][d]) + __uint_as_float(pv[3][d]))) +
                    ((__uint_as_float(pv[4][d]) + __uint_as_float(pv[5][d])) + __uint_as_float(pv[6][d]));
          t += dpp<ROR8>(t);
          y[d] = x32_sum(x16_sum(t));
        }
        const unsigned y0 = pk(y[0], y[1]), y1 = pk(y[2], y[3]);
        if (lane < 8) *(GAS v2u*)(out + rb * DM + col) = v2u{pk(h[0] + blo(y0), h[1] + bhi(y0)), pk(h[2] + blo(y1), h[3] + bhi(y1))};
        if (ln == 0 && lane == 0) __builtin_amdgcn_raw_buffer_store_b64(v2u{(unsigned)tag, (unsigned)(tag >> 32)}, rws, WS_EPOCH, 0, AUX_SC1);
        STAMP(11);
      }
      PDUMP();
      return;
    }
    if (isD && cw == 0) {
      while (rt() - tstart < HELLODELAY) __builtin_amdgcn_s_sleep(2);
      _Pragma("nounroll") for (unsigned sp = 0; sp < SPIN_G; ++sp) {
        CBAR();
        const v4u h0 = __builtin_amdgcn_raw_buffer_load_b128(rws, WS_HELLO + lane * 32, 0, AUX_SC1), h1 = __builtin_amdgcn_raw_buffer_load_b128(rws, WS_HELLO + lane * 32 + 16, 0, AUX_SC1);
        if (__all((h0.x == taglo) & (h0.y == taghi) & (h0.z == taglo) & (h0.w == taghi) & (h1.x == taglo) & (h1.y == taghi) & (h1.z == taglo) & (h1.w == taghi))) break;
        __builtin_amdgcn_s_sleep(8);
      }
      CBAR();
      FL[F_DONE] = 1u;
    }
  }
  if (!isD) { PDUMP(); return; }
  {
    const rsrc_t rx = mk(x, NB * DM * 2u);
    const int hb = w >> 2, hcol = 128 * (w & 3) + 2 * lane;
    const unsigned xp = __builtin_amdgcn_raw_buffer_load_b32(rx, (hb * DM + hcol) * 2, 0, 0);
    if (w < 8) { while (rt() - tstart < DDELAY) __builtin_amdgcn_s_sleep(2); }
    v4u bt[4][4];
    if (w < 4) {
      const GAS unsigned char* p = (const GAS unsigned char*)(r16 < 8 ? wg : wu) + (size_t)(128 * w + 8 * gq) * (FF * 2) + (32 * dI + 4 * (r16 & 7)) * 2;
      v2u ws4[4][8];
#pragma unroll
      for (int ks = 0; ks < 4; ++ks)
#pragma unroll
        for (int j = 0; j < 8; ++j) ws4[ks][j] = __builtin_nontemporal_load((const GAS v2u*)(p + (ks * 32 + j) * (FF * 2)));
      SCHED();
#pragma unroll
      for (int ks = 0; ks < 4; ++ks) tr4(ws4[ks], bt[ks]);
      const v4u ones = {0x3F803F80u, 0x3F803F80u, 0x3F803F80u, 0x3F803F80u};
      f32x4 acc[4];
#pragma unroll
      for (int ks = 0; ks < 4; ++ks)
#pragma unroll
        for (int e = 0; e < 4; ++e) acc[e] = mfma(ones, bt[ks][e], ks == 0 ? f32x4{0.f, 0.f, 0.f, 0.f} : acc[e]);
      if (lane < 16) *(LAS f32x4*)(L + L_SRED + w * 256 + lane * 16) = f32x4{acc[0][0], acc[1][0], acc[2][0], acc[3][0]};
    } else if (w < 8) {
      const rsrc_t rw = mk(wd, FF * DM * 2u);
      v4u src[8], o8[8];
#pragma unroll
      for (int j = 0; j < 8; ++j) src[j] = __builtin_amdgcn_raw_buffer_load_b128(rw, (unsigned)(32 * dI + 8 * gq + j) * 1024 + (w - 4) * 256 + r16 * 16, 0, AUX_NT);
      SCHED();
      tr8(src, o8);
#pragma unroll
      for (int e = 0; e < 8; ++e) bt[e >> 2][e & 3] = o8[e];
    }
    STAMP(7);
    lds_wait(FL + F_DONE, 1, lane);
    while (rt() - tstart < HPDELAY) __builtin_amdgcn_s_sleep(4);
    unsigned hp[8];
    _Pragma("nounroll") for (unsigned sp = 0; sp < SPIN_G; ++sp) {
      CBAR();
      bool ok = true;
#pragma unroll
      for (int j = 0; j < 8; ++j) hp[j] = __builtin_amdgcn_raw_buffer_load_b32(rws, WS_HP + (j * NB + hb) * 1024 + hcol * 2, 0, AUX_SC1);
#pragma unroll
      for (int j = 0; j < 8; ++j) ok &= hp[j] != SENT;
      if (__all(ok)) break;
    }
    STAMP(8);
    {
      float a0 = 0.f, a1 = 0.f, a2 = 0.f, a3 = 0.f;
#pragma unroll
      for (int j = 0; j < 4; ++j) { a0 += blo(hp[j]); a1 += bhi(hp[j]); a2 += blo(hp[4 + j]); a3 += bhi(hp[4 + j]); }
      const unsigned at = pk(a0 + a2, a1 + a3);
      *(LAS unsigned*)(L + L_OA + hb * 1024 + hcol * 2) = pk(blo(xp) + blo(at), bhi(xp) + bhi(at));
    }
    WGBAR();
    STAMP(9);
    f32x2 sg2, su2;
    if (w < 4) {
      f32x4 acc[4];
#pragma unroll
      for (int ks = 0; ks < 4; ++ks) {
        const v4u a = *(const LAS v4u*)(L + L_OA + (r16 >> 2) * 1024 + w * 256 + ks * 64 + gq * 16);
#pragma unroll
        for (int e = 0; e < 4; ++e) acc[e] = mfma(a, bt[ks][e], ks == 0 ? f32x4{0.f, 0.f, 0.f, 0.f} : acc[e]);
      }
      *(LAS f32x4*)(L + L_CRED + w * 1024 + lane * 16) = f32x4{acc[0][0], acc[1][0], acc[2][0], acc[3][0]};
    } else if (w < 8) {
      sg2 = f32x2{0.f, 0.f}; su2 = f32x2{0.f, 0.f};
#pragma unroll
      for (int j = 0; j < 4; ++j) {
        sg2 += *(const LAS f32x2*)(L + L_SRED + j * 256 + gq * 32 + (lane & 3) * 8);
        su2 += *(const LAS f32x2*)(L + L_SRED + j * 256 + 128 + gq * 32 + (lane & 3) * 8);
      }
    } else if (w < 12) {
      const v4u hv = *(const LAS v4u*)(L + L_OA + (w - 8) * 1024 + lane * 16);
      float sum = 0.f;
#pragma unroll
      for (int d = 0; d < 4; ++d) sum += blo(hv[d]) + bhi(hv[d]);
      sum += dpp<XOR1>(sum); sum += dpp<XOR2>(sum); sum += dpp<ROR4>(sum); sum += dpp<ROR8>(sum); sum = x32_sum(x16_sum(sum));
      const float mean = sum * (1.f / DM);
      float var = 0.f;
#pragma unroll
      for (int d = 0; d < 4; ++d) { const float t0 = blo(hv[d]) - mean, t1 = bhi(hv[d]) - mean; var = fmaf(t0, t0, var); var = fmaf(t1, t1, var); }
      var += dpp<XOR1>(var); var += dpp<XOR2>(var); var += dpp<ROR4>(var); var += dpp<ROR8>(var); var = x32_sum(x16_sum(var));
      if (lane == 0) *(LAS f32x2*)(L + L_STAT + (w - 8) * 8) = f32x2{mean, 1.f / sqrtf(var * (1.f / DM) + 1e-5f)};
    }
    WGBAR();
    STAMP(10);
    if (w >= 4 && w < 8) {
      const int bb = r16 >> 2;
      f32x2 gg = {0.f, 0.f}, uu = {0.f, 0.f};
#pragma unroll
      for (int j = 0; j < 4; ++j) {
        gg += *(const LAS f32x2*)(L + L_CRED + j * 1024 + bb * 256 + gq * 32 + (lane & 3) * 8);
        uu += *(const LAS f32x2*)(L + L_CRED + j * 1024 + bb * 256 + 128 + gq * 32 + (lane & 3) * 8);
      }
      const f32x2 st = *(const LAS f32x2*)(L + L_STAT + bb * 8);
      const float g0 = st.y * (gg.x - st.x * sg2.x), g1 = st.y * (gg.y - st.x * sg2.y);
      const float u0 = st.y * (uu.x - st.x * su2.x), u1 = st.y * (uu.y - st.x * su2.y);
      const float h0 = u0 * __builtin_amdgcn_rcpf(1.f + __builtin_amdgcn_exp2f(g0 * -1.4426950408889634f));
      const float h1 = u1 * __builtin_amdgcn_rcpf(1.f + __builtin_amdgcn_exp2f(g1 * -1.4426950408889634f));
      const unsigned hh = pk(h0, h1);
      const v4u a = {dppu<0x00>(hh), dppu<0x55>(hh), dppu<0xAA>(hh), dppu<0xFF>(hh)};
      unsigned y[8];
#pragma unroll
      for (int e = 0; e < 8; ++e) y[e] = nosent(__float_as_uint(mfma(a, bt[e >> 2][e & 3], f32x4{0.f, 0.f, 0.f, 0.f})[0]));
      const unsigned o = WS_OP + dI * 8192 + gq * 2048 + (w - 4) * 512 + r16 * 16;
      __builtin_amdgcn_raw_buffer_store_b128(v4u{y[0], y[1], y[2], y[3]}, rws, o, 0, AUX_SC1);
      __builtin_amdgcn_raw_buffer_store_b128(v4u{y[4], y[5], y[6], y[7]}, rws, o + 256, 0, AUX_SC1);
    }
    STAMP(11);
    PDUMP();
  }
}
}

extern "C" void kernel_launch(void* const* d_in, const int* in_sizes, int n_in, void* d_out, int out_size, void* d_ws, size_t ws_size, hipStream_t stream) {
  (void)in_sizes; (void)n_in; (void)out_size; (void)ws_size;
  fused_decode<<<dim3(256), dim3(1024), 0, stream>>>((const u16*)d_in[0], (const u16*)d_in[1], (const u16*)d_in[2], (const u16*)d_in[3], (const u16*)d_in[4],
                                                     (const u16*)d_in[6], (const u16*)d_in[7], (const u16*)d_in[8], (u16*)d_out, (unsigned char*)d_ws);
}
```

```cpp
#include <hip/hip_runtime.h>
#include <stdint.h>

#define PROF 0
#define DDELAY 200
#define PACE 16
#define HPDELAY 800
#define QDELAY 300
#define KLIN 0
#define HELLODELAY 600
#define ENDWAIT 0
#define ENDMUL 0
#define WO_EARLY 0
#define RDELAY 100

namespace {
typedef unsigned short u16;
typedef unsigned long long u64;
typedef unsigned v4u __attribute__((ext_vector_type(4)));
typedef unsigned v2u __attribute__((ext_vector_type(2)));
typedef float f32x4 __attribute__((ext_vector_type(4)));
typedef float f32x2 __attribute__((ext_vector_type(2)));
typedef short bf16x8 __attribute__((ext_vector_type(8)));
typedef __bf16 bf16x2 __attribute__((ext_vector_type(2)));
#define GAS __attribute__((address_space(1)))
#define LAS __attribute__((address_space(3)))
typedef __amdgpu_buffer_rsrc_t rsrc_t;

constexpr int NB = 4, DM = 512, HKV = 8, SK = 8192, WIN = 2048, DH = 128, FF = 1792, NQ = 4096;
constexpr int NSPL = 6, NDCU = 56;
constexpr int AUX_NT = 2, AUX_SC1 = 16;
constexpr unsigned SENT = 0xFFFFFFFFu;
constexpr int WS_EPOCH = 0, WS_HELLO = 4096, WS_QG = 8192, WS_OREC = WS_QG + 8 * 4 * 4 * 512 * 8, OREC_SZ = 1152;
constexpr int WS_HP = WS_OREC + 8 * 4 * NSPL * OREC_SZ, WS_OP = WS_HP + 8 * 4 * 512 * 2, WS_END = WS_OP + NDCU * 8192;
constexpr int WS_PROF = 2 << 20;
constexpr int L_FLAG = 0, L_QRED = 256, L_QS = L_QRED + 4096, L_MW = L_QS + 1024, L_LW = L_MW + 256, L_OPART = L_LW + 256;
constexpr int L_OA = L_OPART + 12 * 2048, L_CRED = L_OA + 4096, L_SRED = L_CRED + 4096, L_STAT = L_SRED + 1024, L_USED = L_STAT + 64;
constexpr int L_TOTAL = 84 * 1024;
static_assert(L_USED <= L_TOTAL, "lds");
enum { F_QCNT = 0, F_CCNT = 1, F_DONE = 2, F_QS = 4, F_OA = 8, F_MW = 16, F_OPF = 32, F_VIS = 48 };
constexpr unsigned SPIN_G = 1u << 17, SPIN_L = 1u << 21;

__device__ __forceinline__ u64 rt() { return __builtin_amdgcn_s_memrealtime(); }
__device__ __forceinline__ rsrc_t mk(const void* p, unsigned bytes) { return __builtin_amdgcn_make_buffer_rsrc((void*)p, 0, bytes, 0x00020000); }
__device__ __forceinline__ f32x4 mfma(v4u a, v4u b, f32x4 c) { return __builtin_amdgcn_mfma_f32_16x16x32_bf16(__builtin_bit_cast(bf16x8, a), __builtin_bit_cast(bf16x8, b), c, 0, 0, 0); }
__device__ __forceinline__ unsigned pk(float lo, float hi) { return __builtin_bit_cast(unsigned, __builtin_convertvector(f32x2{lo, hi}, bf16x2)); }
__device__ __forceinline__ float blo(unsigned v) { return __builtin_bit_cast(float, v << 16); }
__device__ __forceinline__ float bhi(unsigned v) { return __builtin_bit_cast(float, v & 0xffff0000u); }
__device__ __forceinline__ unsigned plo(unsigned a, unsigned b) { return __builtin_amdgcn_perm(b, a, 0x05040100u); }
__device__ __forceinline__ unsigned phi(unsigned a, unsigned b) { return __builtin_amdgcn_perm(b, a, 0x07060302u); }
__device__ __forceinline__ unsigned nosent(unsigned v) { return v == SENT ? 0xFFFEFFFFu : v; }
template <int C> __device__ __forceinline__ float dpp(float x) { return __builtin_bit_cast(float, __builtin_amdgcn_mov_dpp(__builtin_bit_cast(int, x), C, 0xf, 0xf, true)); }
template <int C> __device__ __forceinline__ unsigned dppu(unsigned x) { return (unsigned)__builtin_amdgcn_mov_dpp((int)x, C, 0xf, 0xf, true); }
constexpr int XOR1 = 0xB1, XOR2 = 0x4E, ROR4 = 0x124, ROR8 = 0x128;
__device__ __forceinline__ float x16_sum(float x) { auto s = __builtin_amdgcn_permlane16_swap(__float_as_uint(x), __float_as_uint(x), false, false); return __uint_as_float(s[0]) + __uint_as_float(s[1]); }
__device__ __forceinline__ float x32_sum(float x) { auto s = __builtin_amdgcn_permlane32_swap(__float_as_uint(x), __float_as_uint(x), false, false); return __uint_as_float(s[0]) + __uint_as_float(s[1]); }
__device__ __forceinline__ float x16_max(float x) { auto s = __builtin_amdgcn_permlane16_swap(__float_as_uint(x), __float_as_uint(x), false, false); return fmaxf(__uint_as_float(s[0]), __uint_as_float(s[1])); }
__device__ __forceinline__ float x32_max(float x) { auto s = __builtin_amdgcn_permlane32_swap(__float_as_uint(x), __float_as_uint(x), false, false); return fmaxf(__uint_as_float(s[0]), __uint_as_float(s[1])); }
__device__ __forceinline__ float dot2(unsigned a, unsigned b, float c) { return __builtin_amdgcn_fdot2_f32_bf16(__builtin_bit_cast(bf16x2, a), __builtin_bit_cast(bf16x2, b), c, false); }
#define CBAR() asm volatile("" ::: "memory")
#define SCHED() __builtin_amdgcn_sched_barrier(0)
#define DRAIN() asm volatile("s_waitcnt vmcnt(0)" ::: "memory")
#define WGBAR() do { __builtin_amdgcn_sched_barrier(0); asm volatile("s_waitcnt lgkmcnt(0)" ::: "memory"); __builtin_amdgcn_s_barrier(); asm volatile("" ::: "memory"); __builtin_amdgcn_sched_barrier(0); } while (0)
__device__ __forceinline__ bool okv(v4u r) { return (r.x != SENT) & (r.y != SENT) & (r.z != SENT) & (r.w != SENT); }
__device__ __forceinline__ void lds_wait(volatile LAS unsigned* f, int n, int lane) {
  const int j = lane < n ? lane : 0;
  _Pragma("nounroll") for (unsigned s = 0; s < SPIN_L; ++s) { if (__all(f[j] != 0u)) break; }
  CBAR();
}
#define STAMP(k) do {} while (0)
#define PDUMP() do {} while (0)

__device__ __forceinline__ void tr4(const v2u (&s)[8], v4u (&o)[4]) {
#pragma unroll
  for (int m = 0; m < 4; ++m) {
    o[0][m] = plo(s[2 * m].x, s[2 * m + 1].x); o[1][m] = phi(s[2 * m].x, s[2 * m + 1].x);
    o[2][m] = plo(s[2 * m].y, s[2 * m + 1].y); o[3][m] = phi(s[2 * m].y, s[2 * m + 1].y);
  }
}
__device__ __forceinline__ void tr8(const v4u (&s)[8], v4u (&o)[8]) {
#pragma unroll
  for (int m = 0; m < 4; ++m)
#pragma unroll
    for (int d = 0; d < 4; ++d) { o[2 * d][m] = plo(s[2 * m][d], s[2 * m + 1][d]); o[2 * d + 1][m] = phi(s[2 * m][d], s[2 * m + 1][d]); }
}

__global__ void __launch_bounds__(1024) fused_decode(const u16* __restrict__ x, const u16* __restrict__ kc, const u16* __restrict__ vc,
                                                     const u16* __restrict__ wq, const u16* __restrict__ wo, const u16* __restrict__ wg,
                                                     const u16* __restrict__ wu, const u16* __restrict__ wd, u16* __restrict__ out, unsigned char* ws) {
  const u64 tstart = rt();
  SCHED();
  __shared__ __attribute__((aligned(16))) unsigned char smem[L_TOTAL];
  LAS unsigned char* L = (LAS unsigned char*)smem;
  volatile LAS unsigned* FL = (volatile LAS unsigned*)(L + L_FLAG);
  const int lane = threadIdx.x & 63, w = (__builtin_amdgcn_readfirstlane(threadIdx.x >> 6) + 12) & 15, tid = w * 64 + lane;
  asm volatile("" :: "s"(x), "s"(kc), "s"(vc), "s"(wq), "s"(wo), "s"(wg), "s"(wu), "s"(wd), "s"(out), "s"(ws));
  const int c = blockIdx.x, g = c & 7, i = c >> 3;
  const int gq = lane >> 4, r16 = lane & 15;
  const bool isAtt = i < 24;
  const int b = i / NSPL, s = i - b * NSPL;
  const bool isC = isAtt && s >= 4;
  const int cc = 2 * b + (s - 4);
  const int dI = (i - 24) * 8 + g;
  const bool isD = !isAtt && dI < NDCU;
  const int ntile = s < 4 ? 11 : 10, tile0 = s < 4 ? 11 * s : 44 + 10 * (s - 4);
  const rsrc_t rws = mk(ws, WS_END);

  if (w < 12) {
    if (isAtt) {
      const bool act = w < ntile;
      v4u kf[8], vf[8];
      const unsigned kvbase = ((unsigned)(b * HKV + g) * SK + (SK - WIN) + 32u * (tile0 + w)) * 256u;
      const rsrc_t rk = mk(kc, NB * HKV * SK * 256u), rv = mk(vc, NB * HKV * SK * 256u);
      const unsigned ko = KLIN ? kvbase + lane * 16 : kvbase + (r16 >> 2) * 256 + (r16 & 3) * 64 + gq * 16;
      const unsigned vo = kvbase + lane * 16;
      if (act) {
#pragma unroll
        for (int t = 0; t < (PACE < 2 ? PACE : 2); ++t) kf[t] = __builtin_amdgcn_raw_buffer_load_b128(rk, ko + t * 1024, 0, AUX_NT);
      }
      WGBAR();
      if (act) {
#pragma unroll
        for (int t = (PACE < 2 ? PACE : 2); t < 16; ++t) {
          SCHED();
          if (t >= PACE) asm volatile("s_waitcnt vmcnt(%0)" :: "i"(PACE - 1) : "memory");
          if (t < 8) kf[t] = __builtin_amdgcn_raw_buffer_load_b128(rk, ko + t * 1024, 0, AUX_NT);
          else vf[t - 8] = __builtin_amdgcn_raw_buffer_load_b128(rv, vo + (t - 8) * 1024, 0, AUX_NT);
        }
        SCHED();
        FL[F_VIS + w] = 1u;
        STAMP(1);
        lds_wait(FL + F_QS, 4, lane);
        STAMP(2);
        const v4u qf = *(const LAS v4u*)(L + L_QS + (r16 >> 2) * 256 + (r16 & 3) * 64 + gq * 16);
        float sc[8];
#pragma unroll
        for (int t = 0; t < 8; ++t) {
          const f32x4 d = mfma(kf[t], qf, f32x4{0.f, 0.f, 0.f, 0.f});
          sc[t] = (dpp<0x00>(d[0]) + dpp<0x55>(d[1])) + (dpp<0xAA>(d[2]) + dpp<0xFF>(d[3]));
        }
        float m = fmaxf(fmaxf(fmaxf(sc[0], sc[1]), fmaxf(sc[2], sc[3])), fmaxf(fmaxf(sc[4], sc[5]), fmaxf(sc[6], sc[7])));
        m = x32_max(x16_max(m));
        if ((lane & 0x33) == 0) ((LAS float*)(L + L_MW))[w * 4 + (lane >> 2)] = m;
        STAMP(3);
        CBAR();
        FL[F_MW + w] = 1u;
        lds_wait(FL + F_MW, ntile, lane);
        STAMP(4);
        {
          const int j = gq * 4 + (lane & 3);
          m = ((const LAS float*)(L + L_MW))[(j < ntile ? j : 0) * 4 + (r16 >> 2)];
          m = fmaxf(m, dpp<XOR1>(m)); m = fmaxf(m, dpp<XOR2>(m));
          m = x32_max(x16_max(m));
        }
        constexpr float C2 = 0.08838834764831845f * 1.4426950408889634f;
        const float ms = m * C2;
        float p[8], l = 0.f;
#pragma unroll
        for (int t = 0; t < 8; ++t) { p[t] = __builtin_amdgcn_exp2f(fmaf(sc[t], C2, -ms)); l += p[t]; }
        l = x32_sum(x16_sum(l));
        if ((lane & 0x33) == 0) ((LAS float*)(L + L_LW))[w * 4 + (lane >> 2)] = l;
        const v4u pf = {pk(p[0], p[1]), pk(p[2], p[3]), pk(p[4], p[5]), pk(p[6], p[7])};
        STAMP(5);
        v4u vt[8];
        tr8(vf, vt);
        STAMP(6);
        float o[8];
#pragma unroll
        for (int e = 0; e < 8; ++e) o[e] = mfma(pf, vt[e], f32x4{0.f, 0.f, 0.f, 0.f})[0];
        LAS f32x4* op = (LAS f32x4*)(L + L_OPART + w * 2048 + gq * 512 + r16 * 32);
        op[0] = f32x4{o[0], o[1], o[2], o[3]};
        op[1] = f32x4{o[4], o[5], o[6], o[7]};
        CBAR();
        FL[F_OPF + w] = 1u;
        STAMP(7);
        if (w < 4) {
          lds_wait(FL + F_OPF, ntile, lane);
          STAMP(8);
          f32x2 a = {0.f, 0.f};
#pragma unroll
          for (int j = 0; j < 11; ++j) if (j < 10 || ntile == 11) a += *(const LAS f32x2*)(L + L_OPART + j * 2048 + tid * 8);
          const unsigned v = nosent(pk(a.x, a.y));
          const v4u o4 = {dppu<0x00>(v), dppu<0x55>(v), dppu<0xAA>(v), dppu<0xFF>(v)};
          const unsigned rec = WS_OREC + ((g * NB + b) * NSPL + s) * OREC_SZ;
          if ((lane & 3) == 0) __builtin_amdgcn_raw_buffer_store_b128(o4, rws, rec + tid * 4, 0, AUX_SC1);
          if (w == 0) {
            float lt = (r16 < ntile) ? ((const LAS float*)(L + L_LW))[r16 * 4 + gq] : 0.f;
            lt += dpp<XOR1>(lt); lt += dpp<XOR2>(lt); lt += dpp<ROR4>(lt); lt += dpp<ROR8>(lt);
            const float mh = __builtin_bit_cast(float, __builtin_amdgcn_ds_bpermute(gq << 4, __builtin_bit_cast(int, ms)));
            if (r16 == 0) __builtin_amdgcn_raw_buffer_store_b64(v2u{nosent(__float_as_uint(mh)), nosent(__float_as_uint(lt))}, rws, rec + 1024 + gq * 8, 0, AUX_SC1);
            FL[F_DONE] = 1u;
          }
          STAMP(9);
        }
      }
      if (isC && w < 8) {
        const int bb = w >> 1, head = 2 * (w & 1) + (lane >> 5), sg = gq & 1;
        const unsigned rec = WS_OREC + ((g * NB + bb) * NSPL + sg) * OREC_SZ;
        v4u od[3]; v2u st[3];
        _Pragma("nounroll") for (unsigned sp = 0; sp < SPIN_G; ++sp) {
          CBAR();
          bool ok = true;
#pragma unroll
          for (int u = 0; u < 3; ++u) {
            od[u] = __builtin_amdgcn_raw_buffer_load_b128(rws, rec + 2 * u * OREC_SZ + head * 256 + r16 * 16, 0, AUX_SC1);
            st[u] = __builtin_amdgcn_raw_buffer_load_b64(rws, rec + 2 * u * OREC_SZ + 1024 + head * 8, 0, AUX_SC1);
          }
#pragma unroll
          for (int u = 0; u < 3; ++u) ok &= okv(od[u]) & (st[u].x != SENT) & (st[u].y != SENT);
          if (__all(ok)) break;
        }
        STAMP(10);
        const float m0 = __uint_as_float(st[0].x), m1 = __uint_as_float(st[1].x), m2 = __uint_as_float(st[2].x);
        const float mg = x16_max(fmaxf(fmaxf(m0, m1), m2));
        float wt[3] = {__builtin_amdgcn_exp2f(m0 - mg), __builtin_amdgcn_exp2f(m1 - mg), __builtin_amdgcn_exp2f(m2 - mg)};
        const float lsum = x16_sum(wt[0] * __uint_as_float(st[0].y) + wt[1] * __uint_as_float(st[1].y) + wt[2] * __uint_as_float(st[2].y));
        const float inv = __builtin_amdgcn_rcpf(lsum);
        float a[8];
#pragma unroll
        for (int u = 0; u < 3; ++u) {
          const float ww = wt[u] * inv;
#pragma unroll
          for (int d = 0; d < 4; ++d) {
            a[2 * d] = u == 0 ? ww * blo(od[u][d]) : fmaf(ww, blo(od[u][d]), a[2 * d]);
            a[2 * d + 1] = u == 0 ? ww * bhi(od[u][d]) : fmaf(ww, bhi(od[u][d]), a[2 * d + 1]);
          }
        }
#pragma unroll
        for (int d = 0; d < 8; ++d) a[d] = x16_sum(a[d]);
        if (sg == 0) *(LAS v4u*)(L + L_OA + bb * 1024 + head * 256 + r16 * 16) = v4u{pk(a[0], a[1]), pk(a[2], a[3]), pk(a[4], a[5]), pk(a[6], a[7])};
        CBAR();
        FL[F_OA + w] = 1u;
        STAMP(11);
      }
      PDUMP();
      return;
    } else {
      WGBAR();
    }
  } else {
    const int cw = w - 12;
    __builtin_amdgcn_s_setprio(3);
    {
      const v4u sv = {SENT, SENT, SENT, SENT};
      if (isAtt) {
        const unsigned rec = WS_OREC + ((g * NB + b) * NSPL + s) * OREC_SZ;
        if (cw == 0) __builtin_amdgcn_raw_buffer_store_b128(sv, rws, rec + lane * 16, 0, AUX_SC1);
        if (cw == 1 && lane < 8) __builtin_amdgcn_raw_buffer_store_b128(sv, rws, rec + 1024 + lane * 16, 0, AUX_SC1);
        if (cw == 2 && isC && lane < 32) __builtin_amdgcn_raw_buffer_store_b128(sv, rws, WS_HP + (g * NB + (lane >> 3)) * 1024 + cc * 128 + (lane & 7) * 16, 0, AUX_SC1);
      } else if (isD) {
#pragma unroll
        for (int u = 0; u < 2; ++u) __builtin_amdgcn_raw_buffer_store_b128(sv, rws, WS_OP + dI * 8192 + (cw * 2 + u) * 1024 + lane * 16, 0, AUX_SC1);
      }
    }
    const v2u ep = __builtin_amdgcn_raw_buffer_load_b64(rws, WS_EPOCH, 0, AUX_SC1);
    const int rq = i & 3, cq = i >> 2, R0 = 128 * rq + 32 * cw;
    v2u wsrc[8];
    {
      const rsrc_t rw = mk(wq, DM * NQ * 2u);
      const unsigned o = (unsigned)(R0 + 8 * gq) * (NQ * 2) + (512 * g + 64 * cq + 4 * r16) * 2;
#pragma unroll
      for (int j = 0; j < 8; ++j) wsrc[j] = __builtin_amdgcn_raw_buffer_load_b64(rw, o + j * (NQ * 2), 0, AUX_NT);
    }
    const rsrc_t rx = mk(x, NB * DM * 2u);
    v4u xs[4];
    const int xb = r16 >> 2;
#pragma unroll
    for (int m = 0; m < 4; ++m) xs[m] = __builtin_amdgcn_raw_buffer_load_b128(rx, xb * 1024 + m * 256 + ((lane & 3) + 4 * gq) * 16, 0, 0);
    const v4u xa = __builtin_amdgcn_raw_buffer_load_b128(rx, xb * 1024 + (R0 + 8 * gq) * 2, 0, 0);
    if (cw == 3) FL[lane] = 0u;
    STAMP(1);
    WGBAR();
    STAMP(2);
    float sum = 0.f;
#pragma unroll
    for (int m = 0; m < 4; ++m)
#pragma unroll
      for (int d = 0; d < 4; ++d) sum += blo(xs[m][d]) + bhi(xs[m][d]);
    sum += dpp<XOR1>(sum); sum += dpp<XOR2>(sum); sum = x32_sum(x16_sum(sum));
    STAMP(3);
    const float mean = sum * (1.f / DM);
    float var = 0.f;
#pragma unroll
    for (int m = 0; m < 4; ++m)
#pragma unroll
      for (int d = 0; d < 4; ++d) { const float t0 = blo(xs[m][d]) - mean, t1 = bhi(xs[m][d]) - mean; var = fmaf(t0, t0, var); var = fmaf(t1, t1, var); }
    var += dpp<XOR1>(var); var += dpp<XOR2>(var); var = x32_sum(x16_sum(var));
    const float rstd = 1.f / sqrtf(var * (1.f / DM) + 1e-5f);
    v4u af;
#pragma unroll
    for (int d = 0; d < 4; ++d) af[d] = pk((blo(xa[d]) - mean) * rstd, (bhi(xa[d]) - mean) * rstd);
    STAMP(4);
    {
      v4u bt[4];
      tr4(wsrc, bt);
      f32x4 qp;
#pragma unroll
      for (int e = 0; e < 4; ++e) qp[e] = mfma(af, bt[e], f32x4{0.f, 0.f, 0.f, 0.f})[0];
      *(LAS f32x4*)(L + L_QRED + cw * 1024 + lane * 16) = qp;
    }
    STAMP(5);
    DRAIN();
    const u64 tag = (((((u64)ep.y) << 32) | ep.x) + 1ull) & 0xFFFFFFFFFFull;
    const unsigned taghi = (unsigned)(tag >> 8), taglo = (unsigned)(tag << 24);
    {
      unsigned old = 0;
      if (lane == 0) old = __hip_atomic_fetch_add((LAS unsigned*)(L + L_FLAG) + F_QCNT, 1u, __ATOMIC_RELAXED, __HIP_MEMORY_SCOPE_WORKGROUP);
      old = __builtin_amdgcn_readfirstlane(old);
      CBAR();
      if (old == 3u) {
#pragma unroll
        for (int u = 0; u < 2; ++u) {
          f32x2 a = {0.f, 0.f};
#pragma unroll
          for (int j = 0; j < 4; ++j) a += *(const LAS f32x2*)(L + L_QRED + j * 1024 + gq * 256 + u * 128 + r16 * 8);
          const v4u gr = {taglo | ((__float_as_uint(a.x) + 0x80u) >> 8), taghi, taglo | ((__float_as_uint(a.y) + 0x80u) >> 8), taghi};
          __builtin_amdgcn_raw_buffer_store_b128(gr, rws, WS_QG + (((g * NB + gq) * 4 + rq) * 512 + 64 * cq + 32 * u + 2 * r16) * 8, 0, AUX_SC1);
        }
        if (lane == 0) __builtin_amdgcn_raw_buffer_store_b64(v2u{taglo, taghi}, rws, WS_HELLO + c * 8, 0, AUX_SC1);
      }
    }
    STAMP(6);
    if (isAtt) {
      const unsigned qo = WS_QG + ((g * NB + b) * 4 * 512 + 2 * (cw * 64 + lane)) * 8;
      v4u qg[4];
      while (rt() - tstart < QDELAY) __builtin_amdgcn_s_sleep(2);
      _Pragma("nounroll") for (unsigned sp = 0; sp < SPIN_G; ++sp) {
        CBAR();
        bool ok = true;
#pragma unroll
        for (int j = 0; j < 4; ++j) qg[j] = __builtin_amdgcn_raw_buffer_load_b128(rws, qo + j * 4096, 0, AUX_SC1);
#pragma unroll
        for (int j = 0; j < 4; ++j) ok &= (qg[j].y == taghi) & (qg[j].w == taghi) & ((qg[j].x >> 24) == (taglo >> 24)) & ((qg[j].z >> 24) == (taglo >> 24));
        if (__all(ok)) break;
      }
      const float q0 = (__uint_as_float(qg[0].x << 8) + __uint_as_float(qg[1].x << 8)) + (__uint_as_float(qg[2].x << 8) + __uint_as_float(qg[3].x << 8));
      const float q1 = (__uint_as_float(qg[0].z << 8) + __uint_as_float(qg[1].z << 8)) + (__uint_as_float(qg[2].z << 8) + __uint_as_float(qg[3].z << 8));
      *(LAS unsigned*)(L + L_QS + (cw * 64 + lane) * 4) = pk(q0, q1);
      CBAR();
      FL[F_QS + cw] = 1u;
      STAMP(7);
      if (isC) {
        const rsrc_t rw = mk(wo, NQ * DM * 2u);
        const unsigned o = (unsigned)(512 * g + 128 * cw + 8 * gq) * 1024 + (64 * cc + 4 * r16) * 2;
        v2u ws4[4][8];
#pragma unroll
        for (int ks = 0; ks < WO_EARLY; ++ks)
#pragma unroll
          for (int j = 0; j < 8; ++j) ws4[ks][j] = __builtin_amdgcn_raw_buffer_load_b64(rw, o + (ks * 32 + j) * 1024, 0, AUX_NT);
        SCHED();
        lds_wait(FL + F_VIS, ntile, lane);
#pragma unroll
        for (int ks = WO_EARLY; ks < 4; ++ks)
#pragma unroll
          for (int j = 0; j < 8; ++j) ws4[ks][j] = __builtin_amdgcn_raw_buffer_load_b64(rw, o + (ks * 32 + j) * 1024, 0, AUX_NT);
        SCHED();
        v4u bt[4][4];
#pragma unroll
        for (int ks = 0; ks < 4; ++ks) tr4(ws4[ks], bt[ks]);
        SCHED();
        STAMP(8);
        {
          const int j = F_OA + 2 * (lane & 3) + (cw >> 1);
          _Pragma("nounroll") for (unsigned sp = 0; sp < SPIN_L; ++sp) { if (__all(FL[j] != 0u)) break; }
          CBAR();
        }
        STAMP(9);
        f32x4 acc[4];
#pragma unroll
        for (int ks = 0; ks < 4; ++ks) {
          const v4u a = *(const LAS v4u*)(L + L_OA + (r16 >> 2) * 1024 + cw * 256 + ks * 64 + gq * 16);
#pragma unroll
          for (int e = 0; e < 4; ++e) acc[e] = mfma(a, bt[ks][e], ks == 0 ? f32x4{0.f, 0.f, 0.f, 0.f} : acc[e]);
        }
        *(LAS f32x4*)(L + L_CRED + cw * 1024 + lane * 16) = f32x4{acc[0][0], acc[1][0], acc[2][0], acc[3][0]};
        CBAR();
        STAMP(10);
        unsigned old = 0;
        if (lane == 0) old = __hip_atomic_fetch_add((LAS unsigned*)(L + L_FLAG) + F_CCNT, 1u, __ATOMIC_RELAXED, __HIP_MEMORY_SCOPE_WORKGROUP);
        old = __builtin_amdgcn_readfirstlane(old);
        CBAR();
        if (old == 3u) {
          f32x4 a = *(const LAS f32x4*)(L + L_CRED + lane * 16);
#pragma unroll
          for (int j = 1; j < 4; ++j) a += *(const LAS f32x4*)(L + L_CRED + j * 1024 + lane * 16);
          __builtin_amdgcn_raw_buffer_store_b64(v2u{nosent(pk(a[0], a[1])), nosent(pk(a[2], a[3]))}, rws, WS_HP + (g * NB + gq) * 1024 + cc * 128 + r16 * 8, 0, AUX_SC1);
        }
        STAMP(11);
      } else if (s < 2 && cw == 0) {
        const int ln = g * 8 + b * 2 + s, rb = ln >> 4, pg = lane >> 3, q4 = lane & 7;
        const int col = 128 * ((ln >> 2) & 3) + 8 * (8 * (ln & 1) + q4) + 4 * ((ln >> 1) & 1);
        const v2u xv = __builtin_amdgcn_raw_buffer_load_b64(rx, (rb * DM + col) * 2, 0, 0);
        while (rt() - tstart < HELLODELAY) __builtin_amdgcn_s_sleep(2);
        _Pragma("nounroll") for (unsigned sp = 0; sp < SPIN_G; ++sp) {
          CBAR();
          const v4u h0 = __builtin_amdgcn_raw_buffer_load_b128(rws, WS_HELLO + lane * 32, 0, AUX_SC1), h1 = __builtin_amdgcn_raw_buffer_load_b128(rws, WS_HELLO + lane * 32 + 16, 0, AUX_SC1);
          if (__all((h0.x == taglo) & (h0.y == taghi) & (h0.z == taglo) & (h0.w == taghi) & (h1.x == taglo) & (h1.y == taghi) & (h1.z == taglo) & (h1.w == taghi))) break;
          __builtin_amdgcn_s_sleep(8);
        }
        lds_wait(FL + F_DONE, 1, lane);
        STAMP(8);
        { const u64 t0 = rt(); while (rt() - t0 < RDELAY) __builtin_amdgcn_s_sleep(1); }
        v2u hv;
        _Pragma("nounroll") for (unsigned sp = 0; sp < SPIN_G; ++sp) {
          CBAR();
          hv = __builtin_amdgcn_raw_buffer_load_b64(rws, WS_HP + (pg * NB + rb) * 1024 + col * 2, 0, AUX_SC1);
          if (__all((hv.x != SENT) & (hv.y != SENT))) break;
        }
        STAMP(9);
        float h[4] = {blo(hv.x), bhi(hv.x), blo(hv.y), bhi(hv.y)};
#pragma unroll
        for (int d = 0; d < 4; ++d) { h[d] += dpp<ROR8>(h[d]); h[d] = x32_sum(x16_sum(h[d])); }
        {
          const unsigned a0 = pk(h[0], h[1]), a1 = pk(h[2], h[3]);
          const unsigned h0 = pk(blo(xv.x) + blo(a0), bhi(xv.x) + bhi(a0)), h1 = pk(blo(xv.y) + blo(a1), bhi(xv.y) + bhi(a1));
          h[0] = blo(h0); h[1] = bhi(h0); h[2] = blo(h1); h[3] = bhi(h1);
        }
        v4u pv[7];
        _Pragma("nounroll") for (unsigned sp = 0; sp < SPIN_G; ++sp) {
          CBAR();
          bool ok = true;
#pragma unroll
          for (int t = 0; t < 7; ++t) pv[t] = __builtin_amdgcn_raw_buffer_load_b128(rws, WS_OP + (pg + 8 * t) * 8192 + ln * 128 + q4 * 16, 0, AUX_SC1);
#pragma unroll
          for (int t = 0; t < 7; ++t) ok &= okv(pv[t]);
          if (__all(ok)) break;
        }
        STAMP(10);
        float y[4];
#pragma unroll
        for (int d = 0; d < 4; ++d) {
          float t = ((__uint_as_float(pv[0][d]) + __uint_as_float(pv[1][d])) + (__uint_as_float(pv[2][d]) + __uint_as_float(pv[3][d]))) +
                    ((__uint_as_float(pv[4][d]) + __uint_as_float(pv[5][d])) + __uint_as_float(pv[6][d]));
          t += dpp<ROR8>(t);
          y[d] = x32_sum(x16_sum(t));
        }
        const unsigned y0 = pk(y[0], y[1]), y1 = pk(y[2], y[3]);
        if (lane < 8) *(GAS v2u*)(out + rb * DM + col) = v2u{pk(h[0] + blo(y0), h[1] + bhi(y0)), pk(h[2] + blo(y1), h[3] + bhi(y1))};
        if (ln == 0 && lane == 0) __builtin_amdgcn_raw_buffer_store_b64(v2u{(unsigned)tag, (unsigned)(tag >> 32)}, rws, WS_EPOCH, 0, AUX_SC1);
        STAMP(11);
        if (ENDWAIT) { while (rt() - tstart < ENDWAIT) __builtin_amdgcn_s_sleep(1); }
        if (ENDMUL) {
          const u64 c0 = __builtin_amdgcn_s_memtime(); while (__builtin_amdgcn_s_memtime() - c0 < ENDMUL) __builtin_amdgcn_s_sleep(1); }
      }
      PDUMP();
      return;
    }
    if (isD && cw == 0) {
      while (rt() - tstart < HELLODELAY) __builtin_amdgcn_s_sleep(2);
      _Pragma("nounroll") for (unsigned sp = 0; sp < SPIN_G; ++sp) {
        CBAR();
        const v4u h0 = __builtin_amdgcn_raw_buffer_load_b128(rws, WS_HELLO + lane * 32, 0, AUX_SC1), h1 = __builtin_amdgcn_raw_buffer_load_b128(rws, WS_HELLO + lane * 32 + 16, 0, AUX_SC1);
        if (__all((h0.x == taglo) & (h0.y == taghi) & (h0.z == taglo) & (h0.w == taghi) & (h1.x == taglo) & (h1.y == taghi) & (h1.z == taglo) & (h1.w == taghi))) break;
        __builtin_amdgcn_s_sleep(8);
      }
      CBAR();
      FL[F_DONE] = 1u;
    }
  }
  if (!isD) { PDUMP(); return; }
  {
    const rsrc_t rx = mk(x, NB * DM * 2u);
    const int hb = w >> 2, hcol = 128 * (w & 3) + 2 * lane;
    const unsigned xp = __builtin_amdgcn_raw_buffer_load_b32(rx, (hb * DM + hcol) * 2, 0, 0);
    if (w < 8) { while (rt() - tstart < DDELAY) __builtin_amdgcn_s_sleep(2); }
    v4u bt[4][4];
    if (w < 4) {
      const GAS unsigned char* p = (const GAS unsigned char*)(r16 < 8 ? wg : wu) + (size_t)(128 * w + 8 * gq) * (FF * 2) + (32 * dI + 4 * (r16 & 7)) * 2;
      v2u ws4[4][8];
#pragma unroll
      for (int ks = 0; ks < 4; ++ks)
#pragma unroll
        for (int j = 0; j < 8; ++j) ws4[ks][j] = __builtin_nontemporal_load((const GAS v2u*)(p + (ks * 32 + j) * (FF * 2)));
      SCHED();
#pragma unroll
      for (int ks = 0; ks < 4; ++ks) tr4(ws4[ks], bt[ks]);
      const v4u ones = {0x3F803F80u, 0x3F803F80u, 0x3F803F80u, 0x3F803F80u};
      f32x4 acc[4];
#pragma unroll
      for (int ks = 0; ks < 4; ++ks)
#pragma unroll
        for (int e = 0; e < 4; ++e) acc[e] = mfma(ones, bt[ks][e], ks == 0 ? f32x4{0.f, 0.f, 0.f, 0.f} : acc[e]);
      if (lane < 16) *(LAS f32x4*)(L + L_SRED + w * 256 + lane * 16) = f32x4{acc[0][0], acc[1][0], acc[2][0], acc[3][0]};
    } else if (w < 8) {
      const rsrc_t rw = mk(wd, FF * DM * 2u);
      v4u src[8], o8[8];
#pragma unroll
      for (int j = 0; j < 8; ++j) src[j] = __builtin_amdgcn_raw_buffer_load_b128(rw, (unsigned)(32 * dI + 8 * gq + j) * 1024 + (w - 4) * 256 + r16 * 16, 0, AUX_NT);
      SCHED();
      tr8(src, o8);
#pragma unroll
      for (int e = 0; e < 8; ++e) bt[e >> 2][e & 3] = o8[e];
    }
    STAMP(7);
    lds_wait(FL + F_DONE, 1, lane);
    while (rt() - tstart < HPDELAY) __builtin_amdgcn_s_sleep(4);
    unsigned hp[8];
    _Pragma("nounroll") for (unsigned sp = 0; sp < SPIN_G; ++sp) {
      CBAR();
      bool ok = true;
#pragma unroll
      for (int j = 0; j < 8; ++j) hp[j] = __builtin_amdgcn_raw_buffer_load_b32(rws, WS_HP + (j * NB + hb) * 1024 + hcol * 2, 0, AUX_SC1);
#pragma unroll
      for (int j = 0; j < 8; ++j) ok &= hp[j] != SENT;
      if (__all(ok)) break;
    }
    STAMP(8);
    {
      float a0 = 0.f, a1 = 0.f, a2 = 0.f, a3 = 0.f;
#pragma unroll
      for (int j = 0; j < 4; ++j) { a0 += blo(hp[j]); a1 += bhi(hp[j]); a2 += blo(hp[4 + j]); a3 += bhi(hp[4 + j]); }
      const unsigned at = pk(a0 + a2, a1 + a3);
      *(LAS unsigned*)(L + L_OA + hb * 1024 + hcol * 2) = pk(blo(xp) + blo(at), bhi(xp) + bhi(at));
    }
    WGBAR();
    STAMP(9);
    f32x2 sg2, su2;
    if (w < 4) {
      f32x4 acc[4];
#pragma unroll
      for (int ks = 0; ks < 4; ++ks) {
        const v4u a = *(const LAS v4u*)(L + L_OA + (r16 >> 2) * 1024 + w * 256 + ks * 64 + gq * 16);
#pragma unroll
        for (int e = 0; e < 4; ++e) acc[e] = mfma(a, bt[ks][e], ks == 0 ? f32x4{0.f, 0.f, 0.f, 0.f} : acc[e]);
      }
      *(LAS f32x4*)(L + L_CRED + w * 1024 + lane * 16) = f32x4{acc[0][0], acc[1][0], acc[2][0], acc[3][0]};
    } else if (w < 8) {
      sg2 = f32x2{0.f, 0.f}; su2 = f32x2{0.f, 0.f};
#pragma unroll
      for (int j = 0; j < 4; ++j) {
        sg2 += *(const LAS f32x2*)(L + L_SRED + j * 256 + gq * 32 + (lane & 3) * 8);
        su2 += *(const LAS f32x2*)(L + L_SRED + j * 256 + 128 + gq * 32 + (lane & 3) * 8);
      }
    } else if (w < 12) {
      const v4u hv = *(const LAS v4u*)(L + L_OA + (w - 8) * 1024 + lane * 16);
      float sum = 0.f;
#pragma unroll
      for (int d = 0; d < 4; ++d) sum += blo(hv[d]) + bhi(hv[d]);
      sum += dpp<XOR1>(sum); sum += dpp<XOR2>(sum); sum += dpp<ROR4>(sum); sum += dpp<ROR8>(sum); sum = x32_sum(x16_sum(sum));
      const float mean = sum * (1.f / DM);
      float var = 0.f;
#pragma unroll
      for (int d = 0; d < 4; ++d) { const float t0 = blo(hv[d]) - mean, t1 = bhi(hv[d]) - mean; var = fmaf(t0, t0, var); var = fmaf(t1, t1, var); }
      var += dpp<XOR1>(var); var += dpp<XOR2>(var); var += dpp<ROR4>(var); var += dpp<ROR8>(var); var = x32_sum(x16_sum(var));
      if (lane == 0) *(LAS f32x2*)(L + L_STAT + (w - 8) * 8) = f32x2{mean, 1.f / sqrtf(var * (1.f / DM) + 1e-5f)};
    }
    WGBAR();
    STAMP(10);
    if (w >= 4 && w < 8) {
      const int bb = r16 >> 2;
      f32x2 gg = {0.f, 0.f}, uu = {0.f, 0.f};
#pragma unroll
      for (int j = 0; j < 4; ++j) {
        gg += *(const LAS f32x2*)(L + L_CRED + j * 1024 + bb * 256 + gq * 32 + (lane & 3) * 8);
        uu += *(const LAS f32x2*)(L + L_CRED + j * 1024 + bb * 256 + 128 + gq * 32 + (lane & 3) * 8);
      }
      const f32x2 st = *(const LAS f32x2*)(L + L_STAT + bb * 8);
      const float g0 = st.y * (gg.x - st.x * sg2.x), g1 = st.y * (gg.y - st.x * sg2.y);
      const float u0 = st.y * (uu.x - st.x * su2.x), u1 = st.y * (uu.y - st.x * su2.y);
      const float h0 = u0 * __builtin_amdgcn_rcpf(1.f + __builtin_amdgcn_exp2f(g0 * -1.4426950408889634f));
      const float h1 = u1 * __builtin_amdgcn_rcpf(1.f + __builtin_amdgcn_exp2f(g1 * -1.4426950408889634f));
      const unsigned hh = pk(h0, h1);
      const v4u a = {dppu<0x00>(hh), dppu<0x55>(hh), dppu<0xAA>(hh), dppu<0xFF>(hh)};
      unsigned y[8];
#pragma unroll
      for (int e = 0; e < 8; ++e) y[e] = nosent(__float_as_uint(mfma(a, bt[e >> 2][e & 3], f32x4{0.f, 0.f, 0.f, 0.f})[0]));
      const unsigned o = WS_OP + dI * 8192 + gq * 2048 + (w - 4) * 512 + r16 * 16;
      __builtin_amdgcn_raw_buffer_store_b128(v4u{y[0], y[1], y[2], y[3]}, rws, o, 0, AUX_SC1);
      __builtin_amdgcn_raw_buffer_store_b128(v4u{y[4], y[5], y[6], y[7]}, rws, o + 256, 0, AUX_SC1);
    }
    STAMP(11);
    PDUMP();
  }
}
}

extern "C" void kernel_launch(void* const* d_in, const int* in_sizes, int n_in, void* d_out, int out_size, void* d_ws, size_t ws_size, hipStream_t stream) {
  (void)in_sizes; (void)n_in; (void)out_size; (void)ws_size;
  fused_decode<<<dim3(256), dim3(1024), 0, stream>>>((const u16*)d_in[0], (const u16*)d_in[1], (const u16*)d_in[2], (const u16*)d_in[3], (const u16*)d_in[4],
                                                     (const u16*)d_in[6], (const u16*)d_in[7], (const u16*)d_in[8], (u16*)d_out, (unsigned char*)d_ws);
}
```

```cpp
#include <hip/hip_runtime.h>
#include <stdint.h>

#define PROF 0
#define DDELAY 200
#define PACE 16
#define HPDELAY 800
#define QDELAY 300
#define KLIN 0
#define HELLODELAY 600
#define ENDWAIT 10000
#define ENDMUL 0
#define ENDREF 1
#define WO_EARLY 0
#define RDELAY 100

namespace {
typedef unsigned short u16;
typedef unsigned long long u64;
typedef unsigned v4u __attribute__((ext_vector_type(4)));
typedef unsigned v2u __attribute__((ext_vector_type(2)));
typedef float f32x4 __attribute__((ext_vector_type(4)));
typedef float f32x2 __attribute__((ext_vector_type(2)));
typedef short bf16x8 __attribute__((ext_vector_type(8)));
typedef __bf16 bf16x2 __attribute__((ext_vector_type(2)));
#define GAS __attribute__((address_space(1)))
#define LAS __attribute__((address_space(3)))
typedef __amdgpu_buffer_rsrc_t rsrc_t;

constexpr int NB = 4, DM = 512, HKV = 8, SK = 8192, WIN = 2048, DH = 128, FF = 1792, NQ = 4096;
constexpr int NSPL = 6, NDCU = 56;
constexpr int AUX_NT = 2, AUX_SC1 = 16;
constexpr unsigned SENT = 0xFFFFFFFFu;
constexpr int WS_EPOCH = 0, WS_HELLO = 4096, WS_QG = 8192, WS_OREC = WS_QG + 8 * 4 * 4 * 512 * 8, OREC_SZ = 1152;
constexpr int WS_HP = WS_OREC + 8 * 4 * NSPL * OREC_SZ, WS_OP = WS_HP + 8 * 4 * 512 * 2, WS_END = WS_OP + NDCU * 8192;
constexpr int WS_PROF = 2 << 20;
constexpr int L_FLAG = 0, L_QRED = 256, L_QS = L_QRED + 4096, L_MW = L_QS + 1024, L_LW = L_MW + 256, L_OPART = L_LW + 256;
constexpr int L_OA = L_OPART + 12 * 2048, L_CRED = L_OA + 4096, L_SRED = L_CRED + 4096, L_STAT = L_SRED + 1024, L_USED = L_STAT + 64;
constexpr int L_TOTAL = 84 * 1024;
static_assert(L_USED <= L_TOTAL, "lds");
enum { F_QCNT = 0, F_CCNT = 1, F_DONE = 2, F_QS = 4, F_OA = 8, F_MW = 16, F_OPF = 32, F_VIS = 48 };
constexpr unsigned SPIN_G = 1u << 17, SPIN_L = 1u << 21;

__device__ __forceinline__ u64 rt() { return __builtin_amdgcn_s_memrealtime(); }
__device__ __forceinline__ rsrc_t mk(const void* p, unsigned bytes) { return __builtin_amdgcn_make_buffer_rsrc((void*)p, 0, bytes, 0x00020000); }
__device__ __forceinline__ f32x4 mfma(v4u a, v4u b, f32x4 c) { return __builtin_amdgcn_mfma_f32_16x16x32_bf16(__builtin_bit_cast(bf16x8, a), __builtin_bit_cast(bf16x8, b), c, 0, 0, 0); }
__device__ __forceinline__ unsigned pk(float lo, float hi) { return __builtin_bit_cast(unsigned, __builtin_convertvector(f32x2{lo, hi}, bf16x2)); }
__device__ __forceinline__ float blo(unsigned v) { return __builtin_bit_cast(float, v << 16); }
__device__ __forceinline__ float bhi(unsigned v) { return __builtin_bit_cast(float, v & 0xffff0000u); }
__device__ __forceinline__ unsigned plo(unsigned a, unsigned b) { return __builtin_amdgcn_perm(b, a, 0x05040100u); }
__device__ __forceinline__ unsigned phi(unsigned a, unsigned b) { return __builtin_amdgcn_perm(b, a, 0x07060302u); }
__device__ __forceinline__ unsigned nosent(unsigned v) { return v == SENT ? 0xFFFEFFFFu : v; }
template <int C> __device__ __forceinline__ float dpp(float x) { return __builtin_bit_cast(float, __builtin_amdgcn_mov_dpp(__builtin_bit_cast(int, x), C, 0xf, 0xf, true)); }
template <int C> __device__ __forceinline__ unsigned dppu(unsigned x) { return (unsigned)__builtin_amdgcn_mov_dpp((int)x, C, 0xf, 0xf, true); }
constexpr int XOR1 = 0xB1, XOR2 = 0x4E, ROR4 = 0x124, ROR8 = 0x128;
__device__ __forceinline__ float x16_sum(float x) { auto s = __builtin_amdgcn_permlane16_swap(__float_as_uint(x), __float_as_uint(x), false, false); return __uint_as_float(s[0]) + __uint_as_float(s[1]); }
__device__ __forceinline__ float x32_sum(float x) { auto s = __builtin_amdgcn_permlane32_swap(__float_as_uint(x), __float_as_uint(x), false, false); return __uint_as_float(s[0]) + __uint_as_float(s[1]); }
__device__ __forceinline__ float x16_max(float x) { auto s = __builtin_amdgcn_permlane16_swap(__float_as_uint(x), __float_as_uint(x), false, false); return fmaxf(__uint_as_float(s[0]), __uint_as_float(s[1])); }
__device__ __forceinline__ float x32_max(float x) { auto s = __builtin_amdgcn_permlane32_swap(__float_as_uint(x), __float_as_uint(x), false, false); return fmaxf(__uint_as_float(s[0]), __uint_as_float(s[1])); }
__device__ __forceinline__ float dot2(unsigned a, unsigned b, float c) { return __builtin_amdgcn_fdot2_f32_bf16(__builtin_bit_cast(bf16x2, a), __builtin_bit_cast(bf16x2, b), c, false); }
#define CBAR() asm volatile("" ::: "memory")
#define SCHED() __builtin_amdgcn_sched_barrier(0)
#define DRAIN() asm volatile("s_waitcnt vmcnt(0)" ::: "memory")
#define WGBAR() do { __builtin_amdgcn_sched_barrier(0); asm volatile("s_waitcnt lgkmcnt(0)" ::: "memory"); __builtin_amdgcn_s_barrier(); asm volatile("" ::: "memory"); __builtin_amdgcn_sched_barrier(0); } while (0)
__device__ __forceinline__ bool okv(v4u r) { return (r.x != SENT) & (r.y != SENT) & (r.z != SENT) & (r.w != SENT); }
__device__ __forceinline__ void lds_wait(volatile LAS unsigned* f, int n, int lane) {
  const int j = lane < n ? lane : 0;
  _Pragma("nounroll") for (unsigned s = 0; s < SPIN_L; ++s) { if (__all(f[j] != 0u)) break; }
  CBAR();
}
#define STAMP(k) do {} while (0)
#define PDUMP() do {} while (0)

__device__ __forceinline__ void tr4(const v2u (&s)[8], v4u (&o)[4]) {
#pragma unroll
  for (int m = 0; m < 4; ++m) {
    o[0][m] = plo(s[2 * m].x, s[2 * m + 1].x); o[1][m] = phi(s[2 * m].x, s[2 * m + 1].x);
    o[2][m] = plo(s[2 * m].y, s[2 * m + 1].y); o[3][m] = phi(s[2 * m].y, s[2 * m + 1].y);
  }
}
__device__ __forceinline__ void tr8(const v4u (&s)[8], v4u (&o)[8]) {
#pragma unroll
  for (int m = 0; m < 4; ++m)
#pragma unroll
    for (int d = 0; d < 4; ++d) { o[2 * d][m] = plo(s[2 * m][d], s[2 * m + 1][d]); o[2 * d + 1][m] = phi(s[2 * m][d], s[2 * m + 1][d]); }
}

__global__ void __launch_bounds__(1024) fused_decode(const u16* __restrict__ x, const u16* __restrict__ kc, const u16* __restrict__ vc,
                                                     const u16* __restrict__ wq, const u16* __restrict__ wo, const u16* __restrict__ wg,
                                                     const u16* __restrict__ wu, const u16* __restrict__ wd, u16* __restrict__ out, unsigned char* ws) {
  const u64 tstart = rt();
  SCHED();
  __shared__ __attribute__((aligned(16))) unsigned char smem[L_TOTAL];
  LAS unsigned char* L = (LAS unsigned char*)smem;
  volatile LAS unsigned* FL = (volatile LAS unsigned*)(L + L_FLAG);
  const int lane = threadIdx.x & 63, w = (__builtin_amdgcn_readfirstlane(threadIdx.x >> 6) + 12) & 15, tid = w * 64 + lane;
  asm volatile("" :: "s"(x), "s"(kc), "s"(vc), "s"(wq), "s"(wo), "s"(wg), "s"(wu), "s"(wd), "s"(out), "s"(ws));
  const int c = blockIdx.x, g = c & 7, i = c >> 3;
  const int gq = lane >> 4, r16 = lane & 15;
  const bool isAtt = i < 24;
  const int b = i / NSPL, s = i - b * NSPL;
  const bool isC = isAtt && s >= 4;
  const int cc = 2 * b + (s - 4);
  const int dI = (i - 24) * 8 + g;
  const bool isD = !isAtt && dI < NDCU;
  const int ntile = s < 4 ? 11 : 10, tile0 = s < 4 ? 11 * s : 44 + 10 * (s - 4);
  const rsrc_t rws = mk(ws, WS_END);

  if (w < 12) {
    if (isAtt) {
      const bool act = w < ntile;
      v4u kf[8], vf[8];
      const unsigned kvbase = ((unsigned)(b * HKV + g) * SK + (SK - WIN) + 32u * (tile0 + w)) * 256u;
      const rsrc_t rk = mk(kc, NB * HKV * SK * 256u), rv = mk(vc, NB * HKV * SK * 256u);
      const unsigned ko = KLIN ? kvbase + lane * 16 : kvbase + (r16 >> 2) * 256 + (r16 & 3) * 64 + gq * 16;
      const unsigned vo = kvbase + lane * 16;
      if (act) {
#pragma unroll
        for (int t = 0; t < (PACE < 2 ? PACE : 2); ++t) kf[t] = __builtin_amdgcn_raw_buffer_load_b128(rk, ko + t * 1024, 0, AUX_NT);
      }
      WGBAR();
      if (act) {
#pragma unroll
        for (int t = (PACE < 2 ? PACE : 2); t < 16; ++t) {
          SCHED();
          if (t >= PACE) asm volatile("s_waitcnt vmcnt(%0)" :: "i"(PACE - 1) : "memory");
          if (t < 8) kf[t] = __builtin_amdgcn_raw_buffer_load_b128(rk, ko + t * 1024, 0, AUX_NT);
          else vf[t - 8] = __builtin_amdgcn_raw_buffer_load_b128(rv, vo + (t - 8) * 1024, 0, AUX_NT);
        }
        SCHED();
        FL[F_VIS + w] = 1u;
        STAMP(1);
        lds_wait(FL + F_QS, 4, lane);
        STAMP(2);
        const v4u qf = *(const LAS v4u*)(L + L_QS + (r16 >> 2) * 256 + (r16 & 3) * 64 + gq * 16);
        float sc[8];
#pragma unroll
        for (int t = 0; t < 8; ++t) {
          const f32x4 d = mfma(kf[t], qf, f32x4{0.f, 0.f, 0.f, 0.f});
          sc[t] = (dpp<0x00>(d[0]) + dpp<0x55>(d[1])) + (dpp<0xAA>(d[2]) + dpp<0xFF>(d[3]));
        }
        float m = fmaxf(fmaxf(fmaxf(sc[0], sc[1]), fmaxf(sc[2], sc[3])), fmaxf(fmaxf(sc[4], sc[5]), fmaxf(sc[6], sc[7])));
        m = x32_max(x16_max(m));
        if ((lane & 0x33) == 0) ((LAS float*)(L + L_MW))[w * 4 + (lane >> 2)] = m;
        STAMP(3);
        CBAR();
        FL[F_MW + w] = 1u;
        lds_wait(FL + F_MW, ntile, lane);
        STAMP(4);
        {
          const int j = gq * 4 + (lane & 3);
          m = ((const LAS float*)(L + L_MW))[(j < ntile ? j : 0) * 4 + (r16 >> 2)];
          m = fmaxf(m, dpp<XOR1>(m)); m = fmaxf(m, dpp<XOR2>(m));
          m = x32_max(x16_max(m));
        }
        constexpr float C2 = 0.08838834764831845f * 1.4426950408889634f;
        const float ms = m * C2;
        float p[8], l = 0.f;
#pragma unroll
        for (int t = 0; t < 8; ++t) { p[t] = __builtin_amdgcn_exp2f(fmaf(sc[t], C2, -ms)); l += p[t]; }
        l = x32_sum(x16_sum(l));
        if ((lane & 0x33) == 0) ((LAS float*)(L + L_LW))[w * 4 + (lane >> 2)] = l;
        const v4u pf = {pk(p[0], p[1]), pk(p[2], p[3]), pk(p[4], p[5]), pk(p[6], p[7])};
        STAMP(5);
        v4u vt[8];
        tr8(vf, vt);
        STAMP(6);
        float o[8];
#pragma unroll
        for (int e = 0; e < 8; ++e) o[e] = mfma(pf, vt[e], f32x4{0.f, 0.f, 0.f, 0.f})[0];
        LAS f32x4* op = (LAS f32x4*)(L + L_OPART + w * 2048 + gq * 512 + r16 * 32);
        op[0] = f32x4{o[0], o[1], o[2], o[3]};
        op[1] = f32x4{o[4], o[5], o[6], o[7]};
        CBAR();
        FL[F_OPF + w] = 1u;
        STAMP(7);
        if (w < 4) {
          lds_wait(FL + F_OPF, ntile, lane);
          STAMP(8);
          f32x2 a = {0.f, 0.f};
#pragma unroll
          for (int j = 0; j < 11; ++j) if (j < 10 || ntile == 11) a += *(const LAS f32x2*)(L + L_OPART + j * 2048 + tid * 8);
          const unsigned v = nosent(pk(a.x, a.y));
          const v4u o4 = {dppu<0x00>(v), dppu<0x55>(v), dppu<0xAA>(v), dppu<0xFF>(v)};
          const unsigned rec = WS_OREC + ((g * NB + b) * NSPL + s) * OREC_SZ;
          if ((lane & 3) == 0) __builtin_amdgcn_raw_buffer_store_b128(o4, rws, rec + tid * 4, 0, AUX_SC1);
          if (w == 0) {
            float lt = (r16 < ntile) ? ((const LAS float*)(L + L_LW))[r16 * 4 + gq] : 0.f;
            lt += dpp<XOR1>(lt); lt += dpp<XOR2>(lt); lt += dpp<ROR4>(lt); lt += dpp<ROR8>(lt);
            const float mh = __builtin_bit_cast(float, __builtin_amdgcn_ds_bpermute(gq << 4, __builtin_bit_cast(int, ms)));
            if (r16 == 0) __builtin_amdgcn_raw_buffer_store_b64(v2u{nosent(__float_as_uint(mh)), nosent(__float_as_uint(lt))}, rws, rec + 1024 + gq * 8, 0, AUX_SC1);
            FL[F_DONE] = 1u;
          }
          STAMP(9);
        }
      }
      if (isC && w < 8) {
        const int bb = w >> 1, head = 2 * (w & 1) + (lane >> 5), sg = gq & 1;
        const unsigned rec = WS_OREC + ((g * NB + bb) * NSPL + sg) * OREC_SZ;
        v4u od[3]; v2u st[3];
        _Pragma("nounroll") for (unsigned sp = 0; sp < SPIN_G; ++sp) {
          CBAR();
          bool ok = true;
#pragma unroll
          for (int u = 0; u < 3; ++u) {
            od[u] = __builtin_amdgcn_raw_buffer_load_b128(rws, rec + 2 * u * OREC_SZ + head * 256 + r16 * 16, 0, AUX_SC1);
            st[u] = __builtin_amdgcn_raw_buffer_load_b64(rws, rec + 2 * u * OREC_SZ + 1024 + head * 8, 0, AUX_SC1);
          }
#pragma unroll
          for (int u = 0; u < 3; ++u) ok &= okv(od[u]) & (st[u].x != SENT) & (st[u].y != SENT);
          if (__all(ok)) break;
        }
        STAMP(10);
        const float m0 = __uint_as_float(st[0].x), m1 = __uint_as_float(st[1].x), m2 = __uint_as_float(st[2].x);
        const float mg = x16_max(fmaxf(fmaxf(m0, m1), m2));
        float wt[3] = {__builtin_amdgcn_exp2f(m0 - mg), __builtin_amdgcn_exp2f(m1 - mg), __builtin_amdgcn_exp2f(m2 - mg)};
        const float lsum = x16_sum(wt[0] * __uint_as_float(st[0].y) + wt[1] * __uint_as_float(st[1].y) + wt[2] * __uint_as_float(st[2].y));
        const float inv = __builtin_amdgcn_rcpf(lsum);
        float a[8];
#pragma unroll
        for (int u = 0; u < 3; ++u) {
          const float ww = wt[u] * inv;
#pragma unroll
          for (int d = 0; d < 4; ++d) {
            a[2 * d] = u == 0 ? ww * blo(od[u][d]) : fmaf(ww, blo(od[u][d]), a[2 * d]);
            a[2 * d + 1] = u == 0 ? ww * bhi(od[u][d]) : fmaf(ww, bhi(od[u][d]), a[2 * d + 1]);
          }
        }
#pragma unroll
        for (int d = 0; d < 8; ++d) a[d] = x16_sum(a[d]);
        if (sg == 0) *(LAS v4u*)(L + L_OA + bb * 1024 + head * 256 + r16 * 16) = v4u{pk(a[0], a[1]), pk(a[2], a[3]), pk(a[4], a[5]), pk(a[6], a[7])};
        CBAR();
        FL[F_OA + w] = 1u;
        STAMP(11);
      }
      PDUMP();
      return;
    } else {
      WGBAR();
    }
  } else {
    const int cw = w - 12;
    __builtin_amdgcn_s_setprio(3);
    {
      const v4u sv = {SENT, SENT, SENT, SENT};
      if (isAtt) {
        const unsigned rec = WS_OREC + ((g * NB + b) * NSPL + s) * OREC_SZ;
        if (cw == 0) __builtin_amdgcn_raw_buffer_store_b128(sv, rws, rec + lane * 16, 0, AUX_SC1);
        if (cw == 1 && lane < 8) __builtin_amdgcn_raw_buffer_store_b128(sv, rws, rec + 1024 + lane * 16, 0, AUX_SC1);
        if (cw == 2 && isC && lane < 32) __builtin_amdgcn_raw_buffer_store_b128(sv, rws, WS_HP + (g * NB + (lane >> 3)) * 1024 + cc * 128 + (lane & 7) * 16, 0, AUX_SC1);
      } else if (isD) {
#pragma unroll
        for (int u = 0; u < 2; ++u) __builtin_amdgcn_raw_buffer_store_b128(sv, rws, WS_OP + dI * 8192 + (cw * 2 + u) * 1024 + lane * 16, 0, AUX_SC1);
      }
    }
    if (ENDREF && cw == 0 && lane == 0) __hip_atomic_store((GAS u64*)(ws + (3 << 20) + c * 8), tstart, __ATOMIC_RELAXED, __HIP_MEMORY_SCOPE_AGENT);
    const v2u ep = __builtin_amdgcn_raw_buffer_load_b64(rws, WS_EPOCH, 0, AUX_SC1);
    const int rq = i & 3, cq = i >> 2, R0 = 128 * rq + 32 * cw;
    v2u wsrc[8];
    {
      const rsrc_t rw = mk(wq, DM * NQ * 2u);
      const unsigned o = (unsigned)(R0 + 8 * gq) * (NQ * 2) + (512 * g + 64 * cq + 4 * r16) * 2;
#pragma unroll
      for (int j = 0; j < 8; ++j) wsrc[j] = __builtin_amdgcn_raw_buffer_load_b64(rw, o + j * (NQ * 2), 0, AUX_NT);
    }
    const rsrc_t rx = mk(x, NB * DM * 2u);
    v4u xs[4];
    const int xb = r16 >> 2;
#pragma unroll
    for (int m = 0; m < 4; ++m) xs[m] = __builtin_amdgcn_raw_buffer_load_b128(rx, xb * 1024 + m * 256 + ((lane & 3) + 4 * gq) * 16, 0, 0);
    const v4u xa = __builtin_amdgcn_raw_buffer_load_b128(rx, xb * 1024 + (R0 + 8 * gq) * 2, 0, 0);
    if (cw == 3) FL[lane] = 0u;
    STAMP(1);
    WGBAR();
    STAMP(2);
    float sum = 0.f;
#pragma unroll
    for (int m = 0; m < 4; ++m)
#pragma unroll
      for (int d = 0; d < 4; ++d) sum += blo(xs[m][d]) + bhi(xs[m][d]);
    sum += dpp<XOR1>(sum); sum += dpp<XOR2>(sum); sum = x32_sum(x16_sum(sum));
    STAMP(3);
    const float mean = sum * (1.f / DM);
    float var = 0.f;
#pragma unroll
    for (int m = 0; m < 4; ++m)
#pragma unroll
      for (int d = 0; d < 4; ++d) { const float t0 = blo(xs[m][d]) - mean, t1 = bhi(xs[m][d]) - mean; var = fmaf(t0, t0, var); var = fmaf(t1, t1, var); }
    var += dpp<XOR1>(var); var += dpp<XOR2>(var); var = x32_sum(x16_sum(var));
    const float rstd = 1.f / sqrtf(var * (1.f / DM) + 1e-5f);
    v4u af;
#pragma unroll
    for (int d = 0; d < 4; ++d) af[d] = pk((blo(xa[d]) - mean) * rstd, (bhi(xa[d]) - mean) * rstd);
    STAMP(4);
    {
      v4u bt[4];
      tr4(wsrc, bt);
      f32x4 qp;
#pragma unroll
      for (int e = 0; e < 4; ++e) qp[e] = mfma(af, bt[e], f32x4{0.f, 0.f, 0.f, 0.f})[0];
      *(LAS f32x4*)(L + L_QRED + cw * 1024 + lane * 16) = qp;
    }
    STAMP(5);
    DRAIN();
    const u64 tag = (((((u64)ep.y) << 32) | ep.x) + 1ull) & 0xFFFFFFFFFFull;
    const unsigned taghi = (unsigned)(tag >> 8), taglo = (unsigned)(tag << 24);
    {
      unsigned old = 0;
      if (lane == 0) old = __hip_atomic_fetch_add((LAS unsigned*)(L + L_FLAG) + F_QCNT, 1u, __ATOMIC_RELAXED, __HIP_MEMORY_SCOPE_WORKGROUP);
      old = __builtin_amdgcn_readfirstlane(old);
      CBAR();
      if (old == 3u) {
#pragma unroll
        for (int u = 0; u < 2; ++u) {
          f32x2 a = {0.f, 0.f};
#pragma unroll
          for (int j = 0; j < 4; ++j) a += *(const LAS f32x2*)(L + L_QRED + j * 1024 + gq * 256 + u * 128 + r16 * 8);
          const v4u gr = {taglo | ((__float_as_uint(a.x) + 0x80u) >> 8), taghi, taglo | ((__float_as_uint(a.y) + 0x80u) >> 8), taghi};
          __builtin_amdgcn_raw_buffer_store_b128(gr, rws, WS_QG + (((g * NB + gq) * 4 + rq) * 512 + 64 * cq + 32 * u + 2 * r16) * 8, 0, AUX_SC1);
        }
        if (lane == 0) __builtin_amdgcn_raw_buffer_store_b64(v2u{taglo, taghi}, rws, WS_HELLO + c * 8, 0, AUX_SC1);
      }
    }
    STAMP(6);
    if (isAtt) {
      const unsigned qo = WS_QG + ((g * NB + b) * 4 * 512 + 2 * (cw * 64 + lane)) * 8;
      v4u qg[4];
      while (rt() - tstart < QDELAY) __builtin_amdgcn_s_sleep(2);
      _Pragma("nounroll") for (unsigned sp = 0; sp < SPIN_G; ++sp) {
        CBAR();
        bool ok = true;
#pragma unroll
        for (int j = 0; j < 4; ++j) qg[j] = __builtin_amdgcn_raw_buffer_load_b128(rws, qo + j * 4096, 0, AUX_SC1);
#pragma unroll
        for (int j = 0; j < 4; ++j) ok &= (qg[j].y == taghi) & (qg[j].w == taghi) & ((qg[j].x >> 24) == (taglo >> 24)) & ((qg[j].z >> 24) == (taglo >> 24));
        if (__all(ok)) break;
      }
      const float q0 = (__uint_as_float(qg[0].x << 8) + __uint_as_float(qg[1].x << 8)) + (__uint_as_float(qg[2].x << 8) + __uint_as_float(qg[3].x << 8));
      const float q1 = (__uint_as_float(qg[0].z << 8) + __uint_as_float(qg[1].z << 8)) + (__uint_as_float(qg[2].z << 8) + __uint_as_float(qg[3].z << 8));
      *(LAS unsigned*)(L + L_QS + (cw * 64 + lane) * 4) = pk(q0, q1);
      CBAR();
      FL[F_QS + cw] = 1u;
      STAMP(7);
      if (isC) {
        const rsrc_t rw = mk(wo, NQ * DM * 2u);
        const unsigned o = (unsigned)(512 * g + 128 * cw + 8 * gq) * 1024 + (64 * cc + 4 * r16) * 2;
        v2u ws4[4][8];
#pragma unroll
        for (int ks = 0; ks < WO_EARLY; ++ks)
#pragma unroll
          for (int j = 0; j < 8; ++j) ws4[ks][j] = __builtin_amdgcn_raw_buffer_load_b64(rw, o + (ks * 32 + j) * 1024, 0, AUX_NT);
        SCHED();
        lds_wait(FL + F_VIS, ntile, lane);
#pragma unroll
        for (int ks = WO_EARLY; ks < 4; ++ks)
#pragma unroll
          for (int j = 0; j < 8; ++j) ws4[ks][j] = __builtin_amdgcn_raw_buffer_load_b64(rw, o + (ks * 32 + j) * 1024, 0, AUX_NT);
        SCHED();
        v4u bt[4][4];
#pragma unroll
        for (int ks = 0; ks < 4; ++ks) tr4(ws4[ks], bt[ks]);
        SCHED();
        STAMP(8);
        {
          const int j = F_OA + 2 * (lane & 3) + (cw >> 1);
          _Pragma("nounroll") for (unsigned sp = 0; sp < SPIN_L; ++sp) { if (__all(FL[j] != 0u)) break; }
          CBAR();
        }
        STAMP(9);
        f32x4 acc[4];
#pragma unroll
        for (int ks = 0; ks < 4; ++ks) {
          const v4u a = *(const LAS v4u*)(L + L_OA + (r16 >> 2) * 1024 + cw * 256 + ks * 64 + gq * 16);
#pragma unroll
          for (int e = 0; e < 4; ++e) acc[e] = mfma(a, bt[ks][e], ks == 0 ? f32x4{0.f, 0.f, 0.f, 0.f} : acc[e]);
        }
        *(LAS f32x4*)(L + L_CRED + cw * 1024 + lane * 16) = f32x4{acc[0][0], acc[1][0], acc[2][0], acc[3][0]};
        CBAR();
        STAMP(10);
        unsigned old = 0;
        if (lane == 0) old = __hip_atomic_fetch_add((LAS unsigned*)(L + L_FLAG) + F_CCNT, 1u, __ATOMIC_RELAXED, __HIP_MEMORY_SCOPE_WORKGROUP);
        old = __builtin_amdgcn_readfirstlane(old);
        CBAR();
        if (old == 3u) {
          f32x4 a = *(const LAS f32x4*)(L + L_CRED + lane * 16);
#pragma unroll
          for (int j = 1; j < 4; ++j) a += *(const LAS f32x4*)(L + L_CRED + j * 1024 + lane * 16);
          __builtin_amdgcn_raw_buffer_store_b64(v2u{nosent(pk(a[0], a[1])), nosent(pk(a[2], a[3]))}, rws, WS_HP + (g * NB + gq) * 1024 + cc * 128 + r16 * 8, 0, AUX_SC1);
        }
        STAMP(11);
      } else if (s < 2 && cw == 0) {
        const int ln = g * 8 + b * 2 + s, rb = ln >> 4, pg = lane >> 3, q4 = lane & 7;
        const int col = 128 * ((ln >> 2) & 3) + 8 * (8 * (ln & 1) + q4) + 4 * ((ln >> 1) & 1);
        const v2u xv = __builtin_amdgcn_raw_buffer_load_b64(rx, (rb * DM + col) * 2, 0, 0);
        while (rt() - tstart < HELLODELAY) __builtin_amdgcn_s_sleep(2);
        _Pragma("nounroll") for (unsigned sp = 0; sp < SPIN_G; ++sp) {
          CBAR();
          const v4u h0 = __builtin_amdgcn_raw_buffer_load_b128(rws, WS_HELLO + lane * 32, 0, AUX_SC1), h1 = __builtin_amdgcn_raw_buffer_load_b128(rws, WS_HELLO + lane * 32 + 16, 0, AUX_SC1);
          if (__all((h0.x == taglo) & (h0.y == taghi) & (h0.z == taglo) & (h0.w == taghi) & (h1.x == taglo) & (h1.y == taghi) & (h1.z == taglo) & (h1.w == taghi))) break;
          __builtin_amdgcn_s_sleep(8);
        }
        lds_wait(FL + F_DONE, 1, lane);
        STAMP(8);
        { const u64 t0 = rt(); while (rt() - t0 < RDELAY) __builtin_amdgcn_s_sleep(1); }
        v2u hv;
        _Pragma("nounroll") for (unsigned sp = 0; sp < SPIN_G; ++sp) {
          CBAR();
          hv = __builtin_amdgcn_raw_buffer_load_b64(rws, WS_HP + (pg * NB + rb) * 1024 + col * 2, 0, AUX_SC1);
          if (__all((hv.x != SENT) & (hv.y != SENT))) break;
        }
        STAMP(9);
        float h[4] = {blo(hv.x), bhi(hv.x), blo(hv.y), bhi(hv.y)};
#pragma unroll
        for (int d = 0; d < 4; ++d) { h[d] += dpp<ROR8>(h[d]); h[d] = x32_sum(x16_sum(h[d])); }
        {
          const unsigned a0 = pk(h[0], h[1]), a1 = pk(h[2], h[3]);
          const unsigned h0 = pk(blo(xv.x) + blo(a0), bhi(xv.x) + bhi(a0)), h1 = pk(blo(xv.y) + blo(a1), bhi(xv.y) + bhi(a1));
          h[0] = blo(h0); h[1] = bhi(h0); h[2] = blo(h1); h[3] = bhi(h1);
        }
        v4u pv[7];
        _Pragma("nounroll") for (unsigned sp = 0; sp < SPIN_G; ++sp) {
          CBAR();
          bool ok = true;
#pragma unroll
          for (int t = 0; t < 7; ++t) pv[t] = __builtin_amdgcn_raw_buffer_load_b128(rws, WS_OP + (pg + 8 * t) * 8192 + ln * 128 + q4 * 16, 0, AUX_SC1);
#pragma unroll
          for (int t = 0; t < 7; ++t) ok &= okv(pv[t]);
          if (__all(ok)) break;
        }
        STAMP(10);
        float y[4];
#pragma unroll
        for (int d = 0; d < 4; ++d) {
          float t = ((__uint_as_float(pv[0][d]) + __uint_as_float(pv[1][d])) + (__uint_as_float(pv[2][d]) + __uint_as_float(pv[3][d]))) +
                    ((__uint_as_float(pv[4][d]) + __uint_as_float(pv[5][d])) + __uint_as_float(pv[6][d]));
          t += dpp<ROR8>(t);
          y[d] = x32_sum(x16_sum(t));
        }
        const unsigned y0 = pk(y[0], y[1]), y1 = pk(y[2], y[3]);
        if (lane < 8) *(GAS v2u*)(out + rb * DM + col) = v2u{pk(h[0] + blo(y0), h[1] + bhi(y0)), pk(h[2] + blo(y1), h[3] + bhi(y1))};
        if (ln == 0 && lane == 0) __builtin_amdgcn_raw_buffer_store_b64(v2u{(unsigned)tag, (unsigned)(tag >> 32)}, rws, WS_EPOCH, 0, AUX_SC1);
        STAMP(11);
        if (ENDWAIT) {
          u64 tref = tstart;
          if (ENDREF) {
            u64 mn = ~0ull, mx = 0;
            for (int j = 0; j < 256; ++j) { const u64 t = __hip_atomic_load((GAS u64*)(ws + (3 << 20) + j * 8), __ATOMIC_RELAXED, __HIP_MEMORY_SCOPE_AGENT); mn = t < mn ? t : mn; mx = t > mx ? t : mx; }
            tref = ENDREF == 1 ? mn : mx;
          }
          while (rt() - tref < ENDWAIT) __builtin_amdgcn_s_sleep(1);
        }
        if (ENDMUL) {
          const u64 c0 = __builtin_amdgcn_s_memtime(); while (__builtin_amdgcn_s_memtime() - c0 < ENDMUL) __builtin_amdgcn_s_sleep(1); }
      }
      PDUMP();
      return;
    }
    if (isD && cw == 0) {
      while (rt() - tstart < HELLODELAY) __builtin_amdgcn_s_sleep(2);
      _Pragma("nounroll") for (unsigned sp = 0; sp < SPIN_G; ++sp) {
        CBAR();
        const v4u h0 = __builtin_amdgcn_raw_buffer_load_b128(rws, WS_HELLO + lane * 32, 0, AUX_SC1), h1 = __builtin_amdgcn_raw_buffer_load_b128(rws, WS_HELLO + lane * 32 + 16, 0, AUX_SC1);
        if (__all((h0.x == taglo) & (h0.y == taghi) & (h0.z == taglo) & (h0.w == taghi) & (h1.x == taglo) & (h1.y == taghi) & (h1.z == taglo) & (h1.w == taghi))) break;
        __builtin_amdgcn_s_sleep(8);
      }
      CBAR();
      FL[F_DONE] = 1u;
    }
  }
  if (!isD) { PDUMP(); return; }
  {
    const rsrc_t rx = mk(x, NB * DM * 2u);
    const int hb = w >> 2, hcol = 128 * (w & 3) + 2 * lane;
    const unsigned xp = __builtin_amdgcn_raw_buffer_load_b32(rx, (hb * DM + hcol) * 2, 0, 0);
    if (w < 8) { while (rt() - tstart < DDELAY) __builtin_amdgcn_s_sleep(2); }
    v4u bt[4][4];
    if (w < 4) {
      const GAS unsigned char* p = (const GAS unsigned char*)(r16 < 8 ? wg : wu) + (size_t)(128 * w + 8 * gq) * (FF * 2) + (32 * dI + 4 * (r16 & 7)) * 2;
      v2u ws4[4][8];
#pragma unroll
      for (int ks = 0; ks < 4; ++ks)
#pragma unroll
        for (int j = 0; j < 8; ++j) ws4[ks][j] = __builtin_nontemporal_load((const GAS v2u*)(p + (ks * 32 + j) * (FF * 2)));
      SCHED();
#pragma unroll
      for (int ks = 0; ks < 4; ++ks) tr4(ws4[ks], bt[ks]);
      const v4u ones = {0x3F803F80u, 0x3F803F80u, 0x3F803F80u, 0x3F803F80u};
      f32x4 acc[4];
#pragma unroll
      for (int ks = 0; ks < 4; ++ks)
#pragma unroll
        for (int e = 0; e < 4; ++e) acc[e] = mfma(ones, bt[ks][e], ks == 0 ? f32x4{0.f, 0.f, 0.f, 0.f} : acc[e]);
      if (lane < 16) *(LAS f32x4*)(L + L_SRED + w * 256 + lane * 16) = f32x4{acc[0][0], acc[1][0], acc[2][0], acc[3][0]};
    } else if (w < 8) {
      const rsrc_t rw = mk(wd, FF * DM * 2u);
      v4u src[8], o8[8];
#pragma unroll
      for (int j = 0; j < 8; ++j) src[j] = __builtin_amdgcn_raw_buffer_load_b128(rw, (unsigned)(32 * dI + 8 * gq + j) * 1024 + (w - 4) * 256 + r16 * 16, 0, AUX_NT);
      SCHED();
      tr8(src, o8);
#pragma unroll
      for (int e = 0; e < 8; ++e) bt[e >> 2][e & 3] = o8[e];
    }
    STAMP(7);
    lds_wait(FL + F_DONE, 1, lane);
    while (rt() - tstart < HPDELAY) __builtin_amdgcn_s_sleep(4);
    unsigned hp[8];
    _Pragma("nounroll") for (unsigned sp = 0; sp < SPIN_G; ++sp) {
      CBAR();
      bool ok = true;
#pragma unroll
      for (int j = 0; j < 8; ++j) hp[j] = __builtin_amdgcn_raw_buffer_load_b32(rws, WS_HP + (j * NB + hb) * 1024 + hcol * 2, 0, AUX_SC1);
#pragma unroll
      for (int j = 0; j < 8; ++j) ok &= hp[j] != SENT;
      if (__all(ok)) break;
    }
    STAMP(8);
    {
      float a0 = 0.f, a1 = 0.f, a2 = 0.f, a3 = 0.f;
#pragma unroll
      for (int j = 0; j < 4; ++j) { a0 += blo(hp[j]); a1 += bhi(hp[j]); a2 += blo(hp[4 + j]); a3 += bhi(hp[4 + j]); }
      const unsigned at = pk(a0 + a2, a1 + a3);
      *(LAS unsigned*)(L + L_OA + hb * 1024 + hcol * 2) = pk(blo(xp) + blo(at), bhi(xp) + bhi(at));
    }
    WGBAR();
    STAMP(9);
    f32x2 sg2, su2;
    if (w < 4) {
      f32x4 acc[4];
#pragma unroll
      for (int ks = 0; ks < 4; ++ks) {
        const v4u a = *(const LAS v4u*)(L + L_OA + (r16 >> 2) * 1024 + w * 256 + ks * 64 + gq * 16);
#pragma unroll
        for (int e = 0; e < 4; ++e) acc[e] = mfma(a, bt[ks][e], ks == 0 ? f32x4{0.f, 0.f, 0.f, 0.f} : acc[e]);
      }
      *(LAS f32x4*)(L + L_CRED + w * 1024 + lane * 16) = f32x4{acc[0][0], acc[1][0], acc[2][0], acc[3][0]};
    } else if (w < 8) {
      sg2 = f32x2{0.f, 0.f}; su2 = f32x2{0.f, 0.f};
#pragma unroll
      for (int j = 0; j < 4; ++j) {
        sg2 += *(const LAS f32x2*)(L + L_SRED + j * 256 + gq * 32 + (lane & 3) * 8);
        su2 += *(const LAS f32x2*)(L + L_SRED + j * 256 + 128 + gq * 32 + (lane & 3) * 8);
      }
    } else if (w < 12) {
      const v4u hv = *(const LAS v4u*)(L + L_OA + (w - 8) * 1024 + lane * 16);
      float sum = 0.f;
#pragma unroll
      for (int d = 0; d < 4; ++d) sum += blo(hv[d]) + bhi(hv[d]);
      sum += dpp<XOR1>(sum); sum += dpp<XOR2>(sum); sum += dpp<ROR4>(sum); sum += dpp<ROR8>(sum); sum = x32_sum(x16_sum(sum));
      const float mean = sum * (1.f / DM);
      float var = 0.f;
#pragma unroll
      for (int d = 0; d < 4; ++d) { const float t0 = blo(hv[d]) - mean, t1 = bhi(hv[d]) - mean; var = fmaf(t0, t0, var); var = fmaf(t1, t1, var); }
      var += dpp<XOR1>(var); var += dpp<XOR2>(var); var += dpp<ROR4>(var); var += dpp<ROR8>(var); var = x32_sum(x16_sum(var));
      if (lane == 0) *(LAS f32x2*)(L + L_STAT + (w - 8) * 8) = f32x2{mean, 1.f / sqrtf(var * (1.f / DM) + 1e-5f)};
    }
    WGBAR();
    STAMP(10);
    if (w >= 4 && w < 8) {
      const int bb = r16 >> 2;
      f32x2 gg = {0.f, 0.f}, uu = {0.f, 0.f};
#pragma unroll
      for (int j = 0; j < 4; ++j) {
        gg += *(const LAS f32x2*)(L + L_CRED + j * 1024 + bb * 256 + gq * 32 + (lane & 3) * 8);
        uu += *(const LAS f32x2*)(L + L_CRED + j * 1024 + bb * 256 + 128 + gq * 32 + (lane & 3) * 8);
      }
      const f32x2 st = *(const LAS f32x2*)(L + L_STAT + bb * 8);
      const float g0 = st.y * (gg.x - st.x * sg2.x), g1 = st.y * (gg.y - st.x * sg2.y);
      const float u0 = st.y * (uu.x - st.x * su2.x), u1 = st.y * (uu.y - st.x * su2.y);
      const float h0 = u0 * __builtin_amdgcn_rcpf(1.f + __builtin_amdgcn_exp2f(g0 * -1.4426950408889634f));
      const float h1 = u1 * __builtin_amdgcn_rcpf(1.f + __builtin_amdgcn_exp2f(g1 * -1.4426950408889634f));
      const unsigned hh = pk(h0, h1);
      const v4u a = {dppu<0x00>(hh), dppu<0x55>(hh), dppu<0xAA>(hh), dppu<0xFF>(hh)};
      unsigned y[8];
#pragma unroll
      for (int e = 0; e < 8; ++e) y[e] = nosent(__float_as_uint(mfma(a, bt[e >> 2][e & 3], f32x4{0.f, 0.f, 0.f, 0.f})[0]));
      const unsigned o = WS_OP + dI * 8192 + gq * 2048 + (w - 4) * 512 + r16 * 16;
      __builtin_amdgcn_raw_buffer_store_b128(v4u{y[0], y[1], y[2], y[3]}, rws, o, 0, AUX_SC1);
      __builtin_amdgcn_raw_buffer_store_b128(v4u{y[4], y[5], y[6], y[7]}, rws, o + 256, 0, AUX_SC1);
    }
    STAMP(11);
    PDUMP();
  }
}
}

extern "C" void kernel_launch(void* const* d_in, const int* in_sizes, int n_in, void* d_out, int out_size, void* d_ws, size_t ws_size, hipStream_t stream) {
  (void)in_sizes; (void)n_in; (void)out_size; (void)ws_size;
  fused_decode<<<dim3(256), dim3(1024), 0, stream>>>((const u16*)d_in[0], (const u16*)d_in[1], (const u16*)d_in[2], (const u16*)d_in[3], (const u16*)d_in[4],
                                                     (const u16*)d_in[6], (const u16*)d_in[7], (const u16*)d_in[8], (u16*)d_out, (unsigned char*)d_ws);
}
```

```cpp
#include <hip/hip_runtime.h>
#include <stdint.h>

#define PROF 0
#define WFIRST 3
#define QDELTA 150
#define TC0 400
#define TD0 280
#define TWD 250
#define DLY2 50
#define DLY3 72
#define DLY5 95
#define VLEFT 1
#define VSPLIT 8
#define TK0 100
#define DRY 0
#define L2PF 0
#define KHOLD 0
#define KPRE 8

namespace {
typedef unsigned short u16;
typedef unsigned long long u64;
typedef unsigned v4u __attribute__((ext_vector_type(4)));
typedef unsigned v2u __attribute__((ext_vector_type(2)));
typedef int v4i __attribute__((ext_vector_type(4)));
typedef float f32x4 __attribute__((ext_vector_type(4)));
typedef float f32x2 __attribute__((ext_vector_type(2)));
typedef short bf16x8 __attribute__((ext_vector_type(8)));
typedef __bf16 bf16x2 __attribute__((ext_vector_type(2)));
#define GAS __attribute__((address_space(1)))
#define LAS __attribute__((address_space(3)))
typedef __amdgpu_buffer_rsrc_t rsrc_t;

constexpr int NB = 4, DM = 512, HKV = 8, SK = 8192, WIN = 2048, FF = 1792, NQ = 4096;
constexpr int NSPL = 6, NDCU = 28;
constexpr int AUX_NT = 2, AUX_SC1 = 16;
constexpr unsigned SENT = 0xFFFFFFFFu, ONE2 = 0x3F803F80u;
constexpr int WS_EPOCH = 0, WS_HB = 4096, WS_HC = WS_HB + 32 * 128, WS_HA = WS_HC + 32 * 128, WS_QG = WS_HA + 8 * 32 * 128;
constexpr int WS_OREC = WS_QG + 8 * 4 * 4 * 512 * 8, OREC_SZ = 1152;
constexpr int WS_HP = WS_OREC + 8 * 24 * OREC_SZ, WS_OP = WS_HP + 8 * 4 * 512 * 2, WS_END = WS_OP + NDCU * 8192;
constexpr int WS_PROF = 2 << 20;
constexpr int L_FLAG = 0, L_MW = 512, L_QS = 1024, L_OA = L_QS + 1024, L_HH = L_OA + 4096, L_SS = L_HH + 512;
constexpr int L_QRED = L_SS + 4096, L_PART = L_QRED + 5120, L_USED = L_PART + 11 * 2048;
constexpr int L_TOTAL = 84 * 1024;
static_assert(L_USED <= L_TOTAL, "lds");
enum { F_QS = 4, F_QPUB = 12, F_QR = 16, F_CL = 24, F_GO = 32, F_SR = 72, F_MW = 80, F_VL = 96, F_OPF = 112 };
constexpr int CODE_BYTES = 16384;
constexpr unsigned SPIN_G = 1u << 17, SPIN_L = 1u << 21;

__device__ __forceinline__ u64 rt() { return __builtin_amdgcn_s_memrealtime(); }
__device__ __forceinline__ rsrc_t mk(const void* p, unsigned bytes) { return __builtin_amdgcn_make_buffer_rsrc((void*)p, 0, bytes, 0x00020000); }
__device__ __forceinline__ f32x4 mfma(v4u a, v4u b, f32x4 c) { return __builtin_amdgcn_mfma_f32_16x16x32_bf16(__builtin_bit_cast(bf16x8, a), __builtin_bit_cast(bf16x8, b), c, 0, 0, 0); }
__device__ __forceinline__ unsigned pk(float lo, float hi) { return __builtin_bit_cast(unsigned, __builtin_convertvector(f32x2{lo, hi}, bf16x2)); }
__device__ __forceinline__ float blo(unsigned v) { return __builtin_bit_cast(float, v << 16); }
__device__ __forceinline__ float bhi(unsigned v) { return __builtin_bit_cast(float, v & 0xffff0000u); }
__device__ __forceinline__ unsigned plo(unsigned a, unsigned b) { return __builtin_amdgcn_perm(b, a, 0x05040100u); }
__device__ __forceinline__ unsigned phi(unsigned a, unsigned b) { return __builtin_amdgcn_perm(b, a, 0x07060302u); }
__device__ __forceinline__ unsigned nosent(unsigned v) { return v < 0xFFFFFFFEu ? v : 0xFFFFFFFEu; }
template <int C> __device__ __forceinline__ float dpp(float x) { return __builtin_bit_cast(float, __builtin_amdgcn_mov_dpp(__builtin_bit_cast(int, x), C, 0xf, 0xf, true)); }
template <int C> __device__ __forceinline__ unsigned dppu(unsigned x) { return (unsigned)__builtin_amdgcn_mov_dpp((int)x, C, 0xf, 0xf, true); }
constexpr int XOR1 = 0xB1, XOR2 = 0x4E, ROR4 = 0x124, ROR8 = 0x128;
__device__ __forceinline__ float x16_sum(float x) { auto s = __builtin_amdgcn_permlane16_swap(__float_as_uint(x), __float_as_uint(x), false, false); return __uint_as_float(s[0]) + __uint_as_float(s[1]); }
__device__ __forceinline__ float x32_sum(float x) { auto s = __builtin_amdgcn_permlane32_swap(__float_as_uint(x), __float_as_uint(x), false, false); return __uint_as_float(s[0]) + __uint_as_float(s[1]); }
__device__ __forceinline__ float x16_max(float x) { auto s = __builtin_amdgcn_permlane16_swap(__float_as_uint(x), __float_as_uint(x), false, false); return fmaxf(__uint_as_float(s[0]), __uint_as_float(s[1])); }
__device__ __forceinline__ float x32_max(float x) { auto s = __builtin_amdgcn_permlane32_swap(__float_as_uint(x), __float_as_uint(x), false, false); return fmaxf(__uint_as_float(s[0]), __uint_as_float(s[1])); }
__device__ __forceinline__ float row_sum(float x) { x += dpp<XOR1>(x); x += dpp<XOR2>(x); x += dpp<ROR4>(x); x += dpp<ROR8>(x); return x; }
__device__ __forceinline__ float row_max(float x) { x = fmaxf(x, dpp<XOR1>(x)); x = fmaxf(x, dpp<XOR2>(x)); x = fmaxf(x, dpp<ROR4>(x)); x = fmaxf(x, dpp<ROR8>(x)); return x; }
__device__ __forceinline__ float dot2(unsigned a, unsigned b, float c) { return __builtin_amdgcn_fdot2_f32_bf16(__builtin_bit_cast(bf16x2, a), __builtin_bit_cast(bf16x2, b), c, false); }
#define CBAR() asm volatile("" ::: "memory")
#define SCHED() __builtin_amdgcn_sched_barrier(0)
#define DRAIN() do { asm volatile("s_waitcnt vmcnt(0)" ::: "memory"); __builtin_amdgcn_s_waitcnt(0x0F70); } while (0)
#define PINV(x) asm volatile("" : "+v"(x))
#define PINS(x) asm volatile("" : "+s"(x))
#define WGBAR() do { SCHED(); asm volatile("s_waitcnt lgkmcnt(0)" ::: "memory"); __builtin_amdgcn_s_barrier(); CBAR(); SCHED(); } while (0)
#define GSPIN _Pragma("nounroll") for (unsigned sp = 0; sp < SPIN_G; ++sp)
#define LD32(o, so) __builtin_amdgcn_raw_buffer_load_b32(rws, (o), (so), AUX_SC1)
#define LD64(o, so) __builtin_amdgcn_raw_buffer_load_b64(rws, (o), (so), AUX_SC1)
#define LD128(o, so) __builtin_amdgcn_raw_buffer_load_b128(rws, (o), (so), AUX_SC1)
#define ST64(v, o) __builtin_amdgcn_raw_buffer_store_b64((v), rws, (o), 0, AUX_SC1)
#define ST128(v, o) __builtin_amdgcn_raw_buffer_store_b128((v), rws, (o), 0, AUX_SC1)
__device__ __forceinline__ bool okv(v4u r) { return max(max(r.x, r.y), max(r.z, r.w)) != SENT; }
__device__ __forceinline__ void lds_wait(volatile LAS unsigned* f, unsigned tg) {
  SCHED();
#pragma nounroll
  for (unsigned s = 0; s < SPIN_L; ++s) { if (__all(*f == tg)) break; }
  CBAR();
  SCHED();
}
__device__ __forceinline__ void lds_set(volatile LAS unsigned* f, unsigned tg) { CBAR(); *f = tg; CBAR(); }
__device__ __forceinline__ void until(u64 t0, unsigned ticks) { while (rt() - t0 < ticks) __builtin_amdgcn_s_sleep(1); }
__device__ __forceinline__ v2u hint(unsigned taglo, unsigned taghi) { return v2u{taglo | ((unsigned)rt() & 0xFFFFu), taghi}; }
__device__ __forceinline__ bool hint_ok(v2u h, unsigned taglo, unsigned taghi) { return (h.y == taghi) & ((h.x ^ taglo) < 65536u); }
__device__ __forceinline__ void wait_age(v2u h, unsigned dly) {
#pragma nounroll
  for (unsigned s = 0; s < 4096u; ++s) { if (__all((((unsigned)rt() - h.x) & 0xFFFFu) >= dly)) break; }
}
template <int SET, int NSET> __device__ __forceinline__ void watch(rsrc_t rws, unsigned ho, unsigned taglo, unsigned taghi, unsigned dly, volatile LAS unsigned* go, unsigned tg, int lane) {
  constexpr u64 ALL = SET * NSET == 64 ? ~0ull : (1ull << (SET * NSET)) - 1;
  v2u h;
  u64 seen = 0;
#pragma nounroll
  for (unsigned sp = 0; sp < SPIN_G; ++sp) {
    if (seen != ALL) {
      CBAR();
      h = LD64(ho, 0);
      seen = __builtin_amdgcn_ballot_w64(hint_ok(h, taglo, taghi)) & ALL;
    }
    const u64 ripe = __builtin_amdgcn_ballot_w64((((unsigned)rt() - h.x) & 0xFFFFu) >= dly) & seen;
    if (lane < NSET && ((unsigned)(ripe >> (SET * lane)) & ((1u << SET) - 1)) == (1u << SET) - 1) go[lane] = tg;
    if (ripe == ALL) break;
  }
  CBAR();
}
__device__ __forceinline__ u64 load_tag(const unsigned char* ws) {
  u64 e;
  asm volatile("s_load_dwordx2 %0, %1, 0x0\n\ts_waitcnt lgkmcnt(0)" : "=s"(e) : "s"(ws) : "memory");
  return (e + 1ull) & 0xFFFFFFFFFFFFull;
}
__device__ __forceinline__ v4i mkd(const void* p, unsigned bytes) { return v4i{(int)(u64)p, (int)((u64)p >> 32) & 0xFFFF, (int)bytes, 0x00020000}; }
template <int OFF> __device__ __forceinline__ void ldnt(v4u& d, unsigned vo, v4i rs, unsigned so) {
  asm volatile("buffer_load_dwordx4 %0, %1, %2, %3 offen offset:%4 nt" : "=v"(d) : "v"(vo), "s"(rs), "s"(so), "i"(OFF) : "memory");
}
template <int LO, int HI> __device__ __forceinline__ void ldtile(v4u* d, unsigned vo, v4i rs, unsigned so) {
  if constexpr (LO < HI) { ldnt<(LO & 3) * 1024>(d[LO], vo, rs, so + (LO >> 2) * 4096); ldtile<LO + 1, HI>(d, vo, rs, so); }
}
__device__ __forceinline__ void ldnt2(v2u& d, unsigned vo, v4i rs, unsigned so) {
  asm volatile("buffer_load_dwordx2 %0, %1, %2, %3 offen nt" : "=v"(d) : "v"(vo), "s"(rs), "s"(so) : "memory");
}
__device__ __forceinline__ void ldp4(v4u& d, unsigned vo, v4i rs) {
  asm volatile("buffer_load_dwordx4 %0, %1, %2, 0 offen" : "=v"(d) : "v"(vo), "s"(rs) : "memory");
}
#define VMWAIT2(n, a, b) asm volatile("s_waitcnt vmcnt(%2)" : "+v"(a), "+v"(b) : "i"(n) : "memory")
__device__ __forceinline__ f32x2 ln_stats(v4u hv) {
  float sum = dot2(hv[0], ONE2, dot2(hv[1], ONE2, 0.f)) + dot2(hv[2], ONE2, dot2(hv[3], ONE2, 0.f));
  sum = x32_sum(x16_sum(row_sum(sum)));
  const float mean = sum * (1.f / DM);
  float v0 = 0.f, v1 = 0.f;
#pragma unroll
  for (int d = 0; d < 4; ++d) { const float t0 = blo(hv[d]) - mean, t1 = bhi(hv[d]) - mean; v0 = fmaf(t0, t0, v0); v1 = fmaf(t1, t1, v1); }
  const float var = x32_sum(x16_sum(row_sum(v0 + v1)));
  return f32x2{mean, __builtin_amdgcn_rsqf(var * (1.f / DM) + 1e-5f)};
}
#define STAMP(k) do {} while (0)
#define STAMPE(k) do {} while (0)
#define PDUMP() do {} while (0)
#define RETRY() do {} while (0)

__device__ __forceinline__ void tr4(const v2u* s, v4u* o) {
#pragma unroll
  for (int m = 0; m < 4; ++m) {
    o[0][m] = plo(s[2 * m].x, s[2 * m + 1].x); o[1][m] = phi(s[2 * m].x, s[2 * m + 1].x);
    o[2][m] = plo(s[2 * m].y, s[2 * m + 1].y); o[3][m] = phi(s[2 * m].y, s[2 * m + 1].y);
  }
#pragma unroll
  for (int e = 0; e < 4; ++e) PINV(o[e]);
}
__device__ __forceinline__ void tr8(const v4u* s, v4u* o) {
#pragma unroll
  for (int m = 0; m < 4; ++m)
#pragma unroll
    for (int d = 0; d < 4; ++d) { o[2 * d][m] = plo(s[2 * m][d], s[2 * m + 1][d]); o[2 * d + 1][m] = phi(s[2 * m][d], s[2 * m + 1][d]); }
#pragma unroll
  for (int e = 0; e < 8; ++e) PINV(o[e]);
}
__device__ __forceinline__ void product(LAS unsigned char* L, unsigned aa, unsigned pa, const v4u* bt) {
  const f32x4 Z4 = {0.f, 0.f, 0.f, 0.f};
  const v4u a0 = *(const LAS v4u*)(L + aa), a1 = *(const LAS v4u*)(L + aa + 64);
#pragma unroll
  for (int h = 0; h < 2; ++h) {
    f32x4 acc[4];
#pragma unroll
    for (int e = 0; e < 4; ++e) acc[e] = mfma(a0, bt[4 * h + e], Z4);
#pragma unroll
    for (int e = 0; e < 4; ++e) acc[e] = mfma(a1, bt[8 + 4 * h + e], acc[e]);
    *(LAS f32x4*)(L + pa + 16 * h) = f32x4{acc[0][0], acc[1][0], acc[2][0], acc[3][0]};
  }
}

__global__ void __launch_bounds__(1024) fused_decode(const u16* __restrict__ x, const u16* __restrict__ kc, const u16* __restrict__ vc,
                                                     const u16* __restrict__ wq, const u16* __restrict__ wo, const u16* __restrict__ wg,
                                                     const u16* __restrict__ wu, const u16* __restrict__ wd, u16* __restrict__ out, unsigned char* ws) {
  const u64 tstart = rt();
  __shared__ __attribute__((aligned(16))) unsigned char smem[L_TOTAL];
  LAS unsigned char* L = (LAS unsigned char*)smem;
  volatile LAS unsigned* FL = (volatile LAS unsigned*)(L + L_FLAG);
  const int lane = threadIdx.x & 63;
  int w = (__builtin_amdgcn_readfirstlane(threadIdx.x >> 6) + 11) & 15;
  const int c = blockIdx.x, g = c & 7, i = c >> 3;
  if (w == 11) {
    u64 pc0;
    unsigned t0, t1, t2, t3;
    asm volatile("s_getpc_b64 %0" : "=s"(pc0));
    const u64 a0 = (pc0 & ~127ull) + (u64)(i * 512 < CODE_BYTES - 512 ? i * 512 : CODE_BYTES - 512);
    asm volatile("s_load_dword %0, %4, 0x0\n\ts_load_dword %1, %4, 0x80\n\ts_load_dword %2, %4, 0x100\n\ts_load_dword %3, %4, 0x180\n\ts_waitcnt lgkmcnt(0)"
                 : "=&s"(t0), "=&s"(t1), "=&s"(t2), "=&s"(t3) : "s"(a0) : "memory");
  }
  const int gq = lane >> 4, r16 = lane & 15;
  const int b = (i * 43) >> 8, s = i - b * NSPL;
  const int ntile = s < 4 ? 11 : 10, tile0 = 11 * s - (s > 4 ? 1 : 0);
  const int cw = w - 12, rq = i & 3, cq = i >> 2, R0 = 128 * rq + 32 * cw;
  asm volatile("" :: "s"(x), "s"(kc), "s"(vc), "s"(wq), "s"(wo), "s"(wg), "s"(wu), "s"(wd), "s"(out), "s"(ws));
  v4u kf[8], vf[8], xa, xr;
  v2u wsrc[8];
  const unsigned kvbase = ((unsigned)(b * HKV + g) * SK + (SK - WIN) + 32u * (tile0 + w)) * 256u;
  const v4i rk = mkd(kc, NB * HKV * SK * 256u), rv = mkd(vc, NB * HKV * SK * 256u);
  if (w >= 12) {
    __builtin_amdgcn_s_setprio(3);
    const v4i rw = mkd(wq, DM * NQ * 2u), rxd = mkd(x, NB * DM * 2u);
    const unsigned o = (unsigned)(8 * gq) * (NQ * 2) + r16 * 8, so = R0 * (NQ * 2) + (512 * g + 64 * cq) * 2;
#pragma unroll
    for (int j = 0; j < 8; ++j) ldnt2(wsrc[j], o, rw, so + j * (NQ * 2));
    ldp4(xa, (r16 >> 2) * 1024 + (R0 + 8 * gq) * 2, rxd);
    ldp4(xr, cw * 1024 + lane * 16, rxd);
  } else if (i < 24 && w < ntile) {
    if (TK0) until(tstart, TK0);
    ldtile<0, KPRE>(kf, r16 * 64 + gq * 16, rk, kvbase);
  }
  STAMP(1);
  PINS(w);
  const rsrc_t rws = mk(ws, WS_END), rx = mk(x, NB * DM * 2u);
  const f32x4 Z4 = {0.f, 0.f, 0.f, 0.f};
  const v4u SV = {SENT, SENT, SENT, SENT};
  const unsigned orec = WS_OREC + (g * 24 + i) * OREC_SZ;

  if (__builtin_expect(w >= 12, 1)) {
    const u64 tag = load_tag(ws);
    const unsigned tg = (unsigned)tag, qhi = (unsigned)(tag >> 8), qlo = (unsigned)(tag << 24);
    asm volatile("s_waitcnt vmcnt(0)" : "+v"(wsrc[0]), "+v"(wsrc[1]), "+v"(wsrc[2]), "+v"(wsrc[3]), "+v"(wsrc[4]), "+v"(wsrc[5]), "+v"(wsrc[6]), "+v"(wsrc[7]), "+v"(xa), "+v"(xr) :: "memory");
    {
      v4u bt[4];
      tr4(wsrc, bt);
      if ((lane & 3) == 1) xa = v4u{ONE2, ONE2, ONE2, ONE2};
      f32x4 qp, cs;
#pragma unroll
      for (int e = 0; e < 4; ++e) { const f32x4 d = mfma(xa, bt[e], Z4); qp[e] = d[0]; cs[e] = d[1]; }
      *(LAS f32x4*)(L + L_QRED + cw * 1024 + lane * 16) = qp;
      if (lane < 16) *(LAS f32x4*)(L + L_QRED + 4096 + cw * 256 + lane * 16) = cs;
    }
    lds_set(FL + F_QR + cw, tg);
    f32x2 xst = ln_stats(xr);
    PINV(xst);
    STAMP(2);
    lds_wait(FL + F_QR + (lane & 3), tg);
    {
      float a[4], cs[4];
#pragma unroll
      for (int j = 0; j < 4; ++j) { a[j] = *(const LAS float*)(L + L_QRED + j * 1024 + cw * 256 + lane * 4); cs[j] = *(const LAS float*)(L + L_QRED + 4096 + j * 256 + lane * 4); }
      const float q = xst.y * (((a[0] + a[1]) + (a[2] + a[3])) - xst.x * ((cs[0] + cs[1]) + (cs[2] + cs[3])));
      ST64((v2u{qlo | ((__float_as_uint(q) + 0x80u) >> 8), qhi}), WS_QG + (((g * NB + cw) * 4 + rq) * 512 + 64 * cq + lane) * 8);
    }
    lds_set(FL + F_QPUB + cw, tg);
    STAMP(3);
    if (i < 24) {
      until(rt(), QDELTA);
      const unsigned qo = WS_QG + ((g * NB + b) * 4 * 512 + 2 * (cw * 64 + lane)) * 8;
      v4u qg[4];
      GSPIN {
        CBAR();
        bool ok = true;
#pragma unroll
        for (int j = 0; j < 4; ++j) qg[j] = LD128(qo, j * 4096);
#pragma unroll
        for (int j = 0; j < 4; ++j) ok &= (qg[j].y == qhi) & (qg[j].w == qhi) & ((qg[j].x ^ qlo) < (1u << 24)) & ((qg[j].z ^ qlo) < (1u << 24));
        if (__all(ok)) break;
        RETRY();
      }
      const float q0 = (__uint_as_float(qg[0].x << 8) + __uint_as_float(qg[1].x << 8)) + (__uint_as_float(qg[2].x << 8) + __uint_as_float(qg[3].x << 8));
      const float q1 = (__uint_as_float(qg[0].z << 8) + __uint_as_float(qg[1].z << 8)) + (__uint_as_float(qg[2].z << 8) + __uint_as_float(qg[3].z << 8));
      *(LAS unsigned*)(L + L_QS + (cw * 64 + lane) * 4) = pk(q0, q1);
      lds_set(FL + F_QS + cw, tg);
      STAMP(4);
      PDUMP();
      return;
    }
    __builtin_amdgcn_s_setprio(0);
  } else if (i < 24) {
    if (w < ntile) {
      const unsigned vo = lane * 16;
      const unsigned tg = (unsigned)load_tag(ws);
      if (KHOLD) lds_wait(FL + F_QPUB + (lane & 3), tg);
      ldtile<KPRE, 8>(kf, r16 * 64 + gq * 16, rk, kvbase);
      ldtile<0, VSPLIT>(vf, vo, rv, kvbase);
      const bool early = __all(FL[lane < 4 ? F_QS + lane : F_CL] == tg);
      if (early) {
        asm volatile("s_waitcnt vmcnt(%0)" :: "i"(VSPLIT) : "memory");
      } else {
        ldtile<VSPLIT, 8>(vf, vo, rv, kvbase);
        STAMP(2);
        lds_wait(FL + (lane < 4 ? F_QS + lane : F_CL), tg);
        asm volatile("s_waitcnt vmcnt(8)" ::: "memory");
      }
#pragma unroll
      for (int t = 0; t < 8; ++t) PINV(kf[t]);
      STAMP(3);
      const v4u qf = *(const LAS v4u*)(L + L_QS + r16 * 64 + gq * 16);
      float sc[8];
#pragma unroll
      for (int t = 0; t < 8; ++t) {
        const f32x4 d = mfma(kf[t], qf, Z4);
        sc[t] = (dpp<0x00>(d[0]) + dpp<0x55>(d[1])) + (dpp<0xAA>(d[2]) + dpp<0xFF>(d[3]));
      }
      float m = fmaxf(fmaxf(fmaxf(sc[0], sc[1]), fmaxf(sc[2], sc[3])), fmaxf(fmaxf(sc[4], sc[5]), fmaxf(sc[6], sc[7])));
      m = x32_max(x16_max(m));
      constexpr float C2 = 0.08838834764831845f * 1.4426950408889634f;
      const float ms = m * C2;
      float p[8], l = 0.f;
#pragma unroll
      for (int t = 0; t < 8; ++t) { p[t] = __builtin_amdgcn_exp2f(fmaf(sc[t], C2, -ms)); l += p[t]; }
      l = x32_sum(x16_sum(l));
      if ((lane & 0x33) == 0) *(LAS f32x2*)(L + L_MW + ((lane >> 2) * 12 + w) * 8) = f32x2{ms, l};
      lds_set(FL + F_MW + w, tg);
      v4u pf = {pk(p[0], p[1]), pk(p[2], p[3]), pk(p[4], p[5]), pk(p[6], p[7])};
      unsigned pa = L_PART + w * 2048 + gq * 512 + r16 * 32;
      PINV(pf); PINV(pa);
      if (early) ldtile<VSPLIT, 8>(vf, vo, rv, kvbase);
      STAMP(4);
      v4u vt[8];
#pragma unroll
      for (int mm = 0; mm < 4; ++mm) {
        if (mm == 0) VMWAIT2(6, vf[0], vf[1]);
        if (mm == 1) VMWAIT2(4, vf[2], vf[3]);
        if (mm == 2) VMWAIT2(2, vf[4], vf[5]);
        if (mm == 3) {
          if (VLEFT) lds_set(FL + F_VL + w, tg);
          VMWAIT2(0, vf[6], vf[7]);
          if (!VLEFT) lds_set(FL + F_VL + w, tg);
          STAMP(5);
        }
#pragma unroll
        for (int d = 0; d < 4; ++d) { vt[2 * d][mm] = plo(vf[2 * mm][d], vf[2 * mm + 1][d]); vt[2 * d + 1][mm] = phi(vf[2 * mm][d], vf[2 * mm + 1][d]); }
        if (mm < 3) {
#pragma unroll
          for (int e = 0; e < 8; ++e) PINV(vt[e]);
        }
        SCHED();
      }
      float o[8];
#pragma unroll
      for (int e = 0; e < 8; ++e) o[e] = mfma(pf, vt[e], Z4)[0];
      *(LAS f32x4*)(L + pa) = f32x4{o[0], o[1], o[2], o[3]};
      *(LAS f32x4*)(L + pa + 16) = f32x4{o[4], o[5], o[6], o[7]};
      lds_set(FL + F_OPF + w, tg);
      STAMP(6);
      if (w < 4) {
        const int jl = r16 < ntile ? r16 : 0;
        lds_wait(FL + F_MW + jl, tg);
        const f32x2 ml = *(const LAS f32x2*)(L + L_MW + (w * 12 + jl) * 8);
        const float mg = row_max(ml.x);
        const float wt = r16 < ntile ? __builtin_amdgcn_exp2f(ml.x - mg) : 0.f;
        const float lt = row_sum(wt * ml.y);
        float wj[11];
#pragma unroll
        for (int j = 0; j < 11; ++j) { wj[j] = __builtin_bit_cast(float, __builtin_amdgcn_readlane(__builtin_bit_cast(int, wt), j)); PINS(wj[j]); }
        if (lane == 0) ST64((v2u{nosent(__float_as_uint(mg)), nosent(__float_as_uint(lt))}), orec + 1024 + w * 8);
        unsigned ra = L_PART + w * 512 + lane * 8, rl = ra + (ntile - 1) * 2048, so = orec + w * 256 + lane * 4;
        PINV(ra); PINV(rl); PINV(so);
        lds_wait(FL + F_OPF + jl, tg);
        f32x2 pv[11];
#pragma unroll
        for (int j = 0; j < 10; ++j) pv[j] = *(const LAS f32x2*)(L + ra + j * 2048);
        pv[10] = *(const LAS f32x2*)(L + rl);
        SCHED();
        f32x2 a = pv[0] * wj[0], a2 = pv[1] * wj[1];
#pragma unroll
        for (int j = 2; j < 10; j += 2) { a += pv[j] * wj[j]; a2 += pv[j + 1] * wj[j + 1]; }
        a += pv[10] * wj[10];
        a += a2;
        const unsigned v = nosent(pk(a.x, a.y));
        const v4u o4 = {dppu<0x00>(v), dppu<0x55>(v), dppu<0xAA>(v), dppu<0xFF>(v)};
        if ((lane & 3) == 0) ST128(o4, so);
        STAMP(7);
      }
    } else if (w == 11) {
      __builtin_amdgcn_s_setprio(3);
      ST128(SV, orec + lane * 16);
      if (lane < 8) ST128(SV, orec + 1024 + lane * 16);
      const u64 tag = load_tag(ws);
      const unsigned taghi = (unsigned)(tag >> 16), taglo = (unsigned)(tag << 16), tg = (unsigned)tag;
      const bool isR = i < 8;
      const int ln = g * 8 + i, rb = ln >> 4, pg = lane >> 3, q4 = lane & 7, col = 32 * (ln & 15) + 4 * q4;
      v2u xv = {0u, 0u};
      if (isR) xv = __builtin_amdgcn_raw_buffer_load_b64(rx, (rb * DM + col) * 2, 0, 0);
      DRAIN();
      lds_set(FL + F_CL, tg);
      STAMP(3);
      lds_wait(FL + F_VL + (lane < ntile ? lane : 0), tg);
      if (lane == 0) ST64(hint(taglo, taghi), WS_HA + (g * 32 + i) * 128);
      STAMPE(4);
      if (L2PF) {
        unsigned t = 0;
#pragma unroll
        for (int u = 0; u < 3; ++u) {
          const int id = (u * 24 + i) * 64 + lane, dd = g + 8 * (id & 3), r = id >> 2;
          const u16* src = r < 512 ? wd + (size_t)dd * (64 * DM) + r * 64 : ((r & 1) ? wu : wg) + (size_t)(511 - ((r - 512) >> 1)) * FF + dd * 64;
          if (dd < NDCU) t ^= __builtin_nontemporal_load((const GAS unsigned*)src);
        }
        asm volatile("" :: "v"(t));
      }
      if (isR) {
        const rsrc_t ro = mk(out, NB * DM * 2u);
        unsigned pf = __builtin_amdgcn_raw_buffer_load_b32(ro, 0, 0, AUX_SC1);
        asm volatile("" :: "v"(pf));
        v2u hb;
        GSPIN {
          CBAR();
          hb = LD64(WS_HB + ((lane & 7) * 4 + ((ln >> 2) & 3)) * 128, 0);
          if (__all(hint_ok(hb, taglo, taghi))) break;
          __builtin_amdgcn_s_sleep(2);
        }
        STAMP(5);
        wait_age(hb, DLY3 + 30);
        v2u hv;
        GSPIN {
          CBAR();
          hv = LD64(WS_HP + (pg * NB + rb) * 1024 + col * 2, 0);
          if (__all(max(hv.x, hv.y) != SENT)) break;
        }
        STAMP(6);
        float h[4] = {blo(hv.x), bhi(hv.x), blo(hv.y), bhi(hv.y)};
#pragma unroll
        for (int d = 0; d < 4; ++d) { h[d] += dpp<ROR8>(h[d]); h[d] = x32_sum(x16_sum(h[d])); }
        {
          const unsigned a0 = pk(h[0], h[1]), a1 = pk(h[2], h[3]);
          const unsigned h0 = pk(blo(xv.x) + blo(a0), bhi(xv.x) + bhi(a0)), h1 = pk(blo(xv.y) + blo(a1), bhi(xv.y) + bhi(a1));
          h[0] = blo(h0); h[1] = bhi(h0); h[2] = blo(h1); h[3] = bhi(h1);
        }
        unsigned po = WS_OP + pg * 8192 + ln * 128 + q4 * 16, po3 = pg < 4 ? po + 24 * 8192 : po, oo = (rb * DM + col) * 2;
        float k3 = pg < 4 ? 1.f : 0.f;
        PINV(po); PINV(po3); PINV(oo); PINV(k3); PINV(h[0]); PINV(h[1]); PINV(h[2]); PINV(h[3]);
        v2u hc;
        GSPIN {
          CBAR();
          hc = LD64(WS_HC + (lane < NDCU ? lane : 0) * 128, 0);
          if (__all(hint_ok(hc, taglo, taghi))) break;
        }
        wait_age(hc, DLY5);
        STAMP(7);
        v4u pv[4];
        GSPIN {
          CBAR();
#pragma unroll
          for (int t = 0; t < 3; ++t) pv[t] = LD128(po, t * 8 * 8192);
          pv[3] = LD128(po3, 0);
          if (__all(okv(pv[0]) & okv(pv[1]) & okv(pv[2]) & okv(pv[3]))) break;
          RETRY();
        }
        STAMP(8);
        float y[4];
#pragma unroll
        for (int d = 0; d < 4; ++d) {
          float t = (__uint_as_float(pv[0][d]) + __uint_as_float(pv[1][d])) + fmaf(__uint_as_float(pv[3][d]), k3, __uint_as_float(pv[2][d]));
          t += dpp<ROR8>(t);
          y[d] = x32_sum(x16_sum(t));
        }
        const unsigned y0 = pk(y[0], y[1]), y1 = pk(y[2], y[3]);
        if (lane < 8) __builtin_amdgcn_raw_buffer_store_b64(v2u{pk(h[0] + blo(y0), h[1] + bhi(y0)), pk(h[2] + blo(y1), h[3] + bhi(y1))}, ro, oo, 0, AUX_SC1);
        if (ln == 0 && lane == 0) ST64((v2u{(unsigned)tag, (unsigned)(tag >> 32)}), WS_EPOCH);
        STAMPE(9);
      }
    }
    PDUMP();
    return;
  }

  const int jq = i - 24;
  const bool isC = jq < 4;
  const int dI = g + 8 * (jq - 4);
  if (!isC && dI >= NDCU) { PDUMP(); return; }
  const u64 tag = load_tag(ws);
  const unsigned taghi = (unsigned)(tag >> 16), taglo = (unsigned)(tag << 16), tg = (unsigned)tag;
#define LBAR() do { if (live) WGBAR(); } while (0)
#define GATHER_H() do { \
    float x0 = blo(xp), x1 = bhi(xp); \
    PINV(x0); PINV(x1); \
    if (live) { \
      const v4i rd = mkd(ws, WS_END); \
      unsigned done = 0; \
      _Pragma("nounroll") for (unsigned sp = 0; sp < SPIN_L && done != 255u; ++sp) { \
        const unsigned m = (unsigned)__builtin_amdgcn_ballot_w64(FL[F_GO + (lane & 7)] == tg) & 255u, n = m & ~done; \
        if (n) { \
          _Pragma("unroll") for (int j = 0; j < 8; ++j) \
            if (n & (1u << j)) asm volatile("buffer_load_dword %0, %1, %2, %3 offen sc1" : "+v"(hp[j]) : "v"(ho), "s"(rd), "s"(j * 4096) : "memory"); \
          done = m; \
        } \
      } \
      STAMP(6); \
      asm volatile("s_waitcnt vmcnt(0)" : "+v"(hp[0]), "+v"(hp[1]), "+v"(hp[2]), "+v"(hp[3]), "+v"(hp[4]), "+v"(hp[5]), "+v"(hp[6]), "+v"(hp[7]) :: "memory"); \
    } \
    GSPIN { \
      if (__all(max(max(max(hp[0], hp[1]), max(hp[2], hp[3])), max(max(hp[4], hp[5]), max(hp[6], hp[7]))) != SENT)) break; \
      RETRY(); \
      CBAR(); \
      _Pragma("unroll") for (int j = 0; j < 8; ++j) hp[j] = LD32(ho, j * 4096); \
    } \
    DRAIN(); \
    STAMP(7); \
    float a0 = 0.f, a1 = 0.f, a2 = 0.f, a3 = 0.f; \
    _Pragma("unroll") for (int j = 0; j < 4; ++j) { a0 = dot2(hp[j], klo, a0); a1 = dot2(hp[j], khi, a1); a2 = dot2(hp[4 + j], klo, a2); a3 = dot2(hp[4 + j], khi, a3); } \
    const unsigned t0 = pk(a0 + a2, a1 + a3); \
    *(LAS unsigned*)(L + oa) = pk(x0 + blo(t0), x1 + bhi(t0)); \
  } while (0)
#define GATHER_H_SETUP() \
    unsigned xp = 0; \
    if (live && !isC) xp = __builtin_amdgcn_raw_buffer_load_b32(rx, w * 256 + lane * 4, 0, 0); \
    unsigned klo = 0x00003F80u, khi = 0x3F800000u, ho = WS_HP + w * 256 + lane * 4, oa = L_OA + w * 256 + lane * 4; \
    unsigned hp[8] = {0u, 0u, 0u, 0u, 0u, 0u, 0u, 0u}; \
    PINS(klo); PINS(khi); PINV(ho); PINV(oa)
#pragma nounroll
  for (int step = !DRY ? 2 : w == 8 ? 0 : w == 12 ? 1 : 2; step < 3; ++step) {
  int live = __builtin_amdgcn_readfirstlane(step >> 1), prod = __builtin_amdgcn_readfirstlane((int)(w < 8 || step == 0));
  PINS(live); PINS(prod);
  int lane_ = threadIdx.x & 63;
  PINV(lane_);
  const int lane = lane_, gq = lane >> 4, r16 = lane & 15;
  if (prod) {
    const unsigned stride = isC ? DM * 2 : FF * 2;
    const GAS unsigned char* p = isC ? (const GAS unsigned char*)wo + (size_t)(512 * g) * (DM * 2) + (128 * jq + 8 * r16) * 2
                                     : (const GAS unsigned char*)(r16 < 8 ? wg : wu) + (64 * dI + 8 * (r16 & 7)) * 2;
    p += (size_t)(64 * w + 8 * gq) * stride;
    unsigned aa = L_OA + (r16 >> 2) * 1024 + w * 128 + gq * 16, pa = L_PART + w * 2048 + gq * 512 + r16 * 32;
    PINV(aa); PINV(pa);
    GATHER_H_SETUP();
    {
      v4u bt[16];
      if (!live) {
#pragma unroll
        for (int e = 0; e < 16; ++e) asm volatile("" : "=v"(bt[e]));
      } else {
        v4u src[16];
        PINV(p);
        until(tstart, isC ? TC0 : TD0);
#pragma unroll
        for (int j = 0; j < WFIRST; ++j) src[j] = __builtin_nontemporal_load((const GAS v4u*)(p + ((j >> 3) * 32 + (j & 7)) * stride));
        lds_wait(FL + F_QPUB + (lane & 3), tg);
#pragma unroll
        for (int j = WFIRST; j < 16; ++j) src[j] = __builtin_nontemporal_load((const GAS v4u*)(p + ((j >> 3) * 32 + (j & 7)) * stride));
        SCHED();
        STAMP(4);
        tr8(src, bt);
        tr8(src + 8, bt + 8);
        if (!isC) {
          const v4u ones = {ONE2, ONE2, ONE2, ONE2};
          float cs[8];
#pragma unroll
          for (int e = 0; e < 8; ++e) cs[e] = mfma(ones, bt[8 + e], mfma(ones, bt[e], Z4))[0];
          if (lane < 16) {
            *(LAS f32x4*)(L + L_SS + w * 512 + lane * 32) = f32x4{cs[0], cs[1], cs[2], cs[3]};
            *(LAS f32x4*)(L + L_SS + w * 512 + lane * 32 + 16) = f32x4{cs[4], cs[5], cs[6], cs[7]};
          }
          lds_set(FL + F_SR + w, tg);
        }
        DRAIN();
        STAMP(5);
        if (w == 0) {
          lds_wait(FL + F_CL + (lane & 7), tg);
          if (isC) watch<6, 4>(rws, WS_HA + (g * 32 + (lane < 24 ? lane : 0)) * 128, taglo, taghi, DLY2, FL + F_GO, tg, lane);
          else watch<4, 8>(rws, WS_HB + (lane & 31) * 128, taglo, taghi, DLY3, FL + F_GO, tg, lane);
          STAMP(10);
          DRAIN();
        } else if (w == 1 && isC) {
          lds_wait(FL + F_SR + (lane & 7), tg);
          if (lane == 0) ST64(hint(taglo, taghi), WS_HB + (g * 4 + jq) * 128);
        }
      }
      if (!isC) GATHER_H();
      LBAR();
      STAMP(8);
      product(L, aa, pa, bt);
      STAMP(9);
      LBAR();
      if (!isC) LBAR();
    }
  } else if (isC) {
    const int v = w & 7, bb = v >> 1, head = 2 * (v & 1) + (lane >> 5), sg = gq & 1;
    unsigned ra = L_PART + (w & 3) * 512 + lane * 8, so = WS_HP + (g * NB + (w & 3)) * 1024 + jq * 256 + lane * 4;
    unsigned rd = WS_OREC + (g * 24 + bb * NSPL + sg) * OREC_SZ + head * 256 + r16 * 16, rs = WS_OREC + (g * 24 + bb * NSPL + sg) * OREC_SZ + 1024 + head * 8;
    unsigned oa = L_OA + bb * 1024 + head * 256 + r16 * 16;
    PINV(ra); PINV(so); PINV(rd); PINV(rs); PINV(oa);
    v4u od[3] = {SV, SV, SV};
    v2u st[3] = {v2u{0u, 0u}, v2u{0u, 0u}, v2u{0u, 0u}};
    {
      if (live) {
        if (w == 15) ST128(SV, WS_HP + (g * NB + gq) * 1024 + jq * 256 + r16 * 16);
        DRAIN();
        lds_set(FL + F_CL + v, tg);
        STAMP(5);
        lds_wait(FL + F_GO + bb, tg);
        STAMP(6);
        GSPIN {
          CBAR();
#pragma unroll
          for (int u = 0; u < 3; ++u) st[u] = LD64(rs, 2 * u * OREC_SZ);
          SCHED();
#pragma unroll
          for (int u = 0; u < 3; ++u) od[u] = LD128(rd, 2 * u * OREC_SZ);
          SCHED();
          if (__all(okv(od[0]) & okv(od[1]) & okv(od[2]) & (max(max(max(st[0].x, st[0].y), max(st[1].x, st[1].y)), max(st[2].x, st[2].y)) != SENT))) break;
          RETRY();
        }
        DRAIN();
        lds_set(FL + F_SR + v, tg);
        STAMP(7);
      }
      const float m0 = __uint_as_float(st[0].x), m1 = __uint_as_float(st[1].x), m2 = __uint_as_float(st[2].x);
      const float mg = x16_max(fmaxf(fmaxf(m0, m1), m2));
      float wt[3] = {__builtin_amdgcn_exp2f(m0 - mg), __builtin_amdgcn_exp2f(m1 - mg), __builtin_amdgcn_exp2f(m2 - mg)};
      const float inv = __builtin_amdgcn_rcpf(x16_sum(wt[0] * __uint_as_float(st[0].y) + wt[1] * __uint_as_float(st[1].y) + wt[2] * __uint_as_float(st[2].y)));
      float a[8];
#pragma unroll
      for (int u = 0; u < 3; ++u) {
#pragma unroll
        for (int d = 0; d < 4; ++d) {
          a[2 * d] = u == 0 ? wt[u] * blo(od[u][d]) : fmaf(wt[u], blo(od[u][d]), a[2 * d]);
          a[2 * d + 1] = u == 0 ? wt[u] * bhi(od[u][d]) : fmaf(wt[u], bhi(od[u][d]), a[2 * d + 1]);
        }
      }
#pragma unroll
      for (int d = 0; d < 8; ++d) a[d] = x16_sum(a[d]) * inv;
      *(LAS v4u*)(L + oa) = v4u{pk(a[0], a[1]), pk(a[2], a[3]), pk(a[4], a[5]), pk(a[6], a[7])};
      STAMP(8);
      LBAR();
      LBAR();
      if (w >= 12) {
        f32x2 pv[8];
#pragma unroll
        for (int j = 0; j < 8; ++j) pv[j] = *(const LAS f32x2*)(L + ra + j * 2048);
        SCHED();
        const f32x2 t = ((pv[0] + pv[1]) + (pv[2] + pv[3])) + ((pv[4] + pv[5]) + (pv[6] + pv[7]));
        const unsigned vv = nosent(pk(t.x, t.y));
        const v4u o4 = {dppu<0x00>(vv), dppu<0x55>(vv), dppu<0xAA>(vv), dppu<0xFF>(vv)};
        if ((lane & 3) + 4 - 4 * live == 0) ST128(o4, so);
        STAMPE(9);
      }
    }
  } else {
    float sgs = 0.f, sus = 0.f;
    GATHER_H_SETUP();
    unsigned ra = L_PART + (w & 3) * 512 + lane * 4, hw = L_HH + (w & 3) * 128 + lane * 2, ha = L_HH + (r16 >> 2) * 128 + gq * 16;
    unsigned so = WS_OP + dI * 8192 + gq * 2048 + (w & 7) * 256 + r16 * 16, sa = L_OA + (w & 3) * 1024 + lane * 16;
    PINV(ra); PINV(hw); PINV(ha); PINV(so); PINV(sa);
    {
      v4u bt[8];
      if (!live) {
#pragma unroll
        for (int e = 0; e < 8; ++e) asm volatile("" : "=v"(bt[e]));
      } else {
        ST128(SV, WS_OP + dI * 8192 + (w - 8) * 1024 + lane * 16);
        DRAIN();
        lds_set(FL + F_CL + w - 8, tg);
        until(tstart, TD0 + TWD);
        const rsrc_t rw = mk(wd, FF * DM * 2u);
        v2u src[16];
#pragma unroll
        for (int j = 0; j < 16; ++j) src[j] = __builtin_amdgcn_raw_buffer_load_b64(rw, (unsigned)(8 * gq + (j & 3)) * 1024 + (w - 8) * 128 + r16 * 8, (64 * dI + (j >> 3) * 32 + (j & 4)) * 1024, AUX_NT);
        SCHED();
        tr4(src, bt);
        tr4(src + 8, bt + 4);
        if (w >= 12) {
          lds_wait(FL + F_SR + (lane & 7), tg);
#pragma unroll
          for (int j = 0; j < 8; ++j) { sgs += *(const LAS float*)(L + L_SS + j * 512 + lane * 4); sus += *(const LAS float*)(L + L_SS + j * 512 + 256 + lane * 4); }
        }
        PINV(sgs); PINV(sus);
        DRAIN();
        STAMP(5);
      }
      GATHER_H();
      LBAR();
      if (w >= 12) {
        const f32x2 st = ln_stats(*(const LAS v4u*)(L + sa));
        float kg = st.y * -1.4426950408889634f, cg = st.x * sgs, cu = st.x * sus, ku = st.y;
        PINV(kg); PINV(cg); PINV(cu); PINV(ku);
        LBAR();
        float gp[8], up[8];
#pragma unroll
        for (int j = 0; j < 8; ++j) { gp[j] = *(const LAS float*)(L + ra + j * 2048); up[j] = *(const LAS float*)(L + ra + j * 2048 + 256); }
        SCHED();
        const float gg = ((gp[0] + gp[1]) + (gp[2] + gp[3])) + ((gp[4] + gp[5]) + (gp[6] + gp[7]));
        const float uu = ((up[0] + up[1]) + (up[2] + up[3])) + ((up[4] + up[5]) + (up[6] + up[7]));
        const float hh = ku * (uu - cu) * __builtin_amdgcn_rcpf(1.f + __builtin_amdgcn_exp2f(kg * (gg - cg)));
        *(LAS u16*)(L + hw) = (u16)pk(hh, hh);
      } else {
        if (w == 8 && lane + 1 - live == 0) ST64(hint(taglo, taghi), WS_HC + dI * 128);
        LBAR();
      }
      LBAR();
      STAMP(8);
      const v4u a0 = *(const LAS v4u*)(L + ha), a1 = *(const LAS v4u*)(L + ha + 64);
      f32x4 acc[4];
#pragma unroll
      for (int e = 0; e < 4; ++e) acc[e] = mfma(a0, bt[e], Z4);
#pragma unroll
      for (int e = 0; e < 4; ++e) acc[e] = mfma(a1, bt[4 + e], acc[e]);
      const v4u y = {nosent(__float_as_uint(acc[0][0])), nosent(__float_as_uint(acc[1][0])), nosent(__float_as_uint(acc[2][0])), nosent(__float_as_uint(acc[3][0]))};
      if (live) ST128(y, so);
      STAMPE(9);
    }
  }
  }
  PDUMP();
}
}

extern "C" void kernel_launch(void* const* d_in, const int* in_sizes, int n_in, void* d_out, int out_size, void* d_ws, size_t ws_size, hipStream_t stream) {
  (void)in_sizes; (void)n_in; (void)out_size; (void)ws_size;
  fused_decode<<<dim3(256), dim3(1024), 0, stream>>>((const u16*)d_in[0], (const u16*)d_in[1], (const u16*)d_in[2], (const u16*)d_in[3], (const u16*)d_in[4],
                                                     (const u16*)d_in[6], (const u16*)d_in[7], (const u16*)d_in[8], (u16*)d_out, (unsigned char*)d_ws);
}
```

```cpp
#include <hip/hip_runtime.h>
#include <stdint.h>

#define PROF 0
#define TK0 80
#define KPRE 4
#define QDELTA 70
#define TC0 300
#define TD0 220
#define TWD 250
#define DLY2 60
#define DLY3 115
#define DLY5 76

namespace {
typedef unsigned short u16;
typedef unsigned long long u64;
typedef unsigned v4u __attribute__((ext_vector_type(4)));
typedef unsigned v2u __attribute__((ext_vector_type(2)));
typedef int v4i __attribute__((ext_vector_type(4)));
typedef float f32x4 __attribute__((ext_vector_type(4)));
typedef float f32x2 __attribute__((ext_vector_type(2)));
typedef short bf16x8 __attribute__((ext_vector_type(8)));
typedef __bf16 bf16x2 __attribute__((ext_vector_type(2)));
#define GAS __attribute__((address_space(1)))
#define LAS __attribute__((address_space(3)))
typedef __amdgpu_buffer_rsrc_t rsrc_t;

constexpr int NB = 4, DM = 512, HKV = 8, SK = 8192, WIN = 2048, FF = 1792, NQ = 4096;
constexpr int NSPL = 6, NDCU = 28;
constexpr int AUX_NT = 2, AUX_SC1 = 16;
constexpr unsigned SENT = 0xFFFFFFFFu, ONE2 = 0x3F803F80u;
constexpr int WS_EPOCH = 0, WS_HB = 4096, WS_HC = WS_HB + 32 * 128, WS_HA = WS_HC + 32 * 128, WS_QG = WS_HA + 8 * 32 * 128;
constexpr int WS_OREC = WS_QG + 8 * 4 * 4 * 512 * 8, OREC_SZ = 1152;
constexpr int WS_HP = WS_OREC + 8 * 24 * OREC_SZ, WS_OP = WS_HP + 8 * 4 * 512 * 2, WS_END = WS_OP + NDCU * 8192;
constexpr int WS_PROF = 2 << 20;
constexpr int L_FLAG = 0, L_MW = 512, L_HINT = 512, L_QS = 1024, L_OA = L_QS + 1024, L_HH = L_OA + 4096, L_SS = L_HH + 512;
constexpr int L_QRED = L_SS + 4096, L_PART = L_QRED + 5120, L_USED = L_PART + 11 * 2048;
constexpr int L_TOTAL = 84 * 1024;
static_assert(L_USED <= L_TOTAL, "lds");
enum { F_QIS = 0, F_QS = 4, F_QPUB = 12, F_QR = 16, F_CL = 24, F_GO = 32, F_SR = 72, F_MW = 80, F_VL = 96, F_OPF = 112 };
constexpr int CODE_BYTES = 15872;
constexpr unsigned SPIN_G = 1u << 17, SPIN_L = 1u << 21;

__device__ __forceinline__ u64 rt() { return __builtin_amdgcn_s_memrealtime(); }
__device__ __forceinline__ rsrc_t mk(const void* p, unsigned bytes) { return __builtin_amdgcn_make_buffer_rsrc((void*)p, 0, bytes, 0x00020000); }
__device__ __forceinline__ f32x4 mfma(v4u a, v4u b, f32x4 c) { return __builtin_amdgcn_mfma_f32_16x16x32_bf16(__builtin_bit_cast(bf16x8, a), __builtin_bit_cast(bf16x8, b), c, 0, 0, 0); }
__device__ __forceinline__ unsigned pk(float lo, float hi) { return __builtin_bit_cast(unsigned, __builtin_convertvector(f32x2{lo, hi}, bf16x2)); }
__device__ __forceinline__ float blo(unsigned v) { return __builtin_bit_cast(float, v << 16); }
__device__ __forceinline__ float bhi(unsigned v) { return __builtin_bit_cast(float, v & 0xffff0000u); }
__device__ __forceinline__ unsigned plo(unsigned a, unsigned b) { return __builtin_amdgcn_perm(b, a, 0x05040100u); }
__device__ __forceinline__ unsigned phi(unsigned a, unsigned b) { return __builtin_amdgcn_perm(b, a, 0x07060302u); }
__device__ __forceinline__ unsigned nosent(unsigned v) { return v < 0xFFFFFFFEu ? v : 0xFFFFFFFEu; }
template <int C> __device__ __forceinline__ float dpp(float x) { return __builtin_bit_cast(float, __builtin_amdgcn_mov_dpp(__builtin_bit_cast(int, x), C, 0xf, 0xf, true)); }
template <int C> __device__ __forceinline__ unsigned dppu(unsigned x) { return (unsigned)__builtin_amdgcn_mov_dpp((int)x, C, 0xf, 0xf, true); }
constexpr int XOR1 = 0xB1, XOR2 = 0x4E, ROR4 = 0x124, ROR8 = 0x128;
__device__ __forceinline__ float x16_sum(float x) { auto s = __builtin_amdgcn_permlane16_swap(__float_as_uint(x), __float_as_uint(x), false, false); return __uint_as_float(s[0]) + __uint_as_float(s[1]); }
__device__ __forceinline__ float x32_sum(float x) { auto s = __builtin_amdgcn_permlane32_swap(__float_as_uint(x), __float_as_uint(x), false, false); return __uint_as_float(s[0]) + __uint_as_float(s[1]); }
__device__ __forceinline__ float x16_max(float x) { auto s = __builtin_amdgcn_permlane16_swap(__float_as_uint(x), __float_as_uint(x), false, false); return fmaxf(__uint_as_float(s[0]), __uint_as_float(s[1])); }
__device__ __forceinline__ float x32_max(float x) { auto s = __builtin_amdgcn_permlane32_swap(__float_as_uint(x), __float_as_uint(x), false, false); return fmaxf(__uint_as_float(s[0]), __uint_as_float(s[1])); }
__device__ __forceinline__ float row_sum(float x) { x += dpp<XOR1>(x); x += dpp<XOR2>(x); x += dpp<ROR4>(x); x += dpp<ROR8>(x); return x; }
__device__ __forceinline__ float row_max(float x) { x = fmaxf(x, dpp<XOR1>(x)); x = fmaxf(x, dpp<XOR2>(x)); x = fmaxf(x, dpp<ROR4>(x)); x = fmaxf(x, dpp<ROR8>(x)); return x; }
__device__ __forceinline__ float dot2(unsigned a, unsigned b, float c) { return __builtin_amdgcn_fdot2_f32_bf16(__builtin_bit_cast(bf16x2, a), __builtin_bit_cast(bf16x2, b), c, false); }
#define CBAR() asm volatile("" ::: "memory")
#define SCHED() __builtin_amdgcn_sched_barrier(0)
#define DRAIN() do { asm volatile("s_waitcnt vmcnt(0)" ::: "memory"); __builtin_amdgcn_s_waitcnt(0x0F70); } while (0)
#define PINV(x) asm volatile("" : "+v"(x))
#define PINS(x) asm volatile("" : "+s"(x))
#define WGBAR() do { SCHED(); asm volatile("s_waitcnt lgkmcnt(0)" ::: "memory"); __builtin_amdgcn_s_barrier(); CBAR(); SCHED(); } while (0)
#define GSPIN _Pragma("nounroll") for (unsigned sp = 0; sp < SPIN_G; ++sp)
#define LD32(o, so) __builtin_amdgcn_raw_buffer_load_b32(rws, (o), (so), AUX_SC1)
#define LD64(o, so) __builtin_amdgcn_raw_buffer_load_b64(rws, (o), (so), AUX_SC1)
#define LD128(o, so) __builtin_amdgcn_raw_buffer_load_b128(rws, (o), (so), AUX_SC1)
#define ST64(v, o) __builtin_amdgcn_raw_buffer_store_b64((v), rws, (o), 0, AUX_SC1)
#define ST128(v, o) __builtin_amdgcn_raw_buffer_store_b128((v), rws, (o), 0, AUX_SC1)
__device__ __forceinline__ bool okv(v4u r) { return max(max(r.x, r.y), max(r.z, r.w)) != SENT; }
__device__ __forceinline__ void lds_wait(volatile LAS unsigned* f, unsigned tg) {
  SCHED();
#pragma nounroll
  for (unsigned s = 0; s < SPIN_L; ++s) { if (__all(*f == tg)) break; }
  CBAR();
  SCHED();
}
__device__ __forceinline__ void lds_set(volatile LAS unsigned* f, unsigned tg) { CBAR(); *f = tg; CBAR(); }
__device__ __forceinline__ void until(u64 t0, unsigned ticks) { while (rt() - t0 < ticks) __builtin_amdgcn_s_sleep(1); }
__device__ __forceinline__ v2u hint(unsigned taglo, unsigned taghi) { return v2u{taglo | ((unsigned)rt() & 0xFFFFu), taghi}; }
__device__ __forceinline__ bool hint_ok(v2u h, unsigned taglo, unsigned taghi) { return (h.y == taghi) & ((h.x ^ taglo) < 65536u); }
__device__ __forceinline__ void wait_age(unsigned t16, unsigned dly) {
#pragma nounroll
  for (unsigned s = 0; s < 4096u; ++s) { if (__all((((unsigned)rt() - t16) & 0xFFFFu) >= dly)) break; }
}
__device__ __forceinline__ void watch(rsrc_t rws, unsigned ho, unsigned taglo, unsigned taghi, LAS v2u* slot, unsigned tg) {
  GSPIN {
    CBAR();
    const v2u h = LD64(ho, 0);
    const bool ok = hint_ok(h, taglo, taghi);
    if (ok) *slot = v2u{tg, h.x};
    CBAR();
    if (__all(ok)) break;
  }
}
__device__ __forceinline__ void watch8(rsrc_t rws, unsigned ho, unsigned taglo, unsigned taghi, unsigned dly, volatile LAS unsigned* go, unsigned tg, int lane) {
  v2u h;
  unsigned seen = 0, done = 0;
#pragma nounroll
  for (unsigned sp = 0; sp < SPIN_G && done != 0x11111111u; ++sp) {
    if (seen != ~0u) {
      CBAR();
      h = LD64(ho, 0);
      seen = (unsigned)__builtin_amdgcn_ballot_w64(hint_ok(h, taglo, taghi));
    }
    unsigned m = (unsigned)__builtin_amdgcn_ballot_w64((((unsigned)rt() - h.x) & 0xFFFFu) >= dly) & seen;
    m &= m >> 1; m &= m >> 2; m &= 0x11111111u;
    if (m != done) { if ((m >> (4 * (lane & 7))) & 1u) go[lane & 7] = tg; done = m; }
  }
  CBAR();
}
__device__ __forceinline__ u64 load_tag(const unsigned char* ws) {
  u64 e;
  asm volatile("s_load_dwordx2 %0, %1, 0x0\n\ts_waitcnt lgkmcnt(0)" : "=s"(e) : "s"(ws) : "memory");
  return (e + 1ull) & 0xFFFFFFFFFFFFull;
}
__device__ __forceinline__ void code_fetch(int part) {
  u64 pc0;
  asm volatile("s_getpc_b64 %0" : "=s"(pc0));
  const u64 a0 = (pc0 & ~127ull) + (u64)(part * 512 < CODE_BYTES - 512 ? part * 512 : CODE_BYTES - 512);
  unsigned t0, t1, t2, t3, t4, t5, t6, t7;
  asm volatile("s_load_dword %0, %8, 0x0\n\ts_load_dword %1, %8, 0x40\n\ts_load_dword %2, %8, 0x80\n\ts_load_dword %3, %8, 0xc0\n\t"
               "s_load_dword %4, %8, 0x100\n\ts_load_dword %5, %8, 0x140\n\ts_load_dword %6, %8, 0x180\n\ts_load_dword %7, %8, 0x1c0\n\ts_waitcnt lgkmcnt(0)"
               : "=&s"(t0), "=&s"(t1), "=&s"(t2), "=&s"(t3), "=&s"(t4), "=&s"(t5), "=&s"(t6), "=&s"(t7) : "s"(a0) : "memory");
}
__device__ __forceinline__ v4i mkd(const void* p, unsigned bytes) { return v4i{(int)(u64)p, (int)((u64)p >> 32) & 0xFFFF, (int)bytes, 0x00020000}; }
template <int OFF> __device__ __forceinline__ void ldnt(v4u& d, unsigned vo, v4i rs, unsigned so) {
  asm volatile("buffer_load_dwordx4 %0, %1, %2, %3 offen offset:%4 nt" : "=v"(d) : "v"(vo), "s"(rs), "s"(so), "i"(OFF) : "memory");
}
template <int LO, int HI> __device__ __forceinline__ void ldtile(v4u* d, unsigned vo, v4i rs, unsigned so, unsigned so2) {
  if constexpr (LO < HI) { ldnt<(LO & 3) * 1024>(d[LO], vo, rs, LO < 4 ? so : so2); ldtile<LO + 1, HI>(d, vo, rs, so, so2); }
}
__device__ __forceinline__ void ldnt2(v2u& d, unsigned vo, v4i rs, unsigned so) {
  asm volatile("buffer_load_dwordx2 %0, %1, %2, %3 offen nt" : "=v"(d) : "v"(vo), "s"(rs), "s"(so) : "memory");
}
__device__ __forceinline__ void ldp4(v4u& d, unsigned vo, v4i rs) {
  asm volatile("buffer_load_dwordx4 %0, %1, %2, 0 offen" : "=v"(d) : "v"(vo), "s"(rs) : "memory");
}
#define VMWAIT2(n, a, b) asm volatile("s_waitcnt vmcnt(%2)" : "+v"(a), "+v"(b) : "i"(n) : "memory")
__device__ __forceinline__ void ask8(unsigned (&hp)[8], unsigned go, unsigned tg, unsigned ho, v4i rs) {
  unsigned t, done, n, cnt;
#define ASK1(j, off) "s_bitcmp1_b32 %10, " #j "\n\ts_cbranch_scc0 2" #j "f\n\ts_movk_i32 %11, " #off "\n\tbuffer_load_dword %" #j ", %14, %15, %11 offen sc1\n2" #j ":\n\t"
  asm volatile("s_mov_b32 %9, 0\n\ts_mov_b32 %11, 0x200000\n"
               "1:\n\tds_read_b32 %8, %12\n\ts_sub_u32 %11, %11, 1\n\ts_cbranch_scc1 3f\n\ts_waitcnt lgkmcnt(0)\n\t"
               "v_cmp_eq_u32 vcc, %13, %8\n\ts_andn2_b32 %10, vcc_lo, %9\n\ts_and_b32 %10, %10, 0xff\n\ts_cbranch_scc0 1b\n\t"
               ASK1(0, 0) ASK1(1, 0x1000) ASK1(2, 0x2000) ASK1(3, 0x3000) ASK1(4, 0x4000) ASK1(5, 0x5000) ASK1(6, 0x6000) ASK1(7, 0x7000)
               "s_mov_b32 %11, 0x200000\n\ts_or_b32 %9, %9, %10\n\ts_cmp_lg_u32 %9, 0xff\n\ts_cbranch_scc1 1b\n"
               "3:\n\ts_waitcnt vmcnt(0) lgkmcnt(0)"
               : "+v"(hp[0]), "+v"(hp[1]), "+v"(hp[2]), "+v"(hp[3]), "+v"(hp[4]), "+v"(hp[5]), "+v"(hp[6]), "+v"(hp[7]), "=&v"(t), "=&s"(done), "=&s"(n), "=&s"(cnt)
               : "v"(go), "s"(tg), "v"(ho), "s"(rs) : "vcc", "scc", "memory");
#undef ASK1
}
__device__ __forceinline__ f32x2 ln_stats(v4u hv) {
  const float c0 = blo((unsigned)__builtin_amdgcn_readfirstlane(hv[0]));
  float t[8];
#pragma unroll
  for (int d = 0; d < 4; ++d) { t[2 * d] = blo(hv[d]) - c0; t[2 * d + 1] = bhi(hv[d]) - c0; }
  float s1 = ((t[0] + t[1]) + (t[2] + t[3])) + ((t[4] + t[5]) + (t[6] + t[7]));
  float s2 = (fmaf(t[1], t[1], t[0] * t[0]) + fmaf(t[3], t[3], t[2] * t[2])) + (fmaf(t[5], t[5], t[4] * t[4]) + fmaf(t[7], t[7], t[6] * t[6]));
  s1 += dpp<XOR1>(s1); s2 += dpp<XOR1>(s2);
  s1 += dpp<XOR2>(s1); s2 += dpp<XOR2>(s2);
  s1 += dpp<ROR4>(s1); s2 += dpp<ROR4>(s2);
  s1 += dpp<ROR8>(s1); s2 += dpp<ROR8>(s2);
  s1 = x16_sum(s1); s2 = x16_sum(s2);
  s1 = x32_sum(s1); s2 = x32_sum(s2);
  const float d = s1 * (1.f / DM), var = fmaxf(fmaf(-d, d, s2 * (1.f / DM)), 0.f);
  return f32x2{c0 + d, __builtin_amdgcn_rsqf(var + 1e-5f)};
}
#define STAMP(k) do {} while (0)
#define STAMPE(k) do {} while (0)
#define PDUMP() do {} while (0)
#define RETRY() do {} while (0)

__device__ __forceinline__ void tr4(const v2u* s, v4u* o) {
#pragma unroll
  for (int m = 0; m < 4; ++m) {
    o[0][m] = plo(s[2 * m].x, s[2 * m + 1].x); o[1][m] = phi(s[2 * m].x, s[2 * m + 1].x);
    o[2][m] = plo(s[2 * m].y, s[2 * m + 1].y); o[3][m] = phi(s[2 * m].y, s[2 * m + 1].y);
  }
#pragma unroll
  for (int e = 0; e < 4; ++e) PINV(o[e]);
}
__device__ __forceinline__ void tr8(const v4u* s, v4u* o) {
#pragma unroll
  for (int m = 0; m < 4; ++m)
#pragma unroll
    for (int d = 0; d < 4; ++d) { o[2 * d][m] = plo(s[2 * m][d], s[2 * m + 1][d]); o[2 * d + 1][m] = phi(s[2 * m][d], s[2 * m + 1][d]); }
#pragma unroll
  for (int e = 0; e < 8; ++e) PINV(o[e]);
}

__global__ void __launch_bounds__(1024) fused_decode(const u16* __restrict__ x, const u16* __restrict__ kc, const u16* __restrict__ vc,
                                                     const u16* __restrict__ wq, const u16* __restrict__ wo, const u16* __restrict__ wg,
                                                     const u16* __restrict__ wu, const u16* __restrict__ wd, u16* __restrict__ out, unsigned char* ws) {
  const u64 tstart = rt();
  __shared__ __attribute__((aligned(16))) unsigned char smem[L_TOTAL];
  LAS unsigned char* L = (LAS unsigned char*)smem;
  volatile LAS unsigned* FL = (volatile LAS unsigned*)(L + L_FLAG);
  const int lane = threadIdx.x & 63;
  int w = (__builtin_amdgcn_readfirstlane(threadIdx.x >> 6) + 11) & 15;
  const int c = blockIdx.x, g = c & 7, i = c >> 3;
  if (w == 11) code_fetch(i);
  const int gq = lane >> 4, r16 = lane & 15;
  const int b = (i * 43) >> 8, s = i - b * NSPL;
  const int ntile = s < 4 ? 11 : 10, tile0 = 11 * s - (s > 4 ? 1 : 0);
  const int cw = w - 12, rq = i & 3, cq = i >> 2, R0 = 128 * rq + 32 * cw;
  asm volatile("" :: "s"(x), "s"(kc), "s"(vc), "s"(wq), "s"(wo), "s"(wg), "s"(wu), "s"(wd), "s"(out), "s"(ws));
  v4u kf[8], vf[8], xa, xr;
  v2u wsrc[8];
  const unsigned kvbase = ((unsigned)(b * HKV + g) * SK + (SK - WIN) + 32u * tile0 + 16u * w) * 256u, kvbase2 = kvbase + ntile * 4096u;
  const v4i rk = mkd(kc, NB * HKV * SK * 256u), rv = mkd(vc, NB * HKV * SK * 256u);
  if (w >= 12) {
    __builtin_amdgcn_s_setprio(3);
    const v4i rw = mkd(wq, DM * NQ * 2u), rxd = mkd(x, NB * DM * 2u);
    const unsigned o = (unsigned)(8 * gq) * (NQ * 2) + r16 * 8, so = R0 * (NQ * 2) + (512 * g + 64 * cq) * 2;
#pragma unroll
    for (int j = 0; j < 8; ++j) ldnt2(wsrc[j], o, rw, so + j * (NQ * 2));
    ldp4(xa, (r16 >> 2) * 1024 + (R0 + 8 * gq) * 2, rxd);
    ldp4(xr, cw * 1024 + lane * 16, rxd);
  } else if (i < 24 && w < ntile) {
    __builtin_amdgcn_s_setprio(2);
    until(tstart, TK0);
    ldtile<0, KPRE>(kf, r16 * 64 + gq * 16, rk, kvbase, kvbase2);
  }
  STAMP(1);
  PINS(w);
  const rsrc_t rws = mk(ws, WS_END), rx = mk(x, NB * DM * 2u);
  const f32x4 Z4 = {0.f, 0.f, 0.f, 0.f};
  const v4u SV = {SENT, SENT, SENT, SENT};
  const unsigned orec = WS_OREC + (g * 24 + i) * OREC_SZ;

  if (__builtin_expect(w >= 12, 1)) {
    const u64 tag = load_tag(ws);
    const unsigned tg = (unsigned)tag, qhi = (unsigned)(tag >> 8), qlo = (unsigned)(tag << 24);
    asm volatile("s_waitcnt vmcnt(0)" : "+v"(wsrc[0]), "+v"(wsrc[1]), "+v"(wsrc[2]), "+v"(wsrc[3]), "+v"(wsrc[4]), "+v"(wsrc[5]), "+v"(wsrc[6]), "+v"(wsrc[7]), "+v"(xa), "+v"(xr) :: "memory");
    {
      v4u bt[4];
      tr4(wsrc, bt);
      if ((lane & 3) == 1) xa = v4u{ONE2, ONE2, ONE2, ONE2};
      f32x4 qp, cs;
#pragma unroll
      for (int e = 0; e < 4; ++e) { const f32x4 d = mfma(xa, bt[e], Z4); qp[e] = d[0]; cs[e] = d[1]; }
      *(LAS f32x4*)(L + L_QRED + cw * 1024 + lane * 16) = qp;
      if (lane < 16) *(LAS f32x4*)(L + L_QRED + 4096 + cw * 256 + lane * 16) = cs;
    }
    lds_set(FL + F_QR + cw, tg);
    f32x2 xst = ln_stats(xr);
    PINV(xst);
    STAMP(2);
    lds_wait(FL + F_QR + (lane & 3), tg);
    {
      float a[4], cs[4];
#pragma unroll
      for (int j = 0; j < 4; ++j) { a[j] = *(const LAS float*)(L + L_QRED + j * 1024 + cw * 256 + lane * 4); cs[j] = *(const LAS float*)(L + L_QRED + 4096 + j * 256 + lane * 4); }
      const float q = xst.y * (((a[0] + a[1]) + (a[2] + a[3])) - xst.x * ((cs[0] + cs[1]) + (cs[2] + cs[3])));
      ST64((v2u{qlo | ((__float_as_uint(q) + 0x80u) >> 8), qhi}), WS_QG + (((g * NB + cw) * 4 + rq) * 512 + 64 * cq + lane) * 8);
    }
    lds_set(FL + F_QPUB + cw, tg);
    STAMP(3);
    if (i < 24) {
      until(rt(), QDELTA);
      const unsigned qo = WS_QG + ((g * NB + b) * 4 * 512 + 2 * (cw * 64 + lane)) * 8;
      v4u qg[4];
      GSPIN {
        CBAR();
        bool ok = true;
#pragma unroll
        for (int j = 0; j < 4; ++j) qg[j] = LD128(qo, j * 4096);
        SCHED();
        lds_set(FL + F_QIS + cw, tg);
        SCHED();
#pragma unroll
        for (int j = 0; j < 4; ++j) ok &= (qg[j].y == qhi) & (qg[j].w == qhi) & ((qg[j].x ^ qlo) < (1u << 24)) & ((qg[j].z ^ qlo) < (1u << 24));
        if (__all(ok)) break;
        RETRY();
      }
      const float q0 = (__uint_as_float(qg[0].x << 8) + __uint_as_float(qg[1].x << 8)) + (__uint_as_float(qg[2].x << 8) + __uint_as_float(qg[3].x << 8));
      const float q1 = (__uint_as_float(qg[0].z << 8) + __uint_as_float(qg[1].z << 8)) + (__uint_as_float(qg[2].z << 8) + __uint_as_float(qg[3].z << 8));
      *(LAS unsigned*)(L + L_QS + (cw * 64 + lane) * 4) = pk(q0, q1);
      lds_set(FL + F_QS + cw, tg);
      STAMP(4);
      PDUMP();
      return;
    }
    __builtin_amdgcn_s_setprio(0);
  } else if (i < 24) {
    if (w < ntile) {
      const unsigned tg = (unsigned)load_tag(ws);
      lds_wait(FL + F_QPUB + (lane & 3), tg);
      ldtile<KPRE, 8>(kf, r16 * 64 + gq * 16, rk, kvbase, kvbase2);
      __builtin_amdgcn_s_setprio(0);
      lds_wait(FL + F_QIS + (lane & 3), tg);
      ldtile<0, 8>(vf, lane * 16, rv, kvbase, kvbase2);
      STAMP(2);
      lds_wait(FL + (lane < 4 ? F_QS + lane : F_CL), tg);
      asm volatile("s_waitcnt vmcnt(8)" : "+v"(kf[0]), "+v"(kf[1]), "+v"(kf[2]), "+v"(kf[3]), "+v"(kf[4]), "+v"(kf[5]), "+v"(kf[6]), "+v"(kf[7]) :: "memory");
      STAMP(3);
      const v4u qf = *(const LAS v4u*)(L + L_QS + r16 * 64 + gq * 16);
      float sc[8];
#pragma unroll
      for (int t = 0; t < 8; ++t) {
        const f32x4 d = mfma(kf[t], qf, Z4);
        sc[t] = (dpp<0x00>(d[0]) + dpp<0x55>(d[1])) + (dpp<0xAA>(d[2]) + dpp<0xFF>(d[3]));
      }
      float m = fmaxf(fmaxf(fmaxf(sc[0], sc[1]), fmaxf(sc[2], sc[3])), fmaxf(fmaxf(sc[4], sc[5]), fmaxf(sc[6], sc[7])));
      m = x32_max(x16_max(m));
      constexpr float C2 = 0.08838834764831845f * 1.4426950408889634f;
      const float ms = m * C2;
      float p[8], l = 0.f;
#pragma unroll
      for (int t = 0; t < 8; ++t) { p[t] = __builtin_amdgcn_exp2f(fmaf(sc[t], C2, -ms)); l += p[t]; }
      l = x32_sum(x16_sum(l));
      if ((lane & 0x33) == 0) *(LAS f32x2*)(L + L_MW + ((lane >> 2) * 12 + w) * 8) = f32x2{ms, l};
      lds_set(FL + F_MW + w, tg);
      v4u pf = {pk(p[0], p[1]), pk(p[2], p[3]), pk(p[4], p[5]), pk(p[6], p[7])};
      unsigned pa = L_PART + w * 2048 + gq * 512 + r16 * 32;
      PINV(pf); PINV(pa);
      STAMP(4);
      v4u vt[8];
#pragma unroll
      for (int mm = 0; mm < 4; ++mm) {
        if (mm == 0) VMWAIT2(6, vf[0], vf[1]);
        if (mm == 1) VMWAIT2(4, vf[2], vf[3]);
        if (mm == 2) { lds_set(FL + F_VL + w, tg); VMWAIT2(2, vf[4], vf[5]); }
        if (mm == 3) { VMWAIT2(0, vf[6], vf[7]); STAMP(5); }
#pragma unroll
        for (int d = 0; d < 4; ++d) { vt[2 * d][mm] = plo(vf[2 * mm][d], vf[2 * mm + 1][d]); vt[2 * d + 1][mm] = phi(vf[2 * mm][d], vf[2 * mm + 1][d]); }
        if (mm < 3) {
#pragma unroll
          for (int e = 0; e < 8; ++e) PINV(vt[e]);
        }
        SCHED();
      }
      float o[8];
#pragma unroll
      for (int e = 0; e < 8; ++e) o[e] = mfma(pf, vt[e], Z4)[0];
      *(LAS f32x4*)(L + pa) = f32x4{o[0], o[1], o[2], o[3]};
      *(LAS f32x4*)(L + pa + 16) = f32x4{o[4], o[5], o[6], o[7]};
      lds_set(FL + F_OPF + w, tg);
      STAMP(6);
      if (w < 4) {
        const int jl = r16 < ntile ? r16 : 0;
        lds_wait(FL + F_MW + jl, tg);
        const f32x2 ml = *(const LAS f32x2*)(L + L_MW + (w * 12 + jl) * 8);
        const float mg = row_max(ml.x);
        const float wt = r16 < ntile ? __builtin_amdgcn_exp2f(ml.x - mg) : 0.f;
        const float lt = row_sum(wt * ml.y);
        float wj[11];
#pragma unroll
        for (int j = 0; j < 11; ++j) { wj[j] = __builtin_bit_cast(float, __builtin_amdgcn_readlane(__builtin_bit_cast(int, wt), j)); PINS(wj[j]); }
        if (lane == 0) ST64((v2u{nosent(__float_as_uint(mg)), nosent(__float_as_uint(lt))}), orec + 1024 + w * 8);
        unsigned ra = L_PART + w * 512 + lane * 8, rl = ra + (ntile - 1) * 2048, so = orec + w * 256 + lane * 4;
        PINV(ra); PINV(rl); PINV(so);
        lds_wait(FL + F_OPF + jl, tg);
        f32x2 pv[11];
#pragma unroll
        for (int j = 0; j < 10; ++j) pv[j] = *(const LAS f32x2*)(L + ra + j * 2048);
        pv[10] = *(const LAS f32x2*)(L + rl);
        SCHED();
        f32x2 a = pv[0] * wj[0], a2 = pv[1] * wj[1];
#pragma unroll
        for (int j = 2; j < 10; j += 2) { a += pv[j] * wj[j]; a2 += pv[j + 1] * wj[j + 1]; }
        a += pv[10] * wj[10];
        a += a2;
        const unsigned v = nosent(pk(a.x, a.y));
        const v4u o4 = {dppu<0x00>(v), dppu<0x55>(v), dppu<0xAA>(v), dppu<0xFF>(v)};
        if ((lane & 3) == 0) ST128(o4, so);
        STAMP(7);
      }
    } else if (w == 11) {
      __builtin_amdgcn_s_setprio(3);
      ST128(SV, orec + lane * 16);
      if (lane < 8) ST128(SV, orec + 1024 + lane * 16);
      const u64 tag = load_tag(ws);
      const unsigned taghi = (unsigned)(tag >> 16), taglo = (unsigned)(tag << 16), tg = (unsigned)tag;
      const bool isR = i < 8;
      const int ln = g * 8 + i, rb = ln >> 4, pg = lane >> 3, q4 = lane & 7, col = 32 * (ln & 15) + 4 * q4;
      v2u xv = {0u, 0u};
      if (isR) xv = __builtin_amdgcn_raw_buffer_load_b64(rx, (rb * DM + col) * 2, 0, 0);
      DRAIN();
      lds_set(FL + F_CL, tg);
      STAMP(3);
      lds_wait(FL + F_VL + (lane < ntile ? lane : 0), tg);
      if (lane == 0) ST64(hint(taglo, taghi), WS_HA + (g * 32 + i) * 128);
      STAMPE(4);
      if (isR) {
        const rsrc_t ro = mk(out, NB * DM * 2u);
        unsigned pf = __builtin_amdgcn_raw_buffer_load_b32(ro, 0, 0, AUX_SC1);
        asm volatile("" :: "v"(pf));
        v2u hb;
        GSPIN {
          CBAR();
          hb = LD64(WS_HB + ((lane & 7) * 4 + ((ln >> 2) & 3)) * 128, 0);
          if (__all(hint_ok(hb, taglo, taghi))) break;
          __builtin_amdgcn_s_sleep(2);
        }
        STAMP(5);
        wait_age(hb.x, DLY3 + 20);
        v2u hv;
        GSPIN {
          CBAR();
          hv = LD64(WS_HP + (pg * NB + rb) * 1024 + col * 2, 0);
          if (__all(max(hv.x, hv.y) != SENT)) break;
        }
        STAMP(6);
        float h[4] = {blo(hv.x), bhi(hv.x), blo(hv.y), bhi(hv.y)};
#pragma unroll
        for (int d = 0; d < 4; ++d) { h[d] += dpp<ROR8>(h[d]); h[d] = x32_sum(x16_sum(h[d])); }
        {
          const unsigned a0 = pk(h[0], h[1]), a1 = pk(h[2], h[3]);
          const unsigned h0 = pk(blo(xv.x) + blo(a0), bhi(xv.x) + bhi(a0)), h1 = pk(blo(xv.y) + blo(a1), bhi(xv.y) + bhi(a1));
          h[0] = blo(h0); h[1] = bhi(h0); h[2] = blo(h1); h[3] = bhi(h1);
        }
        unsigned po = WS_OP + pg * 8192 + ln * 128 + q4 * 16, po3 = pg < 4 ? po + 24 * 8192 : po, oo = (rb * DM + col) * 2;
        PINV(po); PINV(po3); PINV(oo); PINV(h[0]); PINV(h[1]); PINV(h[2]); PINV(h[3]);
        v2u hc;
        GSPIN {
          CBAR();
          hc = LD64(WS_HC + (lane < NDCU ? lane : 0) * 128, 0);
          if (__all(hint_ok(hc, taglo, taghi))) break;
        }
        wait_age(hc.x, DLY5);
        STAMP(7);
        v4u pv[4];
        GSPIN {
          CBAR();
#pragma unroll
          for (int t = 0; t < 3; ++t) pv[t] = LD128(po, t * 8 * 8192);
          pv[3] = LD128(po3, 0);
          if (__all(okv(pv[0]) & okv(pv[1]) & okv(pv[2]) & okv(pv[3]))) break;
          RETRY();
        }
        STAMP(8);
        float y[4];
#pragma unroll
        for (int d = 0; d < 4; ++d) {
          float t = (__uint_as_float(pv[0][d]) + __uint_as_float(pv[1][d])) + (__uint_as_float(pv[2][d]) + (pg < 4 ? __uint_as_float(pv[3][d]) : 0.f));
          t += dpp<ROR8>(t);
          y[d] = x32_sum(x16_sum(t));
        }
        const unsigned y0 = pk(y[0], y[1]), y1 = pk(y[2], y[3]);
        if (lane < 8) __builtin_amdgcn_raw_buffer_store_b64(v2u{pk(h[0] + blo(y0), h[1] + bhi(y0)), pk(h[2] + blo(y1), h[3] + bhi(y1))}, ro, oo, 0, AUX_SC1);
        if (ln == 0 && lane == 0) ST64((v2u{(unsigned)tag, (unsigned)(tag >> 32)}), WS_EPOCH);
        STAMPE(9);
      }
    }
    PDUMP();
    return;
  }

  const int jq = i - 24;
  const bool isC = jq < 4;
  const int dI = g + 8 * (jq - 4);
  if (!isC && dI >= NDCU) { PDUMP(); return; }
  const u64 tag = load_tag(ws);
  const unsigned taghi = (unsigned)(tag >> 16), taglo = (unsigned)(tag << 16), tg = (unsigned)tag;
#define GATHER_H() do { \
    unsigned klo = 0x00003F80u, khi = 0x3F800000u, ho = WS_HP + w * 256 + lane * 4, oa = L_OA + w * 256 + lane * 4; \
    unsigned hp[8] = {0u, 0u, 0u, 0u, 0u, 0u, 0u, 0u}; \
    float x0 = blo(xp), x1 = bhi(xp); \
    PINS(klo); PINS(khi); PINV(oa); PINV(x0); PINV(x1); \
    ask8(hp, (unsigned)(size_t)(FL + F_GO + (lane & 7)), tg, ho, mkd(ws, WS_END)); \
    if (!__all(max(max(max(hp[0], hp[1]), max(hp[2], hp[3])), max(max(hp[4], hp[5]), max(hp[6], hp[7]))) != SENT)) { \
      GSPIN { \
        RETRY(); \
        CBAR(); \
        _Pragma("unroll") for (int j = 0; j < 8; ++j) hp[j] = LD32(ho, j * 4096); \
        if (__all(max(max(max(hp[0], hp[1]), max(hp[2], hp[3])), max(max(hp[4], hp[5]), max(hp[6], hp[7]))) != SENT)) break; \
      } \
      DRAIN(); \
    } \
    STAMP(7); \
    float a0 = 0.f, a1 = 0.f, a2 = 0.f, a3 = 0.f; \
    _Pragma("unroll") for (int j = 0; j < 4; ++j) { a0 = dot2(hp[j], klo, a0); a1 = dot2(hp[j], khi, a1); a2 = dot2(hp[4 + j], klo, a2); a3 = dot2(hp[4 + j], khi, a3); } \
    const unsigned t0 = pk(a0 + a2, a1 + a3); \
    *(LAS unsigned*)(L + oa) = pk(x0 + blo(t0), x1 + bhi(t0)); \
  } while (0)
  unsigned xp = 0;
  if (!isC) xp = __builtin_amdgcn_raw_buffer_load_b32(rx, w * 256 + lane * 4, 0, 0);
  if (w < 8) {
    const unsigned stride = isC ? DM * 2 : FF * 2;
    const GAS unsigned char* p = isC ? (const GAS unsigned char*)wo + (size_t)(512 * g) * (DM * 2) + (128 * jq + 8 * r16) * 2
                                     : (const GAS unsigned char*)(r16 < 8 ? wg : wu) + (64 * dI + 8 * (r16 & 7)) * 2;
    p += (size_t)(64 * w + 8 * gq) * stride;
    unsigned aa = L_OA + (r16 >> 2) * 1024 + w * 128 + gq * 16, pa = L_PART + w * 2048 + gq * 512 + r16 * 32;
    PINV(aa); PINV(pa);
    v4u src[16], bt[16];
    until(tstart, isC ? TC0 : TD0);
    lds_wait(FL + F_QPUB + (lane & 3), tg);
#pragma unroll
    for (int j = 0; j < 16; ++j) src[j] = __builtin_nontemporal_load((const GAS v4u*)(p + ((j >> 3) * 32 + (j & 7)) * stride));
    SCHED();
    STAMP(4);
    tr8(src, bt);
    tr8(src + 8, bt + 8);
    if (!isC) {
      const v4u ones = {ONE2, ONE2, ONE2, ONE2};
      float cs[8];
#pragma unroll
      for (int e = 0; e < 8; ++e) cs[e] = mfma(ones, bt[8 + e], mfma(ones, bt[e], Z4))[0];
      if (lane < 16) {
        *(LAS f32x4*)(L + L_SS + w * 512 + lane * 32) = f32x4{cs[0], cs[1], cs[2], cs[3]};
        *(LAS f32x4*)(L + L_SS + w * 512 + lane * 32 + 16) = f32x4{cs[4], cs[5], cs[6], cs[7]};
      }
      lds_set(FL + F_SR + w, tg);
    }
    DRAIN();
    STAMP(5);
    if (w == 0) {
      if (isC) {
        const int hl = lane < 24 ? lane : 0;
        watch(rws, WS_HA + (g * 32 + hl) * 128, taglo, taghi, (LAS v2u*)(L + L_HINT + hl * 8), tg);
      } else {
        watch8(rws, WS_HB + (lane & 31) * 128, taglo, taghi, DLY3, FL + F_GO, tg, lane);
      }
      STAMP(10);
      DRAIN();
    } else if (w == 1 && isC) {
      lds_wait(FL + F_SR + (lane & 7), tg);
      if (lane == 0) ST64(hint(taglo, taghi), WS_HB + (g * 4 + jq) * 128);
    }
    if (!isC) GATHER_H();
    WGBAR();
    STAMP(8);
    {
      const v4u a0 = *(const LAS v4u*)(L + aa), a1 = *(const LAS v4u*)(L + aa + 64);
      f32x4 acc[8];
#pragma unroll
      for (int e = 0; e < 8; ++e) acc[e] = mfma(a0, bt[e], Z4);
#pragma unroll
      for (int e = 0; e < 8; ++e) acc[e] = mfma(a1, bt[8 + e], acc[e]);
      *(LAS f32x4*)(L + pa) = f32x4{acc[0][0], acc[1][0], acc[2][0], acc[3][0]};
      *(LAS f32x4*)(L + pa + 16) = f32x4{acc[4][0], acc[5][0], acc[6][0], acc[7][0]};
    }
    STAMP(9);
    WGBAR();
    if (!isC) WGBAR();
  } else if (isC) {
    const int v = w & 7, bb = v >> 1, head = 2 * (v & 1) + (lane >> 5), sg = gq & 1;
    unsigned ra = L_PART + (w & 3) * 512 + lane * 8, so = WS_HP + (g * NB + (w & 3)) * 1024 + jq * 256 + lane * 4;
    unsigned rd = WS_OREC + (g * 24 + bb * NSPL + sg) * OREC_SZ + head * 256 + r16 * 16, rs = WS_OREC + (g * 24 + bb * NSPL + sg) * OREC_SZ + 1024 + head * 8;
    unsigned oa = L_OA + bb * 1024 + head * 256 + r16 * 16;
    PINV(ra); PINV(so); PINV(rd); PINV(rs); PINV(oa);
    v4u od[3];
    v2u st[3];
    if (w == 15) ST128(SV, WS_HP + (g * NB + gq) * 1024 + jq * 256 + r16 * 16);
    DRAIN();
    STAMP(5);
    {
      v2u e;
#pragma nounroll
      for (unsigned sp = 0; sp < SPIN_L; ++sp) { e = *(volatile LAS v2u*)(L + L_HINT + (bb * NSPL + (lane < NSPL ? lane : 0)) * 8); if (__all(e.x == tg)) break; }
      wait_age(e.y, DLY2);
    }
    STAMP(6);
    GSPIN {
      CBAR();
#pragma unroll
      for (int u = 0; u < 3; ++u) st[u] = LD64(rs, 2 * u * OREC_SZ);
      SCHED();
#pragma unroll
      for (int u = 0; u < 3; ++u) od[u] = LD128(rd, 2 * u * OREC_SZ);
      SCHED();
      lds_set(FL + F_SR + v, tg);
      SCHED();
      if (__all(okv(od[0]) & okv(od[1]) & okv(od[2]) & (max(max(max(st[0].x, st[0].y), max(st[1].x, st[1].y)), max(st[2].x, st[2].y)) != SENT))) break;
      RETRY();
    }
    DRAIN();
    STAMP(7);
    const float m0 = __uint_as_float(st[0].x), m1 = __uint_as_float(st[1].x), m2 = __uint_as_float(st[2].x);
    const float mg = x16_max(fmaxf(fmaxf(m0, m1), m2));
    float wt[3] = {__builtin_amdgcn_exp2f(m0 - mg), __builtin_amdgcn_exp2f(m1 - mg), __builtin_amdgcn_exp2f(m2 - mg)};
    const float inv = __builtin_amdgcn_rcpf(x16_sum(wt[0] * __uint_as_float(st[0].y) + wt[1] * __uint_as_float(st[1].y) + wt[2] * __uint_as_float(st[2].y)));
    float a[8];
#pragma unroll
    for (int u = 0; u < 3; ++u) {
#pragma unroll
      for (int d = 0; d < 4; ++d) {
        a[2 * d] = u == 0 ? wt[u] * blo(od[u][d]) : fmaf(wt[u], blo(od[u][d]), a[2 * d]);
        a[2 * d + 1] = u == 0 ? wt[u] * bhi(od[u][d]) : fmaf(wt[u], bhi(od[u][d]), a[2 * d + 1]);
      }
    }
#pragma unroll
    for (int d = 0; d < 8; ++d) a[d] = x16_sum(a[d]) * inv;
    *(LAS v4u*)(L + oa) = v4u{pk(a[0], a[1]), pk(a[2], a[3]), pk(a[4], a[5]), pk(a[6], a[7])};
    STAMP(8);
    WGBAR();
    WGBAR();
    if (w >= 12) {
      f32x2 pv[8];
#pragma unroll
      for (int j = 0; j < 8; ++j) pv[j] = *(const LAS f32x2*)(L + ra + j * 2048);
      SCHED();
      const f32x2 t = ((pv[0] + pv[1]) + (pv[2] + pv[3])) + ((pv[4] + pv[5]) + (pv[6] + pv[7]));
      const unsigned vv = nosent(pk(t.x, t.y));
      const v4u o4 = {dppu<0x00>(vv), dppu<0x55>(vv), dppu<0xAA>(vv), dppu<0xFF>(vv)};
      if ((lane & 3) == 0) ST128(o4, so);
      STAMPE(9);
    }
  } else {
    unsigned ra = L_PART + (w & 3) * 512 + lane * 4, hw = L_HH + (w & 3) * 128 + lane * 2, ha = L_HH + (r16 >> 2) * 128 + gq * 16;
    unsigned so = WS_OP + dI * 8192 + gq * 2048 + (w & 7) * 256 + r16 * 16, sa = L_OA + (w & 3) * 1024 + lane * 16;
    PINV(ra); PINV(hw); PINV(ha); PINV(so); PINV(sa);
    ST128(SV, WS_OP + dI * 8192 + (w - 8) * 1024 + lane * 16);
    until(tstart, TD0 + TWD);
    const rsrc_t rw = mk(wd, FF * DM * 2u);
    v2u src[16];
    v4u bt[8];
#pragma unroll
    for (int j = 0; j < 16; ++j) src[j] = __builtin_amdgcn_raw_buffer_load_b64(rw, (unsigned)(8 * gq + (j & 3)) * 1024 + (w - 8) * 128 + r16 * 8, (64 * dI + (j >> 3) * 32 + (j & 4)) * 1024, AUX_NT);
    SCHED();
    tr4(src, bt);
    tr4(src + 8, bt + 4);
    float sgs = 0.f, sus = 0.f;
    if (w >= 12) {
      lds_wait(FL + F_SR + (lane & 7), tg);
#pragma unroll
      for (int j = 0; j < 8; ++j) { sgs += *(const LAS float*)(L + L_SS + j * 512 + lane * 4); sus += *(const LAS float*)(L + L_SS + j * 512 + 256 + lane * 4); }
    }
    PINV(sgs); PINV(sus);
    DRAIN();
    STAMP(5);
    GATHER_H();
    WGBAR();
    if (w >= 12) {
      const f32x2 st = ln_stats(*(const LAS v4u*)(L + sa));
      float kg = st.y * -1.4426950408889634f, cg = st.x * sgs, cu = st.x * sus, ku = st.y;
      PINV(kg); PINV(cg); PINV(cu); PINV(ku);
      WGBAR();
      float gp[8], up[8];
#pragma unroll
      for (int j = 0; j < 8; ++j) { gp[j] = *(const LAS float*)(L + ra + j * 2048); up[j] = *(const LAS float*)(L + ra + j * 2048 + 256); }
      SCHED();
      const float gg = ((gp[0] + gp[1]) + (gp[2] + gp[3])) + ((gp[4] + gp[5]) + (gp[6] + gp[7]));
      const float uu = ((up[0] + up[1]) + (up[2] + up[3])) + ((up[4] + up[5]) + (up[6] + up[7]));
      const float hh = ku * (uu - cu) * __builtin_amdgcn_rcpf(1.f + __builtin_amdgcn_exp2f(kg * (gg - cg)));
      *(LAS u16*)(L + hw) = (u16)pk(hh, hh);
    } else {
      if (w == 8 && lane == 0) ST64(hint(taglo, taghi), WS_HC + dI * 128);
      WGBAR();
    }
    WGBAR();
    STAMP(8);
    const v4u a0 = *(const LAS v4u*)(L + ha), a1 = *(const LAS v4u*)(L + ha + 64);
    f32x4 acc[4];
#pragma unroll
    for (int e = 0; e < 4; ++e) acc[e] = mfma(a0, bt[e], Z4);
#pragma unroll
    for (int e = 0; e < 4; ++e) acc[e] = mfma(a1, bt[4 + e], acc[e]);
    ST128((v4u{nosent(__float_as_uint(acc[0][0])), nosent(__float_as_uint(acc[1][0])), nosent(__float_as_uint(acc[2][0])), nosent(__float_as_uint(acc[3][0]))}), so);
    STAMPE(9);
  }
  PDUMP();
}
}

extern "C" void kernel_launch(void* const* d_in, const int* in_sizes, int n_in, void* d_out, int out_size, void* d_ws, size_t ws_size, hipStream_t stream) {
  (void)in_sizes; (void)n_in; (void)out_size; (void)ws_size;
  fused_decode<<<dim3(256), dim3(1024), 0, stream>>>((const u16*)d_in[0], (const u16*)d_in[1], (const u16*)d_in[2], (const u16*)d_in[3], (const u16*)d_in[4],
                                                     (const u16*)d_in[6], (const u16*)d_in[7], (const u16*)d_in[8], (u16*)d_out, (unsigned char*)d_ws);
}
```

```cpp
#include <hip/hip_runtime.h>
#include <stdint.h>

#define PROF 0
#define TK0 80
#define KPRE 4
#define QDELTA 70
#define TC0 300
#define TD0 220
#define WPRE 0
#define TWD 470
#define DLY2 60
#define DLY3 90
#define DLY5 76

namespace {
typedef unsigned short u16;
typedef unsigned long long u64;
typedef unsigned v4u __attribute__((ext_vector_type(4)));
typedef unsigned v2u __attribute__((ext_vector_type(2)));
typedef int v4i __attribute__((ext_vector_type(4)));
typedef float f32x4 __attribute__((ext_vector_type(4)));
typedef float f32x2 __attribute__((ext_vector_type(2)));
typedef short bf16x8 __attribute__((ext_vector_type(8)));
typedef __bf16 bf16x2 __attribute__((ext_vector_type(2)));
#define GAS __attribute__((address_space(1)))
#define LAS __attribute__((address_space(3)))
typedef __amdgpu_buffer_rsrc_t rsrc_t;

constexpr int NB = 4, DM = 512, HKV = 8, SK = 8192, WIN = 2048, FF = 1792, NQ = 4096;
constexpr int NSPL = 6, NDCU = 28;
constexpr int AUX_NT = 2, AUX_SC1 = 16;
constexpr unsigned SENT = 0xFFFFFFFFu, ONE2 = 0x3F803F80u;
constexpr int WS_EPOCH = 0, WS_HB = 4096, WS_HC = WS_HB + 32 * 128, WS_HA = WS_HC + 32 * 128, WS_QG = WS_HA + 8 * 32 * 128;
constexpr int WS_OREC = WS_QG + 8 * 4 * 4 * 512 * 8, OREC_SZ = 1152;
constexpr int WS_HP = WS_OREC + 8 * 24 * OREC_SZ, WS_OP = WS_HP + 8 * 4 * 512 * 2, WS_END = WS_OP + NDCU * 8192;
constexpr int WS_PROF = 2 << 20;
constexpr int L_FLAG = 0, L_MW = 512, L_HINT = 512, L_QS = 1024, L_OA = L_QS + 1024, L_HH = L_OA + 4096, L_SS = L_HH + 512;
constexpr int L_QRED = L_SS + 4096, L_PART = L_QRED + 5120, L_USED = L_PART + 11 * 2048;
constexpr int L_TOTAL = 84 * 1024;
static_assert(L_USED <= L_TOTAL, "lds");
enum { F_QIS = 0, F_QS = 4, F_QPUB = 12, F_QR = 16, F_CL = 24, F_GO = 32, F_SR = 72, F_MW = 80, F_VL = 96, F_OPF = 112 };
constexpr int CODE_BYTES = 15360;
constexpr unsigned SPIN_G = 1u << 17, SPIN_L = 1u << 21;

__device__ __forceinline__ u64 rt() { return __builtin_amdgcn_s_memrealtime(); }
__device__ __forceinline__ rsrc_t mk(const void* p, unsigned bytes) { return __builtin_amdgcn_make_buffer_rsrc((void*)p, 0, bytes, 0x00020000); }
__device__ __forceinline__ f32x4 mfma(v4u a, v4u b, f32x4 c) { return __builtin_amdgcn_mfma_f32_16x16x32_bf16(__builtin_bit_cast(bf16x8, a), __builtin_bit_cast(bf16x8, b), c, 0, 0, 0); }
__device__ __forceinline__ unsigned pk(float lo, float hi) { return __builtin_bit_cast(unsigned, __builtin_convertvector(f32x2{lo, hi}, bf16x2)); }
__device__ __forceinline__ float blo(unsigned v) { return __builtin_bit_cast(float, v << 16); }
__device__ __forceinline__ float bhi(unsigned v) { return __builtin_bit_cast(float, v & 0xffff0000u); }
__device__ __forceinline__ unsigned plo(unsigned a, unsigned b) { return __builtin_amdgcn_perm(b, a, 0x05040100u); }
__device__ __forceinline__ unsigned phi(unsigned a, unsigned b) { return __builtin_amdgcn_perm(b, a, 0x07060302u); }
__device__ __forceinline__ unsigned nosent(unsigned v) { return v < 0xFFFFFFFEu ? v : 0xFFFFFFFEu; }
template <int C> __device__ __forceinline__ float dpp(float x) { return __builtin_bit_cast(float, __builtin_amdgcn_mov_dpp(__builtin_bit_cast(int, x), C, 0xf, 0xf, true)); }
template <int C> __device__ __forceinline__ unsigned dppu(unsigned x) { return (unsigned)__builtin_amdgcn_mov_dpp((int)x, C, 0xf, 0xf, true); }
constexpr int XOR1 = 0xB1, XOR2 = 0x4E, ROR4 = 0x124, ROR8 = 0x128;
__device__ __forceinline__ float x16_sum(float x) { auto s = __builtin_amdgcn_permlane16_swap(__float_as_uint(x), __float_as_uint(x), false, false); return __uint_as_float(s[0]) + __uint_as_float(s[1]); }
__device__ __forceinline__ float x32_sum(float x) { auto s = __builtin_amdgcn_permlane32_swap(__float_as_uint(x), __float_as_uint(x), false, false); return __uint_as_float(s[0]) + __uint_as_float(s[1]); }
__device__ __forceinline__ float x16_max(float x) { auto s = __builtin_amdgcn_permlane16_swap(__float_as_uint(x), __float_as_uint(x), false, false); return fmaxf(__uint_as_float(s[0]), __uint_as_float(s[1])); }
__device__ __forceinline__ float x32_max(float x) { auto s = __builtin_amdgcn_permlane32_swap(__float_as_uint(x), __float_as_uint(x), false, false); return fmaxf(__uint_as_float(s[0]), __uint_as_float(s[1])); }
__device__ __forceinline__ float row_sum(float x) { x += dpp<XOR1>(x); x += dpp<XOR2>(x); x += dpp<ROR4>(x); x += dpp<ROR8>(x); return x; }
__device__ __forceinline__ float row_max(float x) { x = fmaxf(x, dpp<XOR1>(x)); x = fmaxf(x, dpp<XOR2>(x)); x = fmaxf(x, dpp<ROR4>(x)); x = fmaxf(x, dpp<ROR8>(x)); return x; }
__device__ __forceinline__ float dot2(unsigned a, unsigned b, float c) { return __builtin_amdgcn_fdot2_f32_bf16(__builtin_bit_cast(bf16x2, a), __builtin_bit_cast(bf16x2, b), c, false); }
#define CBAR() asm volatile("" ::: "memory")
#define SCHED() __builtin_amdgcn_sched_barrier(0)
#define DRAIN() do { asm volatile("s_waitcnt vmcnt(0)" ::: "memory"); __builtin_amdgcn_s_waitcnt(0x0F70); } while (0)
#define PINV(x) asm volatile("" : "+v"(x))
#define PINS(x) asm volatile("" : "+s"(x))
#define WGBAR() do { SCHED(); asm volatile("s_waitcnt lgkmcnt(0)" ::: "memory"); __builtin_amdgcn_s_barrier(); CBAR(); SCHED(); } while (0)
#define GSPIN _Pragma("nounroll") for (unsigned sp = 0; sp < SPIN_G; ++sp)
#define LD32(o, so) __builtin_amdgcn_raw_buffer_load_b32(rws, (o), (so), AUX_SC1)
#define LD64(o, so) __builtin_amdgcn_raw_buffer_load_b64(rws, (o), (so), AUX_SC1)
#define LD128(o, so) __builtin_amdgcn_raw_buffer_load_b128(rws, (o), (so), AUX_SC1)
#define ST64(v, o) __builtin_amdgcn_raw_buffer_store_b64((v), rws, (o), 0, AUX_SC1)
#define ST128(v, o) __builtin_amdgcn_raw_buffer_store_b128((v), rws, (o), 0, AUX_SC1)
__device__ __forceinline__ bool okv(v4u r) { return max(max(r.x, r.y), max(r.z, r.w)) != SENT; }
__device__ __forceinline__ void lds_wait(volatile LAS unsigned* f, unsigned tg) {
  SCHED();
#pragma nounroll
  for (unsigned s = 0; s < SPIN_L; ++s) { if (__all(*f == tg)) break; }
  CBAR();
  SCHED();
}
__device__ __forceinline__ void lds_set(volatile LAS unsigned* f, unsigned tg) { CBAR(); *f = tg; CBAR(); }
__device__ __forceinline__ void until(u64 t0, unsigned ticks) { while (rt() - t0 < ticks) __builtin_amdgcn_s_sleep(1); }
__device__ __forceinline__ v2u hint(unsigned taglo, unsigned taghi) { return v2u{taglo | ((unsigned)rt() & 0xFFFFu), taghi}; }
__device__ __forceinline__ bool hint_ok(v2u h, unsigned taglo, unsigned taghi) { return (h.y == taghi) & ((h.x ^ taglo) < 65536u); }
__device__ __forceinline__ void wait_age(unsigned t16, unsigned dly) {
#pragma nounroll
  for (unsigned s = 0; s < 4096u; ++s) { if (__all((((unsigned)rt() - t16) & 0xFFFFu) >= dly)) break; }
}
__device__ __forceinline__ void watch(rsrc_t rws, unsigned ho, unsigned taglo, unsigned taghi, LAS v2u* slot, unsigned tg) {
  GSPIN {
    CBAR();
    const v2u h = LD64(ho, 0);
    const bool ok = hint_ok(h, taglo, taghi);
    if (ok) *slot = v2u{tg, h.x};
    CBAR();
    if (__all(ok)) break;
  }
}
__device__ __forceinline__ void watch8(rsrc_t rws, unsigned ho, unsigned taglo, unsigned taghi, unsigned dly, volatile LAS unsigned* go, unsigned tg, int lane) {
  v2u h;
  unsigned seen = 0, done = 0;
#pragma nounroll
  for (unsigned sp = 0; sp < SPIN_G && done != 0x11111111u; ++sp) {
    if (seen != ~0u) {
      CBAR();
      h = LD64(ho, 0);
      seen = (unsigned)__builtin_amdgcn_ballot_w64(hint_ok(h, taglo, taghi));
    }
    unsigned m = (unsigned)__builtin_amdgcn_ballot_w64((((unsigned)rt() - h.x) & 0xFFFFu) >= dly) & seen;
    m &= m >> 1; m &= m >> 2; m &= 0x11111111u;
    if (m != done) { if ((m >> (4 * (lane & 7))) & 1u) go[lane & 7] = tg; done = m; }
  }
  CBAR();
}
__device__ __forceinline__ u64 load_tag(const unsigned char* ws) {
  u64 e;
  asm volatile("s_load_dwordx2 %0, %1, 0x0\n\ts_waitcnt lgkmcnt(0)" : "=s"(e) : "s"(ws) : "memory");
  return (e + 1ull) & 0xFFFFFFFFFFFFull;
}
__device__ __forceinline__ void code_fetch(int part) {
  u64 pc0;
  asm volatile("s_getpc_b64 %0" : "=s"(pc0));
  const u64 a0 = (pc0 & ~127ull) + (u64)(part * 512 < CODE_BYTES - 512 ? part * 512 : CODE_BYTES - 512);
  unsigned t0, t1, t2, t3, t4, t5, t6, t7;
  asm volatile("s_load_dword %0, %8, 0x0\n\ts_load_dword %1, %8, 0x40\n\ts_load_dword %2, %8, 0x80\n\ts_load_dword %3, %8, 0xc0\n\t"
               "s_load_dword %4, %8, 0x100\n\ts_load_dword %5, %8, 0x140\n\ts_load_dword %6, %8, 0x180\n\ts_load_dword %7, %8, 0x1c0\n\ts_waitcnt lgkmcnt(0)"
               : "=&s"(t0), "=&s"(t1), "=&s"(t2), "=&s"(t3), "=&s"(t4), "=&s"(t5), "=&s"(t6), "=&s"(t7) : "s"(a0) : "memory");
}
__device__ __forceinline__ v4i mkd(const void* p, unsigned bytes) { return v4i{(int)(u64)p, (int)((u64)p >> 32) & 0xFFFF, (int)bytes, 0x00020000}; }
template <int OFF> __device__ __forceinline__ void ldnt(v4u& d, unsigned vo, v4i rs, unsigned so) {
  asm volatile("buffer_load_dwordx4 %0, %1, %2, %3 offen offset:%4 nt" : "=v"(d) : "v"(vo), "s"(rs), "s"(so), "i"(OFF) : "memory");
}
template <int LO, int HI> __device__ __forceinline__ void ldtile(v4u* d, unsigned vo, v4i rs, unsigned so, unsigned so2) {
  if constexpr (LO < HI) { ldnt<(LO & 3) * 1024>(d[LO], vo, rs, LO < 4 ? so : so2); ldtile<LO + 1, HI>(d, vo, rs, so, so2); }
}
__device__ __forceinline__ void ldnt2(v2u& d, unsigned vo, v4i rs, unsigned so) {
  asm volatile("buffer_load_dwordx2 %0, %1, %2, %3 offen nt" : "=v"(d) : "v"(vo), "s"(rs), "s"(so) : "memory");
}
__device__ __forceinline__ void ldp4(v4u& d, unsigned vo, v4i rs) {
  asm volatile("buffer_load_dwordx4 %0, %1, %2, 0 offen" : "=v"(d) : "v"(vo), "s"(rs) : "memory");
}
#define VMWAIT2(n, a, b) asm volatile("s_waitcnt vmcnt(%2)" : "+v"(a), "+v"(b) : "i"(n) : "memory")
__device__ __forceinline__ void ask8(unsigned (&hp)[8], unsigned go, unsigned tg, unsigned ho, v4i rs) {
  unsigned t, done, n, cnt;
#define ASK1(j, off) "s_bitcmp1_b32 %10, " #j "\n\ts_cbranch_scc0 2" #j "f\n\ts_movk_i32 %11, " #off "\n\tbuffer_load_dword %" #j ", %14, %15, %11 offen sc1\n2" #j ":\n\t"
  asm volatile("s_mov_b32 %9, 0\n\ts_mov_b32 %11, 0x200000\n"
               "1:\n\tds_read_b32 %8, %12\n\ts_sub_u32 %11, %11, 1\n\ts_cbranch_scc1 3f\n\ts_waitcnt lgkmcnt(0)\n\t"
               "v_cmp_eq_u32 vcc, %13, %8\n\ts_andn2_b32 %10, vcc_lo, %9\n\ts_and_b32 %10, %10, 0xff\n\ts_cbranch_scc0 1b\n\t"
               ASK1(0, 0) ASK1(1, 0x1000) ASK1(2, 0x2000) ASK1(3, 0x3000) ASK1(4, 0x4000) ASK1(5, 0x5000) ASK1(6, 0x6000) ASK1(7, 0x7000)
               "s_mov_b32 %11, 0x200000\n\ts_or_b32 %9, %9, %10\n\ts_cmp_lg_u32 %9, 0xff\n\ts_cbranch_scc1 1b\n"
               "3:\n\ts_waitcnt vmcnt(0) lgkmcnt(0)"
               : "+v"(hp[0]), "+v"(hp[1]), "+v"(hp[2]), "+v"(hp[3]), "+v"(hp[4]), "+v"(hp[5]), "+v"(hp[6]), "+v"(hp[7]), "=&v"(t), "=&s"(done), "=&s"(n), "=&s"(cnt)
               : "v"(go), "s"(tg), "v"(ho), "s"(rs) : "vcc", "scc", "memory");
#undef ASK1
}
__device__ __forceinline__ f32x2 ln_stats(v4u hv) {
  const float c0 = blo((unsigned)__builtin_amdgcn_readfirstlane(hv[0]));
  float t[8];
#pragma unroll
  for (int d = 0; d < 4; ++d) { t[2 * d] = blo(hv[d]) - c0; t[2 * d + 1] = bhi(hv[d]) - c0; }
  float s1 = ((t[0] + t[1]) + (t[2] + t[3])) + ((t[4] + t[5]) + (t[6] + t[7]));
  float s2 = (fmaf(t[1], t[1], t[0] * t[0]) + fmaf(t[3], t[3], t[2] * t[2])) + (fmaf(t[5], t[5], t[4] * t[4]) + fmaf(t[7], t[7], t[6] * t[6]));
  s1 += dpp<XOR1>(s1); s2 += dpp<XOR1>(s2);
  s1 += dpp<XOR2>(s1); s2 += dpp<XOR2>(s2);
  s1 += dpp<ROR4>(s1); s2 += dpp<ROR4>(s2);
  s1 += dpp<ROR8>(s1); s2 += dpp<ROR8>(s2);
  s1 = x16_sum(s1); s2 = x16_sum(s2);
  s1 = x32_sum(s1); s2 = x32_sum(s2);
  const float d = s1 * (1.f / DM), var = fmaxf(fmaf(-d, d, s2 * (1.f / DM)), 0.f);
  return f32x2{c0 + d, __builtin_amdgcn_rsqf(var + 1e-5f)};
}
#define STAMP(k) do {} while (0)
#define STAMPE(k) do {} while (0)
#define PDUMP() do {} while (0)
#define RETRY() do {} while (0)

__device__ __forceinline__ void tr4(const v2u* s, v4u* o) {
#pragma unroll
  for (int m = 0; m < 4; ++m) {
    o[0][m] = plo(s[2 * m].x, s[2 * m + 1].x); o[1][m] = phi(s[2 * m].x, s[2 * m + 1].x);
    o[2][m] = plo(s[2 * m].y, s[2 * m + 1].y); o[3][m] = phi(s[2 * m].y, s[2 * m + 1].y);
  }
#pragma unroll
  for (int e = 0; e < 4; ++e) PINV(o[e]);
}
__device__ __forceinline__ void tr8(const v4u* s, v4u* o) {
#pragma unroll
  for (int m = 0; m < 4; ++m)
#pragma unroll
    for (int d = 0; d < 4; ++d) { o[2 * d][m] = plo(s[2 * m][d], s[2 * m + 1][d]); o[2 * d + 1][m] = phi(s[2 * m][d], s[2 * m + 1][d]); }
#pragma unroll
  for (int e = 0; e < 8; ++e) PINV(o[e]);
}

__global__ void __launch_bounds__(1024) fused_decode(const u16* __restrict__ x, const u16* __restrict__ kc, const u16* __restrict__ vc,
                                                     const u16* __restrict__ wq, const u16* __restrict__ wo, const u16* __restrict__ wg,
                                                     const u16* __restrict__ wu, const u16* __restrict__ wd, u16* __restrict__ out, unsigned char* ws) {
  const u64 tstart = rt();
  __shared__ __attribute__((aligned(16))) unsigned char smem[L_TOTAL];
  LAS unsigned char* L = (LAS unsigned char*)smem;
  volatile LAS unsigned* FL = (volatile LAS unsigned*)(L + L_FLAG);
  const int lane = threadIdx.x & 63;
  int w = (__builtin_amdgcn_readfirstlane(threadIdx.x >> 6) + 11) & 15;
  const int c = blockIdx.x, g = c & 7, i = c >> 3;
  if (w == 11) code_fetch(i);
  const int gq = lane >> 4, r16 = lane & 15;
  const int b = (i * 43) >> 8, s = i - b * NSPL;
  const int ntile = s < 4 ? 11 : 10, tile0 = 11 * s - (s > 4 ? 1 : 0);
  const int cw = w - 12, rq = i & 3, cq = i >> 2, R0 = 128 * rq + 32 * cw;
  asm volatile("" :: "s"(x), "s"(kc), "s"(vc), "s"(wq), "s"(wo), "s"(wg), "s"(wu), "s"(wd), "s"(out), "s"(ws));
  v4u kf[8], vf[8], xa, xr;
  v2u wsrc[8];
  const unsigned kvbase = ((unsigned)(b * HKV + g) * SK + (SK - WIN) + 32u * tile0 + 16u * w) * 256u, kvbase2 = kvbase + ntile * 4096u;
  const v4i rk = mkd(kc, NB * HKV * SK * 256u), rv = mkd(vc, NB * HKV * SK * 256u);
  if (w >= 12) {
    __builtin_amdgcn_s_setprio(3);
    const v4i rw = mkd(wq, DM * NQ * 2u), rxd = mkd(x, NB * DM * 2u);
    const unsigned o = (unsigned)(8 * gq) * (NQ * 2) + r16 * 8, so = R0 * (NQ * 2) + (512 * g + 64 * cq) * 2;
#pragma unroll
    for (int j = 0; j < 8; ++j) ldnt2(wsrc[j], o, rw, so + j * (NQ * 2));
    ldp4(xa, (r16 >> 2) * 1024 + (R0 + 8 * gq) * 2, rxd);
    ldp4(xr, cw * 1024 + lane * 16, rxd);
  } else if (i < 24 && w < ntile) {
    __builtin_amdgcn_s_setprio(2);
    until(tstart, TK0);
    ldtile<0, KPRE>(kf, r16 * 64 + gq * 16, rk, kvbase, kvbase2);
  }
  STAMP(1);
  PINS(w);
  const rsrc_t rws = mk(ws, WS_END), rx = mk(x, NB * DM * 2u);
  const f32x4 Z4 = {0.f, 0.f, 0.f, 0.f};
  const v4u SV = {SENT, SENT, SENT, SENT};
  const unsigned orec = WS_OREC + (g * 24 + i) * OREC_SZ;

  if (__builtin_expect(w >= 12, 1)) {
    const u64 tag = load_tag(ws);
    const unsigned tg = (unsigned)tag, qhi = (unsigned)(tag >> 8), qlo = (unsigned)(tag << 24);
    asm volatile("s_waitcnt vmcnt(0)" : "+v"(wsrc[0]), "+v"(wsrc[1]), "+v"(wsrc[2]), "+v"(wsrc[3]), "+v"(wsrc[4]), "+v"(wsrc[5]), "+v"(wsrc[6]), "+v"(wsrc[7]), "+v"(xa), "+v"(xr) :: "memory");
    {
      v4u bt[4];
      tr4(wsrc, bt);
      if ((lane & 3) == 1) xa = v4u{ONE2, ONE2, ONE2, ONE2};
      f32x4 qp, cs;
#pragma unroll
      for (int e = 0; e < 4; ++e) { const f32x4 d = mfma(xa, bt[e], Z4); qp[e] = d[0]; cs[e] = d[1]; }
      *(LAS f32x4*)(L + L_QRED + cw * 1024 + lane * 16) = qp;
      if (lane < 16) *(LAS f32x4*)(L + L_QRED + 4096 + cw * 256 + lane * 16) = cs;
    }
    lds_set(FL + F_QR + cw, tg);
    f32x2 xst = ln_stats(xr);
    PINV(xst);
    STAMP(2);
    lds_wait(FL + F_QR + (lane & 3), tg);
    {
      float a[4], cs[4];
#pragma unroll
      for (int j = 0; j < 4; ++j) { a[j] = *(const LAS float*)(L + L_QRED + j * 1024 + cw * 256 + lane * 4); cs[j] = *(const LAS float*)(L + L_QRED + 4096 + j * 256 + lane * 4); }
      const float q = xst.y * (((a[0] + a[1]) + (a[2] + a[3])) - xst.x * ((cs[0] + cs[1]) + (cs[2] + cs[3])));
      ST64((v2u{qlo | ((__float_as_uint(q) + 0x80u) >> 8), qhi}), WS_QG + (((g * NB + cw) * 4 + rq) * 512 + 64 * cq + lane) * 8);
    }
    lds_set(FL + F_QPUB + cw, tg);
    STAMP(3);
    if (i < 24) {
      until(rt(), QDELTA);
      const unsigned qo = WS_QG + ((g * NB + b) * 4 * 512 + 2 * (cw * 64 + lane)) * 8;
      v4u qg[4];
      GSPIN {
        CBAR();
        bool ok = true;
#pragma unroll
        for (int j = 0; j < 4; ++j) qg[j] = LD128(qo, j * 4096);
        SCHED();
        lds_set(FL + F_QIS + cw, tg);
        SCHED();
#pragma unroll
        for (int j = 0; j < 4; ++j) ok &= (qg[j].y == qhi) & (qg[j].w == qhi) & ((qg[j].x ^ qlo) < (1u << 24)) & ((qg[j].z ^ qlo) < (1u << 24));
        if (__all(ok)) break;
        RETRY();
      }
      const float q0 = (__uint_as_float(qg[0].x << 8) + __uint_as_float(qg[1].x << 8)) + (__uint_as_float(qg[2].x << 8) + __uint_as_float(qg[3].x << 8));
      const float q1 = (__uint_as_float(qg[0].z << 8) + __uint_as_float(qg[1].z << 8)) + (__uint_as_float(qg[2].z << 8) + __uint_as_float(qg[3].z << 8));
      *(LAS unsigned*)(L + L_QS + (cw * 64 + lane) * 4) = pk(q0, q1);
      lds_set(FL + F_QS + cw, tg);
      STAMP(4);
      PDUMP();
      return;
    }
    __builtin_amdgcn_s_setprio(0);
  } else if (i < 24) {
    if (w < ntile) {
      const unsigned tg = (unsigned)load_tag(ws);
      lds_wait(FL + F_QPUB + (lane & 3), tg);
      ldtile<KPRE, 8>(kf, r16 * 64 + gq * 16, rk, kvbase, kvbase2);
      __builtin_amdgcn_s_setprio(0);
      lds_wait(FL + F_QIS + (lane & 3), tg);
      ldtile<0, 8>(vf, lane * 16, rv, kvbase, kvbase2);
      STAMP(2);
      lds_wait(FL + (lane < 4 ? F_QS + lane : F_CL), tg);
      asm volatile("s_waitcnt vmcnt(8)" : "+v"(kf[0]), "+v"(kf[1]), "+v"(kf[2]), "+v"(kf[3]), "+v"(kf[4]), "+v"(kf[5]), "+v"(kf[6]), "+v"(kf[7]) :: "memory");
      STAMP(3);
      const v4u qf = *(const LAS v4u*)(L + L_QS + r16 * 64 + gq * 16);
      float sc[8];
#pragma unroll
      for (int t = 0; t < 8; ++t) {
        const f32x4 d = mfma(kf[t], qf, Z4);
        sc[t] = (dpp<0x00>(d[0]) + dpp<0x55>(d[1])) + (dpp<0xAA>(d[2]) + dpp<0xFF>(d[3]));
      }
      float m = fmaxf(fmaxf(fmaxf(sc[0], sc[1]), fmaxf(sc[2], sc[3])), fmaxf(fmaxf(sc[4], sc[5]), fmaxf(sc[6], sc[7])));
      m = x32_max(x16_max(m));
      constexpr float C2 = 0.08838834764831845f * 1.4426950408889634f;
      const float ms = m * C2;
      float p[8], l = 0.f;
#pragma unroll
      for (int t = 0; t < 8; ++t) { p[t] = __builtin_amdgcn_exp2f(fmaf(sc[t], C2, -ms)); l += p[t]; }
      l = x32_sum(x16_sum(l));
      if ((lane & 0x33) == 0) *(LAS f32x2*)(L + L_MW + ((lane >> 2) * 12 + w) * 8) = f32x2{ms, l};
      lds_set(FL + F_MW + w, tg);
      v4u pf = {pk(p[0], p[1]), pk(p[2], p[3]), pk(p[4], p[5]), pk(p[6], p[7])};
      unsigned pa = L_PART + w * 2048 + gq * 512 + r16 * 32;
      PINV(pf); PINV(pa);
      STAMP(4);
      v4u vt[8];
#pragma unroll
      for (int mm = 0; mm < 4; ++mm) {
        if (mm == 0) VMWAIT2(6, vf[0], vf[1]);
        if (mm == 1) VMWAIT2(4, vf[2], vf[3]);
        if (mm == 2) { lds_set(FL + F_VL + w, tg); VMWAIT2(2, vf[4], vf[5]); }
        if (mm == 3) { VMWAIT2(0, vf[6], vf[7]); STAMP(5); }
#pragma unroll
        for (int d = 0; d < 4; ++d) { vt[2 * d][mm] = plo(vf[2 * mm][d], vf[2 * mm + 1][d]); vt[2 * d + 1][mm] = phi(vf[2 * mm][d], vf[2 * mm + 1][d]); }
        if (mm < 3) {
#pragma unroll
          for (int e = 0; e < 8; ++e) PINV(vt[e]);
        }
        SCHED();
      }
      float o[8];
#pragma unroll
      for (int e = 0; e < 8; ++e) o[e] = mfma(pf, vt[e], Z4)[0];
      *(LAS f32x4*)(L + pa) = f32x4{o[0], o[1], o[2], o[3]};
      *(LAS f32x4*)(L + pa + 16) = f32x4{o[4], o[5], o[6], o[7]};
      lds_set(FL + F_OPF + w, tg);
      STAMP(6);
      if (w < 4) {
        const int jl = r16 < ntile ? r16 : 0;
        lds_wait(FL + F_MW + jl, tg);
        const f32x2 ml = *(const LAS f32x2*)(L + L_MW + (w * 12 + jl) * 8);
        const float mg = row_max(ml.x);
        const float wt = r16 < ntile ? __builtin_amdgcn_exp2f(ml.x - mg) : 0.f;
        const float lt = row_sum(wt * ml.y);
        float wj[11];
#pragma unroll
        for (int j = 0; j < 11; ++j) { wj[j] = __builtin_bit_cast(float, __builtin_amdgcn_readlane(__builtin_bit_cast(int, wt), j)); PINS(wj[j]); }
        if (lane == 0) ST64((v2u{nosent(__float_as_uint(mg)), nosent(__float_as_uint(lt))}), orec + 1024 + w * 8);
        unsigned ra = L_PART + w * 512 + lane * 8, rl = ra + (ntile - 1) * 2048, so = orec + w * 256 + lane * 4;
        PINV(ra); PINV(rl); PINV(so);
        lds_wait(FL + F_OPF + jl, tg);
        f32x2 pv[11];
#pragma unroll
        for (int j = 0; j < 10; ++j) pv[j] = *(const LAS f32x2*)(L + ra + j * 2048);
        pv[10] = *(const LAS f32x2*)(L + rl);
        SCHED();
        f32x2 a = pv[0] * wj[0], a2 = pv[1] * wj[1];
#pragma unroll
        for (int j = 2; j < 10; j += 2) { a += pv[j] * wj[j]; a2 += pv[j + 1] * wj[j + 1]; }
        a += pv[10] * wj[10];
        a += a2;
        const unsigned v = nosent(pk(a.x, a.y));
        const v4u o4 = {dppu<0x00>(v), dppu<0x55>(v), dppu<0xAA>(v), dppu<0xFF>(v)};
        if ((lane & 3) == 0) ST128(o4, so);
        STAMP(7);
      }
    } else if (w == 11) {
      __builtin_amdgcn_s_setprio(3);
      ST128(SV, orec + lane * 16);
      if (lane < 8) ST128(SV, orec + 1024 + lane * 16);
      const u64 tag = load_tag(ws);
      const unsigned taghi = (unsigned)(tag >> 16), taglo = (unsigned)(tag << 16), tg = (unsigned)tag;
      const bool isR = i < 8;
      const int ln = g * 8 + i, rb = ln >> 4, pg = lane >> 3, q4 = lane & 7, col = 32 * (ln & 15) + 4 * q4;
      v2u xv = {0u, 0u};
      if (isR) xv = __builtin_amdgcn_raw_buffer_load_b64(rx, (rb * DM + col) * 2, 0, 0);
      DRAIN();
      lds_set(FL + F_CL, tg);
      STAMP(3);
      lds_wait(FL + F_VL + (lane < ntile ? lane : 0), tg);
      if (lane == 0) ST64(hint(taglo, taghi), WS_HA + (g * 32 + i) * 128);
      STAMPE(4);
      if (isR) {
        const rsrc_t ro = mk(out, NB * DM * 2u);
        unsigned pf = __builtin_amdgcn_raw_buffer_load_b32(ro, 0, 0, AUX_SC1);
        asm volatile("" :: "v"(pf));
        v2u hb;
        GSPIN {
          CBAR();
          hb = LD64(WS_HB + ((lane & 7) * 4 + ((ln >> 2) & 3)) * 128, 0);
          if (__all(hint_ok(hb, taglo, taghi))) break;
          __builtin_amdgcn_s_sleep(2);
        }
        STAMP(5);
        wait_age(hb.x, DLY3 + 20);
        v2u hv;
        GSPIN {
          CBAR();
          hv = LD64(WS_HP + (pg * NB + rb) * 1024 + col * 2, 0);
          if (__all(max(hv.x, hv.y) != SENT)) break;
        }
        STAMP(6);
        float h[4] = {blo(hv.x), bhi(hv.x), blo(hv.y), bhi(hv.y)};
#pragma unroll
        for (int d = 0; d < 4; ++d) { h[d] += dpp<ROR8>(h[d]); h[d] = x32_sum(x16_sum(h[d])); }
        {
          const unsigned a0 = pk(h[0], h[1]), a1 = pk(h[2], h[3]);
          const unsigned h0 = pk(blo(xv.x) + blo(a0), bhi(xv.x) + bhi(a0)), h1 = pk(blo(xv.y) + blo(a1), bhi(xv.y) + bhi(a1));
          h[0] = blo(h0); h[1] = bhi(h0); h[2] = blo(h1); h[3] = bhi(h1);
        }
        unsigned po = WS_OP + pg * 8192 + ln * 128 + q4 * 16, po3 = pg < 4 ? po + 24 * 8192 : po, oo = (rb * DM + col) * 2;
        PINV(po); PINV(po3); PINV(oo); PINV(h[0]); PINV(h[1]); PINV(h[2]); PINV(h[3]);
        v2u hc;
        GSPIN {
          CBAR();
          hc = LD64(WS_HC + (lane < NDCU ? lane : 0) * 128, 0);
          if (__all(hint_ok(hc, taglo, taghi))) break;
        }
        wait_age(hc.x, DLY5);
        STAMP(7);
        v4u pv[4];
        GSPIN {
          CBAR();
#pragma unroll
          for (int t = 0; t < 3; ++t) pv[t] = LD128(po, t * 8 * 8192);
          pv[3] = LD128(po3, 0);
          if (__all(okv(pv[0]) & okv(pv[1]) & okv(pv[2]) & okv(pv[3]))) break;
          RETRY();
        }
        STAMP(8);
        float y[4];
#pragma unroll
        for (int d = 0; d < 4; ++d) {
          float t = (__uint_as_float(pv[0][d]) + __uint_as_float(pv[1][d])) + (__uint_as_float(pv[2][d]) + (pg < 4 ? __uint_as_float(pv[3][d]) : 0.f));
          t += dpp<ROR8>(t);
          y[d] = x32_sum(x16_sum(t));
        }
        const unsigned y0 = pk(y[0], y[1]), y1 = pk(y[2], y[3]);
        if (lane < 8) __builtin_amdgcn_raw_buffer_store_b64(v2u{pk(h[0] + blo(y0), h[1] + bhi(y0)), pk(h[2] + blo(y1), h[3] + bhi(y1))}, ro, oo, 0, AUX_SC1);
        if (ln == 0 && lane == 0) ST64((v2u{(unsigned)tag, (unsigned)(tag >> 32)}), WS_EPOCH);
        STAMPE(9);
      }
    }
    PDUMP();
    return;
  }

  const int jq = i - 24;
  const bool isC = jq < 4;
  const int dI = g + 8 * (jq - 4);
  if (!isC && dI >= NDCU) { PDUMP(); return; }
  const u64 tag = load_tag(ws);
  const unsigned taghi = (unsigned)(tag >> 16), taglo = (unsigned)(tag << 16), tg = (unsigned)tag;
#define GATHER_H() do { \
    unsigned klo = 0x00003F80u, khi = 0x3F800000u, ho = WS_HP + w * 256 + lane * 4, oa = L_OA + w * 256 + lane * 4; \
    unsigned hp[8] = {0u, 0u, 0u, 0u, 0u, 0u, 0u, 0u}; \
    float x0 = blo(xp), x1 = bhi(xp); \
    PINS(klo); PINS(khi); PINV(oa); PINV(x0); PINV(x1); \
    ask8(hp, (unsigned)(size_t)(FL + F_GO + (lane & 7)), tg, ho, mkd(ws, WS_END)); \
    if (!__all(max(max(max(hp[0], hp[1]), max(hp[2], hp[3])), max(max(hp[4], hp[5]), max(hp[6], hp[7]))) != SENT)) { \
      GSPIN { \
        RETRY(); \
        CBAR(); \
        _Pragma("unroll") for (int j = 0; j < 8; ++j) hp[j] = LD32(ho, j * 4096); \
        if (__all(max(max(max(hp[0], hp[1]), max(hp[2], hp[3])), max(max(hp[4], hp[5]), max(hp[6], hp[7]))) != SENT)) break; \
      } \
      DRAIN(); \
    } \
    STAMP(7); \
    float a0 = 0.f, a1 = 0.f, a2 = 0.f, a3 = 0.f; \
    _Pragma("unroll") for (int j = 0; j < 4; ++j) { a0 = dot2(hp[j], klo, a0); a1 = dot2(hp[j], khi, a1); a2 = dot2(hp[4 + j], klo, a2); a3 = dot2(hp[4 + j], khi, a3); } \
    const unsigned t0 = pk(a0 + a2, a1 + a3); \
    *(LAS unsigned*)(L + oa) = pk(x0 + blo(t0), x1 + bhi(t0)); \
  } while (0)
  unsigned xp = 0;
  if (!isC) xp = __builtin_amdgcn_raw_buffer_load_b32(rx, w * 256 + lane * 4, 0, 0);
  if (w < 8) {
    const unsigned stride = isC ? DM * 2 : FF * 2;
    const GAS unsigned char* p = isC ? (const GAS unsigned char*)wo + (size_t)(512 * g) * (DM * 2) + (128 * jq + 8 * r16) * 2
                                     : (const GAS unsigned char*)(r16 < 8 ? wg : wu) + (64 * dI + 8 * (r16 & 7)) * 2;
    p += (size_t)(64 * w + 8 * gq) * stride;
    unsigned aa = L_OA + (r16 >> 2) * 1024 + w * 128 + gq * 16, pa = L_PART + w * 2048 + gq * 512 + r16 * 32;
    PINV(aa); PINV(pa);
    v4u src[16], bt[16];
    until(tstart, isC ? TC0 : TD0);
#pragma unroll
    for (int j = 0; j < WPRE; ++j) src[j] = __builtin_nontemporal_load((const GAS v4u*)(p + ((j >> 3) * 32 + (j & 7)) * stride));
    lds_wait(FL + F_QPUB + (lane & 3), tg);
#pragma unroll
    for (int j = WPRE; j < 16; ++j) src[j] = __builtin_nontemporal_load((const GAS v4u*)(p + ((j >> 3) * 32 + (j & 7)) * stride));
    SCHED();
    STAMP(4);
    tr8(src, bt);
    tr8(src + 8, bt + 8);
    if (!isC) {
      const v4u ones = {ONE2, ONE2, ONE2, ONE2};
      float cs[8];
#pragma unroll
      for (int e = 0; e < 8; ++e) cs[e] = mfma(ones, bt[8 + e], mfma(ones, bt[e], Z4))[0];
      if (lane < 16) {
        *(LAS f32x4*)(L + L_SS + w * 512 + lane * 32) = f32x4{cs[0], cs[1], cs[2], cs[3]};
        *(LAS f32x4*)(L + L_SS + w * 512 + lane * 32 + 16) = f32x4{cs[4], cs[5], cs[6], cs[7]};
      }
      lds_set(FL + F_SR + w, tg);
    }
    DRAIN();
    STAMP(5);
    if (w == 0) {
      if (isC) {
        const int hl = lane < 24 ? lane : 0;
        watch(rws, WS_HA + (g * 32 + hl) * 128, taglo, taghi, (LAS v2u*)(L + L_HINT + hl * 8), tg);
      } else {
        watch8(rws, WS_HB + (lane & 31) * 128, taglo, taghi, DLY3, FL + F_GO, tg, lane);
      }
      STAMP(10);
      DRAIN();
    } else if (w == 1 && isC) {
      lds_wait(FL + F_SR + (lane & 7), tg);
      if (lane == 0) ST64(hint(taglo, taghi), WS_HB + (g * 4 + jq) * 128);
    }
    if (!isC) GATHER_H();
    WGBAR();
    STAMP(8);
    {
      const v4u a0 = *(const LAS v4u*)(L + aa), a1 = *(const LAS v4u*)(L + aa + 64);
#pragma unroll
      for (int h = 0; h < 2; ++h) {
        f32x4 acc[4];
#pragma unroll
        for (int e = 0; e < 4; ++e) acc[e] = mfma(a0, bt[4 * h + e], Z4);
#pragma unroll
        for (int e = 0; e < 4; ++e) acc[e] = mfma(a1, bt[8 + 4 * h + e], acc[e]);
        *(LAS f32x4*)(L + pa + 16 * h) = f32x4{acc[0][0], acc[1][0], acc[2][0], acc[3][0]};
      }
    }
    STAMP(9);
    WGBAR();
    if (!isC) WGBAR();
  } else if (isC) {
    const int v = w & 7, bb = v >> 1, head = 2 * (v & 1) + (lane >> 5), sg = gq & 1;
    unsigned ra = L_PART + (w & 3) * 512 + lane * 8, so = WS_HP + (g * NB + (w & 3)) * 1024 + jq * 256 + lane * 4;
    unsigned rd = WS_OREC + (g * 24 + bb * NSPL + sg) * OREC_SZ + head * 256 + r16 * 16, rs = WS_OREC + (g * 24 + bb * NSPL + sg) * OREC_SZ + 1024 + head * 8;
    unsigned oa = L_OA + bb * 1024 + head * 256 + r16 * 16 + sg * 8;
    PINV(ra); PINV(so); PINV(rd); PINV(rs); PINV(oa);
    if (w == 15) ST128(SV, WS_HP + (g * NB + gq) * 1024 + jq * 256 + r16 * 16);
    DRAIN();
    STAMP(5);
    {
      v2u e;
#pragma nounroll
      for (unsigned sp = 0; sp < SPIN_L; ++sp) { e = *(volatile LAS v2u*)(L + L_HINT + (bb * NSPL + (lane < NSPL ? lane : 0)) * 8); if (__all(e.x == tg)) break; }
      wait_age(e.y, DLY2);
    }
    STAMP(6);
    GSPIN {
      v4u od[3];
      v2u st[3];
      CBAR();
#pragma unroll
      for (int u = 0; u < 3; ++u) st[u] = LD64(rs, 2 * u * OREC_SZ);
      SCHED();
#pragma unroll
      for (int u = 0; u < 3; ++u) od[u] = LD128(rd, 2 * u * OREC_SZ);
      SCHED();
      lds_set(FL + F_SR + v, tg);
      SCHED();
      unsigned bad = max(max(max(st[0].x, st[0].y), max(st[1].x, st[1].y)), max(st[2].x, st[2].y));
      const float m0 = __uint_as_float(st[0].x), m1 = __uint_as_float(st[1].x), m2 = __uint_as_float(st[2].x);
      const float mg = x16_max(fmaxf(fmaxf(m0, m1), m2));
      float wt[3] = {__builtin_amdgcn_exp2f(m0 - mg), __builtin_amdgcn_exp2f(m1 - mg), __builtin_amdgcn_exp2f(m2 - mg)};
      const float inv = __builtin_amdgcn_rcpf(x16_sum(fmaf(wt[0], __uint_as_float(st[0].y), fmaf(wt[1], __uint_as_float(st[1].y), wt[2] * __uint_as_float(st[2].y)))));
#pragma unroll
      for (int u = 0; u < 3; ++u) { wt[u] *= inv; PINV(wt[u]); }
      PINV(bad);
      SCHED();
      f32x2 a[4];
#pragma unroll
      for (int u = 0; u < 3; ++u) {
        f32x2 t[4];
#pragma unroll
        for (int d = 0; d < 4; ++d) { t[d] = f32x2{blo(od[u][d]), bhi(od[u][d])}; PINV(t[d]); }
        bad = max(bad, max(max(od[u].x, od[u].y), max(od[u].z, od[u].w)));
#pragma unroll
        for (int d = 0; d < 4; ++d) a[d] = u == 0 ? t[d] * wt[0] : t[d] * wt[u] + a[d];
      }
      float r[4];
#pragma unroll
      for (int d = 0; d < 4; ++d) {
        const auto sw = __builtin_amdgcn_permlane16_swap(__float_as_uint(a[d >> 1][d & 1]), __float_as_uint(a[2 + (d >> 1)][d & 1]), false, false);
        r[d] = __uint_as_float(sw[0]) + __uint_as_float(sw[1]);
      }
      *(LAS v2u*)(L + oa) = v2u{pk(r[0], r[1]), pk(r[2], r[3])};
      STAMP(7);
      if (__all(bad != SENT)) break;
      RETRY();
    }
    DRAIN();
    STAMP(8);
    WGBAR();
    WGBAR();
    if (w >= 12) {
      f32x2 pv[8];
#pragma unroll
      for (int j = 0; j < 8; ++j) pv[j] = *(const LAS f32x2*)(L + ra + j * 2048);
      SCHED();
      const f32x2 t = ((pv[0] + pv[1]) + (pv[2] + pv[3])) + ((pv[4] + pv[5]) + (pv[6] + pv[7]));
      const unsigned vv = nosent(pk(t.x, t.y));
      const v4u o4 = {dppu<0x00>(vv), dppu<0x55>(vv), dppu<0xAA>(vv), dppu<0xFF>(vv)};
      if ((lane & 3) == 0) ST128(o4, so);
      STAMPE(9);
    }
  } else {
    unsigned ra = L_PART + (w & 3) * 512 + lane * 4, hw = L_HH + (w & 3) * 128 + lane * 2, ha = L_HH + (r16 >> 2) * 128 + gq * 16;
    unsigned so = WS_OP + dI * 8192 + gq * 2048 + (w & 7) * 256 + r16 * 16, sa = L_OA + (w & 3) * 1024 + lane * 16;
    PINV(ra); PINV(hw); PINV(ha); PINV(so); PINV(sa);
    ST128(SV, WS_OP + dI * 8192 + (w - 8) * 1024 + lane * 16);
    until(tstart, TWD);
    const rsrc_t rw = mk(wd, FF * DM * 2u);
    v2u src[16];
    v4u bt[8];
#pragma unroll
    for (int j = 0; j < 16; ++j) src[j] = __builtin_amdgcn_raw_buffer_load_b64(rw, (unsigned)(8 * gq + (j & 3)) * 1024 + (w - 8) * 128 + r16 * 8, (64 * dI + (j >> 3) * 32 + (j & 4)) * 1024, AUX_NT);
    SCHED();
    tr4(src, bt);
    tr4(src + 8, bt + 4);
    float sgs = 0.f, sus = 0.f;
    if (w >= 12) {
      lds_wait(FL + F_SR + (lane & 7), tg);
#pragma unroll
      for (int j = 0; j < 8; ++j) { sgs += *(const LAS float*)(L + L_SS + j * 512 + lane * 4); sus += *(const LAS float*)(L + L_SS + j * 512 + 256 + lane * 4); }
    }
    PINV(sgs); PINV(sus);
    DRAIN();
    STAMP(5);
    GATHER_H();
    WGBAR();
    if (w >= 12) {
      const f32x2 st = ln_stats(*(const LAS v4u*)(L + sa));
      float kg = st.y * -1.4426950408889634f, cg = st.x * sgs, cu = st.x * sus, ku = st.y;
      PINV(kg); PINV(cg); PINV(cu); PINV(ku);
      WGBAR();
      float gp[8], up[8];
#pragma unroll
      for (int j = 0; j < 8; ++j) { gp[j] = *(const LAS float*)(L + ra + j * 2048); up[j] = *(const LAS float*)(L + ra + j * 2048 + 256); }
      SCHED();
      const float gg = ((gp[0] + gp[1]) + (gp[2] + gp[3])) + ((gp[4] + gp[5]) + (gp[6] + gp[7]));
      const float uu = ((up[0] + up[1]) + (up[2] + up[3])) + ((up[4] + up[5]) + (up[6] + up[7]));
      const float hh = ku * (uu - cu) * __builtin_amdgcn_rcpf(1.f + __builtin_amdgcn_exp2f(kg * (gg - cg)));
      *(LAS u16*)(L + hw) = (u16)pk(hh, hh);
    } else {
      if (w == 8 && lane == 0) ST64(hint(taglo, taghi), WS_HC + dI * 128);
      WGBAR();
    }
    WGBAR();
    STAMP(8);
    const v4u a0 = *(const LAS v4u*)(L + ha), a1 = *(const LAS v4u*)(L + ha + 64);
    f32x4 acc[4];
#pragma unroll
    for (int e = 0; e < 4; ++e) acc[e] = mfma(a0, bt[e], Z4);
#pragma unroll
    for (int e = 0; e < 4; ++e) acc[e] = mfma(a1, bt[4 + e], acc[e]);
    ST128((v4u{nosent(__float_as_uint(acc[0][0])), nosent(__float_as_uint(acc[1][0])), nosent(__float_as_uint(acc[2][0])), nosent(__float_as_uint(acc[3][0]))}), so);
    STAMPE(9);
  }
  PDUMP();
}
}

extern "C" void kernel_launch(void* const* d_in, const int* in_sizes, int n_in, void* d_out, int out_size, void* d_ws, size_t ws_size, hipStream_t stream) {
  (void)in_sizes; (void)n_in; (void)out_size; (void)ws_size;
  fused_decode<<<dim3(256), dim3(1024), 0, stream>>>((const u16*)d_in[0], (const u16*)d_in[1], (const u16*)d_in[2], (const u16*)d_in[3], (const u16*)d_in[4],
                                                     (const u16*)d_in[6], (const u16*)d_in[7], (const u16*)d_in[8], (u16*)d_out, (unsigned char*)d_ws);
}
```

```cpp
#include <hip/hip_runtime.h>
#include <stdint.h>

#define PROF 0
#define TK0 80
#define KPRE 4
#define QDELTA 70
#define TC0 300
#define TD0 220
#define KNT 0
#define VNT 0
#define WNT 0
#define QNT 0
#define WPRE 0
#define TWD 470
#define DLY2 60
#define DLY3 90
#define DLY5 76

namespace {
typedef unsigned short u16;
typedef unsigned long long u64;
typedef unsigned v4u __attribute__((ext_vector_type(4)));
typedef unsigned v2u __attribute__((ext_vector_type(2)));
typedef int v4i __attribute__((ext_vector_type(4)));
typedef float f32x4 __attribute__((ext_vector_type(4)));
typedef float f32x2 __attribute__((ext_vector_type(2)));
typedef short bf16x8 __attribute__((ext_vector_type(8)));
typedef __bf16 bf16x2 __attribute__((ext_vector_type(2)));
#define GAS __attribute__((address_space(1)))
#define LAS __attribute__((address_space(3)))
typedef __amdgpu_buffer_rsrc_t rsrc_t;

constexpr int NB = 4, DM = 512, HKV = 8, SK = 8192, WIN = 2048, FF = 1792, NQ = 4096;
constexpr int NSPL = 6, NDCU = 28;
constexpr int AUX_NT = 2, AUX_SC1 = 16;
constexpr unsigned SENT = 0xFFFFFFFFu, ONE2 = 0x3F803F80u;
constexpr int WS_EPOCH = 0, WS_HB = 4096, WS_HC = WS_HB + 32 * 128, WS_HA = WS_HC + 32 * 128, WS_QG = WS_HA + 8 * 32 * 128;
constexpr int WS_OREC = WS_QG + 8 * 4 * 4 * 512 * 8, OREC_SZ = 1152;
constexpr int WS_HP = WS_OREC + 8 * 24 * OREC_SZ, WS_OP = WS_HP + 8 * 4 * 512 * 2, WS_END = WS_OP + NDCU * 8192;
constexpr int WS_PROF = 2 << 20;
constexpr int L_FLAG = 0, L_MW = 512, L_HINT = 512, L_QS = 1024, L_OA = L_QS + 1024, L_HH = L_OA + 4096, L_SS = L_HH + 512;
constexpr int L_QRED = L_SS + 4096, L_PART = L_QRED + 5120, L_USED = L_PART + 11 * 2048;
constexpr int L_TOTAL = 84 * 1024;
static_assert(L_USED <= L_TOTAL, "lds");
enum { F_QIS = 0, F_QS = 4, F_QPUB = 12, F_QR = 16, F_CL = 24, F_GO = 32, F_SR = 72, F_MW = 80, F_VL = 96, F_OPF = 112 };
constexpr int CODE_BYTES = 15360;
constexpr unsigned SPIN_G = 1u << 17, SPIN_L = 1u << 21;

__device__ __forceinline__ u64 rt() { return __builtin_amdgcn_s_memrealtime(); }
__device__ __forceinline__ rsrc_t mk(const void* p, unsigned bytes) { return __builtin_amdgcn_make_buffer_rsrc((void*)p, 0, bytes, 0x00020000); }
__device__ __forceinline__ f32x4 mfma(v4u a, v4u b, f32x4 c) { return __builtin_amdgcn_mfma_f32_16x16x32_bf16(__builtin_bit_cast(bf16x8, a), __builtin_bit_cast(bf16x8, b), c, 0, 0, 0); }
__device__ __forceinline__ unsigned pk(float lo, float hi) { return __builtin_bit_cast(unsigned, __builtin_convertvector(f32x2{lo, hi}, bf16x2)); }
__device__ __forceinline__ float blo(unsigned v) { return __builtin_bit_cast(float, v << 16); }
__device__ __forceinline__ float bhi(unsigned v) { return __builtin_bit_cast(float, v & 0xffff0000u); }
__device__ __forceinline__ unsigned plo(unsigned a, unsigned b) { return __builtin_amdgcn_perm(b, a, 0x05040100u); }
__device__ __forceinline__ unsigned phi(unsigned a, unsigned b) { return __builtin_amdgcn_perm(b, a, 0x07060302u); }
__device__ __forceinline__ unsigned nosent(unsigned v) { return v < 0xFFFFFFFEu ? v : 0xFFFFFFFEu; }
template <int C> __device__ __forceinline__ float dpp(float x) { return __builtin_bit_cast(float, __builtin_amdgcn_mov_dpp(__builtin_bit_cast(int, x), C, 0xf, 0xf, true)); }
template <int C> __device__ __forceinline__ unsigned dppu(unsigned x) { return (unsigned)__builtin_amdgcn_mov_dpp((int)x, C, 0xf, 0xf, true); }
constexpr int XOR1 = 0xB1, XOR2 = 0x4E, ROR4 = 0x124, ROR8 = 0x128;
__device__ __forceinline__ float x16_sum(float x) { auto s = __builtin_amdgcn_permlane16_swap(__float_as_uint(x), __float_as_uint(x), false, false); return __uint_as_float(s[0]) + __uint_as_float(s[1]); }
__device__ __forceinline__ float x32_sum(float x) { auto s = __builtin_amdgcn_permlane32_swap(__float_as_uint(x), __float_as_uint(x), false, false); return __uint_as_float(s[0]) + __uint_as_float(s[1]); }
__device__ __forceinline__ float x16_max(float x) { auto s = __builtin_amdgcn_permlane16_swap(__float_as_uint(x), __float_as_uint(x), false, false); return fmaxf(__uint_as_float(s[0]), __uint_as_float(s[1])); }
__device__ __forceinline__ float x32_max(float x) { auto s = __builtin_amdgcn_permlane32_swap(__float_as_uint(x), __float_as_uint(x), false, false); return fmaxf(__uint_as_float(s[0]), __uint_as_float(s[1])); }
__device__ __forceinline__ float row_sum(float x) { x += dpp<XOR1>(x); x += dpp<XOR2>(x); x += dpp<ROR4>(x); x += dpp<ROR8>(x); return x; }
__device__ __forceinline__ float row_max(float x) { x = fmaxf(x, dpp<XOR1>(x)); x = fmaxf(x, dpp<XOR2>(x)); x = fmaxf(x, dpp<ROR4>(x)); x = fmaxf(x, dpp<ROR8>(x)); return x; }
__device__ __forceinline__ float dot2(unsigned a, unsigned b, float c) { return __builtin_amdgcn_fdot2_f32_bf16(__builtin_bit_cast(bf16x2, a), __builtin_bit_cast(bf16x2, b), c, false); }
#define CBAR() asm volatile("" ::: "memory")
#define SCHED() __builtin_amdgcn_sched_barrier(0)
#define DRAIN() do { asm volatile("s_waitcnt vmcnt(0)" ::: "memory"); __builtin_amdgcn_s_waitcnt(0x0F70); } while (0)
#define PINV(x) asm volatile("" : "+v"(x))
#define PINS(x) asm volatile("" : "+s"(x))
#define WGBAR() do { SCHED(); asm volatile("s_waitcnt lgkmcnt(0)" ::: "memory"); __builtin_amdgcn_s_barrier(); CBAR(); SCHED(); } while (0)
#define GSPIN _Pragma("nounroll") for (unsigned sp = 0; sp < SPIN_G; ++sp)
#define LD32(o, so) __builtin_amdgcn_raw_buffer_load_b32(rws, (o), (so), AUX_SC1)
#define LD64(o, so) __builtin_amdgcn_raw_buffer_load_b64(rws, (o), (so), AUX_SC1)
#define LD128(o, so) __builtin_amdgcn_raw_buffer_load_b128(rws, (o), (so), AUX_SC1)
#define ST64(v, o) __builtin_amdgcn_raw_buffer_store_b64((v), rws, (o), 0, AUX_SC1)
#define ST128(v, o) __builtin_amdgcn_raw_buffer_store_b128((v), rws, (o), 0, AUX_SC1)
__device__ __forceinline__ bool okv(v4u r) { return max(max(r.x, r.y), max(r.z, r.w)) != SENT; }
__device__ __forceinline__ void lds_wait(volatile LAS unsigned* f, unsigned tg) {
  SCHED();
#pragma nounroll
  for (unsigned s = 0; s < SPIN_L; ++s) { if (__all(*f == tg)) break; }
  CBAR();
  SCHED();
}
__device__ __forceinline__ void lds_set(volatile LAS unsigned* f, unsigned tg) { CBAR(); *f = tg; CBAR(); }
__device__ __forceinline__ void until(u64 t0, unsigned ticks) { while (rt() - t0 < ticks) __builtin_amdgcn_s_sleep(1); }
__device__ __forceinline__ v2u hint(unsigned taglo, unsigned taghi) { return v2u{taglo | ((unsigned)rt() & 0xFFFFu), taghi}; }
__device__ __forceinline__ bool hint_ok(v2u h, unsigned taglo, unsigned taghi) { return (h.y == taghi) & ((h.x ^ taglo) < 65536u); }
__device__ __forceinline__ void wait_age(unsigned t16, unsigned dly) {
#pragma nounroll
  for (unsigned s = 0; s < 4096u; ++s) { if (__all((((unsigned)rt() - t16) & 0xFFFFu) >= dly)) break; }
}
__device__ __forceinline__ void watch(rsrc_t rws, unsigned ho, unsigned taglo, unsigned taghi, LAS v2u* slot, unsigned tg) {
  GSPIN {
    CBAR();
    const v2u h = LD64(ho, 0);
    const bool ok = hint_ok(h, taglo, taghi);
    if (ok) *slot = v2u{tg, h.x};
    CBAR();
    if (__all(ok)) break;
  }
}
__device__ __forceinline__ void watch8(rsrc_t rws, unsigned ho, unsigned taglo, unsigned taghi, unsigned dly, volatile LAS unsigned* go, unsigned tg, int lane) {
  v2u h;
  unsigned seen = 0, done = 0;
#pragma nounroll
  for (unsigned sp = 0; sp < SPIN_G && done != 0x11111111u; ++sp) {
    if (seen != ~0u) {
      CBAR();
      h = LD64(ho, 0);
      seen = (unsigned)__builtin_amdgcn_ballot_w64(hint_ok(h, taglo, taghi));
    }
    unsigned m = (unsigned)__builtin_amdgcn_ballot_w64((((unsigned)rt() - h.x) & 0xFFFFu) >= dly) & seen;
    m &= m >> 1; m &= m >> 2; m &= 0x11111111u;
    if (m != done) { if ((m >> (4 * (lane & 7))) & 1u) go[lane & 7] = tg; done = m; }
  }
  CBAR();
}
__device__ __forceinline__ u64 load_tag(const unsigned char* ws) {
  u64 e;
  asm volatile("s_load_dwordx2 %0, %1, 0x0\n\ts_waitcnt lgkmcnt(0)" : "=s"(e) : "s"(ws) : "memory");
  return (e + 1ull) & 0xFFFFFFFFFFFFull;
}
__device__ __forceinline__ void code_fetch(int part) {
  u64 pc0;
  asm volatile("s_getpc_b64 %0" : "=s"(pc0));
  const u64 a0 = (pc0 & ~127ull) + (u64)(part * 512 < CODE_BYTES - 512 ? part * 512 : CODE_BYTES - 512);
  unsigned t0, t1, t2, t3, t4, t5, t6, t7;
  asm volatile("s_load_dword %0, %8, 0x0\n\ts_load_dword %1, %8, 0x40\n\ts_load_dword %2, %8, 0x80\n\ts_load_dword %3, %8, 0xc0\n\t"
               "s_load_dword %4, %8, 0x100\n\ts_load_dword %5, %8, 0x140\n\ts_load_dword %6, %8, 0x180\n\ts_load_dword %7, %8, 0x1c0\n\ts_waitcnt lgkmcnt(0)"
               : "=&s"(t0), "=&s"(t1), "=&s"(t2), "=&s"(t3), "=&s"(t4), "=&s"(t5), "=&s"(t6), "=&s"(t7) : "s"(a0) : "memory");
}
__device__ __forceinline__ v4i mkd(const void* p, unsigned bytes) { return v4i{(int)(u64)p, (int)((u64)p >> 32) & 0xFFFF, (int)bytes, 0x00020000}; }
template <int OFF, bool NT> __device__ __forceinline__ void ldnt(v4u& d, unsigned vo, v4i rs, unsigned so) {
  if (NT) asm volatile("buffer_load_dwordx4 %0, %1, %2, %3 offen offset:%4 nt" : "=v"(d) : "v"(vo), "s"(rs), "s"(so), "i"(OFF) : "memory");
  else asm volatile("buffer_load_dwordx4 %0, %1, %2, %3 offen offset:%4" : "=v"(d) : "v"(vo), "s"(rs), "s"(so), "i"(OFF) : "memory");
}
template <int LO, int HI, int NTMASK> __device__ __forceinline__ void ldtile(v4u* d, unsigned vo, v4i rs, unsigned so, unsigned so2) {
  if constexpr (LO < HI) { ldnt<(LO & 3) * 1024, ((NTMASK >> LO) & 1) != 0>(d[LO], vo, rs, LO < 4 ? so : so2); ldtile<LO + 1, HI, NTMASK>(d, vo, rs, so, so2); }
}
__device__ __forceinline__ void ldnt2(v2u& d, unsigned vo, v4i rs, unsigned so) {
  if (QNT) asm volatile("buffer_load_dwordx2 %0, %1, %2, %3 offen nt" : "=v"(d) : "v"(vo), "s"(rs), "s"(so) : "memory");
  else asm volatile("buffer_load_dwordx2 %0, %1, %2, %3 offen" : "=v"(d) : "v"(vo), "s"(rs), "s"(so) : "memory");
}
__device__ __forceinline__ void ldp4(v4u& d, unsigned vo, v4i rs) {
  asm volatile("buffer_load_dwordx4 %0, %1, %2, 0 offen" : "=v"(d) : "v"(vo), "s"(rs) : "memory");
}
#define VMWAIT2(n, a, b) asm volatile("s_waitcnt vmcnt(%2)" : "+v"(a), "+v"(b) : "i"(n) : "memory")
__device__ __forceinline__ void ask8(unsigned (&hp)[8], unsigned go, unsigned tg, unsigned ho, v4i rs) {
  unsigned t, done, n, cnt;
#define ASK1(j, off) "s_bitcmp1_b32 %10, " #j "\n\ts_cbranch_scc0 2" #j "f\n\ts_movk_i32 %11, " #off "\n\tbuffer_load_dword %" #j ", %14, %15, %11 offen sc1\n2" #j ":\n\t"
  asm volatile("s_mov_b32 %9, 0\n\ts_mov_b32 %11, 0x200000\n"
               "1:\n\tds_read_b32 %8, %12\n\ts_sub_u32 %11, %11, 1\n\ts_cbranch_scc1 3f\n\ts_waitcnt lgkmcnt(0)\n\t"
               "v_cmp_eq_u32 vcc, %13, %8\n\ts_andn2_b32 %10, vcc_lo, %9\n\ts_and_b32 %10, %10, 0xff\n\ts_cbranch_scc0 1b\n\t"
               ASK1(0, 0) ASK1(1, 0x1000) ASK1(2, 0x2000) ASK1(3, 0x3000) ASK1(4, 0x4000) ASK1(5, 0x5000) ASK1(6, 0x6000) ASK1(7, 0x7000)
               "s_mov_b32 %11, 0x200000\n\ts_or_b32 %9, %9, %10\n\ts_cmp_lg_u32 %9, 0xff\n\ts_cbranch_scc1 1b\n"
               "3:\n\ts_waitcnt vmcnt(0) lgkmcnt(0)"
               : "+v"(hp[0]), "+v"(hp[1]), "+v"(hp[2]), "+v"(hp[3]), "+v"(hp[4]), "+v"(hp[5]), "+v"(hp[6]), "+v"(hp[7]), "=&v"(t), "=&s"(done), "=&s"(n), "=&s"(cnt)
               : "v"(go), "s"(tg), "v"(ho), "s"(rs) : "vcc", "scc", "memory");
#undef ASK1
}
__device__ __forceinline__ f32x2 ln_stats(v4u hv) {
  const float c0 = blo((unsigned)__builtin_amdgcn_readfirstlane(hv[0]));
  float t[8];
#pragma unroll
  for (int d = 0; d < 4; ++d) { t[2 * d] = blo(hv[d]) - c0; t[2 * d + 1] = bhi(hv[d]) - c0; }
  float s1 = ((t[0] + t[1]) + (t[2] + t[3])) + ((t[4] + t[5]) + (t[6] + t[7]));
  float s2 = (fmaf(t[1], t[1], t[0] * t[0]) + fmaf(t[3], t[3], t[2] * t[2])) + (fmaf(t[5], t[5], t[4] * t[4]) + fmaf(t[7], t[7], t[6] * t[6]));
  s1 += dpp<XOR1>(s1); s2 += dpp<XOR1>(s2);
  s1 += dpp<XOR2>(s1); s2 += dpp<XOR2>(s2);
  s1 += dpp<ROR4>(s1); s2 += dpp<ROR4>(s2);
  s1 += dpp<ROR8>(s1); s2 += dpp<ROR8>(s2);
  s1 = x16_sum(s1); s2 = x16_sum(s2);
  s1 = x32_sum(s1); s2 = x32_sum(s2);
  const float d = s1 * (1.f / DM), var = fmaxf(fmaf(-d, d, s2 * (1.f / DM)), 0.f);
  return f32x2{c0 + d, __builtin_amdgcn_rsqf(var + 1e-5f)};
}
#define STAMP(k) do {} while (0)
#define STAMPE(k) do {} while (0)
#define PDUMP() do {} while (0)
#define RETRY() do {} while (0)

__device__ __forceinline__ void tr4(const v2u* s, v4u* o) {
#pragma unroll
  for (int m = 0; m < 4; ++m) {
    o[0][m] = plo(s[2 * m].x, s[2 * m + 1].x); o[1][m] = phi(s[2 * m].x, s[2 * m + 1].x);
    o[2][m] = plo(s[2 * m].y, s[2 * m + 1].y); o[3][m] = phi(s[2 * m].y, s[2 * m + 1].y);
  }
#pragma unroll
  for (int e = 0; e < 4; ++e) PINV(o[e]);
}
__device__ __forceinline__ void tr8(const v4u* s, v4u* o) {
#pragma unroll
  for (int m = 0; m < 4; ++m)
#pragma unroll
    for (int d = 0; d < 4; ++d) { o[2 * d][m] = plo(s[2 * m][d], s[2 * m + 1][d]); o[2 * d + 1][m] = phi(s[2 * m][d], s[2 * m + 1][d]); }
#pragma unroll
  for (int e = 0; e < 8; ++e) PINV(o[e]);
}

__global__ void __launch_bounds__(1024) fused_decode(const u16* __restrict__ x, const u16* __restrict__ kc, const u16* __restrict__ vc,
                                                     const u16* __restrict__ wq, const u16* __restrict__ wo, const u16* __restrict__ wg,
                                                     const u16* __restrict__ wu, const u16* __restrict__ wd, u16* __restrict__ out, unsigned char* ws) {
  const u64 tstart = rt();
  __shared__ __attribute__((aligned(16))) unsigned char smem[L_TOTAL];
  LAS unsigned char* L = (LAS unsigned char*)smem;
  volatile LAS unsigned* FL = (volatile LAS unsigned*)(L + L_FLAG);
  const int lane = threadIdx.x & 63;
  int w = (__builtin_amdgcn_readfirstlane(threadIdx.x >> 6) + 11) & 15;
  const int c = blockIdx.x, g = c & 7, i = c >> 3;
  if (w == 11) code_fetch(i);
  const int gq = lane >> 4, r16 = lane & 15;
  const int b = (i * 43) >> 8, s = i - b * NSPL;
  const int ntile = s < 4 ? 11 : 10, tile0 = 11 * s - (s > 4 ? 1 : 0);
  const int cw = w - 12, rq = i & 3, cq = i >> 2, R0 = 128 * rq + 32 * cw;
  asm volatile("" :: "s"(x), "s"(kc), "s"(vc), "s"(wq), "s"(wo), "s"(wg), "s"(wu), "s"(wd), "s"(out), "s"(ws));
  v4u kf[8], vf[8], xa, xr;
  v2u wsrc[8];
  const unsigned kvbase = ((unsigned)(b * HKV + g) * SK + (SK - WIN) + 32u * tile0 + 16u * w) * 256u, kvbase2 = kvbase + ntile * 4096u;
  const v4i rk = mkd(kc, NB * HKV * SK * 256u), rv = mkd(vc, NB * HKV * SK * 256u);
  if (w >= 12) {
    __builtin_amdgcn_s_setprio(3);
    const v4i rw = mkd(wq, DM * NQ * 2u), rxd = mkd(x, NB * DM * 2u);
    const unsigned o = (unsigned)(8 * gq) * (NQ * 2) + r16 * 8, so = R0 * (NQ * 2) + (512 * g + 64 * cq) * 2;
#pragma unroll
    for (int j = 0; j < 8; ++j) ldnt2(wsrc[j], o, rw, so + j * (NQ * 2));
    ldp4(xa, (r16 >> 2) * 1024 + (R0 + 8 * gq) * 2, rxd);
    ldp4(xr, cw * 1024 + lane * 16, rxd);
  } else if (i < 24 && w < ntile) {
    __builtin_amdgcn_s_setprio(2);
    until(tstart, TK0);
    ldtile<0, KPRE, KNT>(kf, r16 * 64 + gq * 16, rk, kvbase, kvbase2);
  }
  STAMP(1);
  PINS(w);
  const rsrc_t rws = mk(ws, WS_END), rx = mk(x, NB * DM * 2u);
  const f32x4 Z4 = {0.f, 0.f, 0.f, 0.f};
  const v4u SV = {SENT, SENT, SENT, SENT};
  const unsigned orec = WS_OREC + (g * 24 + i) * OREC_SZ;

  if (__builtin_expect(w >= 12, 1)) {
    const u64 tag = load_tag(ws);
    const unsigned tg = (unsigned)tag, qhi = (unsigned)(tag >> 8), qlo = (unsigned)(tag << 24);
    asm volatile("s_waitcnt vmcnt(0)" : "+v"(wsrc[0]), "+v"(wsrc[1]), "+v"(wsrc[2]), "+v"(wsrc[3]), "+v"(wsrc[4]), "+v"(wsrc[5]), "+v"(wsrc[6]), "+v"(wsrc[7]), "+v"(xa), "+v"(xr) :: "memory");
    {
      v4u bt[4];
      tr4(wsrc, bt);
      if ((lane & 3) == 1) xa = v4u{ONE2, ONE2, ONE2, ONE2};
      f32x4 qp, cs;
#pragma unroll
      for (int e = 0; e < 4; ++e) { const f32x4 d = mfma(xa, bt[e], Z4); qp[e] = d[0]; cs[e] = d[1]; }
      *(LAS f32x4*)(L + L_QRED + cw * 1024 + lane * 16) = qp;
      if (lane < 16) *(LAS f32x4*)(L + L_QRED + 4096 + cw * 256 + lane * 16) = cs;
    }
    lds_set(FL + F_QR + cw, tg);
    f32x2 xst = ln_stats(xr);
    PINV(xst);
    STAMP(2);
    lds_wait(FL + F_QR + (lane & 3), tg);
    {
      float a[4], cs[4];
#pragma unroll
      for (int j = 0; j < 4; ++j) { a[j] = *(const LAS float*)(L + L_QRED + j * 1024 + cw * 256 + lane * 4); cs[j] = *(const LAS float*)(L + L_QRED + 4096 + j * 256 + lane * 4); }
      const float q = xst.y * (((a[0] + a[1]) + (a[2] + a[3])) - xst.x * ((cs[0] + cs[1]) + (cs[2] + cs[3])));
      ST64((v2u{qlo | ((__float_as_uint(q) + 0x80u) >> 8), qhi}), WS_QG + (((g * NB + cw) * 4 + rq) * 512 + 64 * cq + lane) * 8);
    }
    lds_set(FL + F_QPUB + cw, tg);
    STAMP(3);
    if (i < 24) {
      until(rt(), QDELTA);
      const unsigned qo = WS_QG + ((g * NB + b) * 4 * 512 + 2 * (cw * 64 + lane)) * 8;
      v4u qg[4];
      GSPIN {
        CBAR();
        bool ok = true;
#pragma unroll
        for (int j = 0; j < 4; ++j) qg[j] = LD128(qo, j * 4096);
        SCHED();
        lds_set(FL + F_QIS + cw, tg);
        SCHED();
#pragma unroll
        for (int j = 0; j < 4; ++j) ok &= (qg[j].y == qhi) & (qg[j].w == qhi) & ((qg[j].x ^ qlo) < (1u << 24)) & ((qg[j].z ^ qlo) < (1u << 24));
        if (__all(ok)) break;
        RETRY();
      }
      const float q0 = (__uint_as_float(qg[0].x << 8) + __uint_as_float(qg[1].x << 8)) + (__uint_as_float(qg[2].x << 8) + __uint_as_float(qg[3].x << 8));
      const float q1 = (__uint_as_float(qg[0].z << 8) + __uint_as_float(qg[1].z << 8)) + (__uint_as_float(qg[2].z << 8) + __uint_as_float(qg[3].z << 8));
      *(LAS unsigned*)(L + L_QS + (cw * 64 + lane) * 4) = pk(q0, q1);
      lds_set(FL + F_QS + cw, tg);
      STAMP(4);
      PDUMP();
      return;
    }
    __builtin_amdgcn_s_setprio(0);
  } else if (i < 24) {
    if (w < ntile) {
      const unsigned tg = (unsigned)load_tag(ws);
      lds_wait(FL + F_QPUB + (lane & 3), tg);
      ldtile<KPRE, 8, KNT>(kf, r16 * 64 + gq * 16, rk, kvbase, kvbase2);
      __builtin_amdgcn_s_setprio(0);
      lds_wait(FL + F_QIS + (lane & 3), tg);
      ldtile<0, 8, VNT>(vf, lane * 16, rv, kvbase, kvbase2);
      STAMP(2);
      lds_wait(FL + (lane < 4 ? F_QS + lane : F_CL), tg);
      asm volatile("s_waitcnt vmcnt(8)" : "+v"(kf[0]), "+v"(kf[1]), "+v"(kf[2]), "+v"(kf[3]), "+v"(kf[4]), "+v"(kf[5]), "+v"(kf[6]), "+v"(kf[7]) :: "memory");
      STAMP(3);
      const v4u qf = *(const LAS v4u*)(L + L_QS + r16 * 64 + gq * 16);
      float sc[8];
#pragma unroll
      for (int t = 0; t < 8; ++t) {
        const f32x4 d = mfma(kf[t], qf, Z4);
        sc[t] = (dpp<0x00>(d[0]) + dpp<0x55>(d[1])) + (dpp<0xAA>(d[2]) + dpp<0xFF>(d[3]));
      }
      float m = fmaxf(fmaxf(fmaxf(sc[0], sc[1]), fmaxf(sc[2], sc[3])), fmaxf(fmaxf(sc[4], sc[5]), fmaxf(sc[6], sc[7])));
      m = x32_max(x16_max(m));
      constexpr float C2 = 0.08838834764831845f * 1.4426950408889634f;
      const float ms = m * C2;
      float p[8], l = 0.f;
#pragma unroll
      for (int t = 0; t < 8; ++t) { p[t] = __builtin_amdgcn_exp2f(fmaf(sc[t], C2, -ms)); l += p[t]; }
      l = x32_sum(x16_sum(l));
      if ((lane & 0x33) == 0) *(LAS f32x2*)(L + L_MW + ((lane >> 2) * 12 + w) * 8) = f32x2{ms, l};
      lds_set(FL + F_MW + w, tg);
      v4u pf = {pk(p[0], p[1]), pk(p[2], p[3]), pk(p[4], p[5]), pk(p[6], p[7])};
      unsigned pa = L_PART + w * 2048 + gq * 512 + r16 * 32;
      PINV(pf); PINV(pa);
      STAMP(4);
      v4u vt[8];
#pragma unroll
      for (int mm = 0; mm < 4; ++mm) {
        if (mm == 0) VMWAIT2(6, vf[0], vf[1]);
        if (mm == 1) VMWAIT2(4, vf[2], vf[3]);
        if (mm == 2) { lds_set(FL + F_VL + w, tg); VMWAIT2(2, vf[4], vf[5]); }
        if (mm == 3) { VMWAIT2(0, vf[6], vf[7]); STAMP(5); }
#pragma unroll
        for (int d = 0; d < 4; ++d) { vt[2 * d][mm] = plo(vf[2 * mm][d], vf[2 * mm + 1][d]); vt[2 * d + 1][mm] = phi(vf[2 * mm][d], vf[2 * mm + 1][d]); }
        if (mm < 3) {
#pragma unroll
          for (int e = 0; e < 8; ++e) PINV(vt[e]);
        }
        SCHED();
      }
      float o[8];
#pragma unroll
      for (int e = 0; e < 8; ++e) o[e] = mfma(pf, vt[e], Z4)[0];
      *(LAS f32x4*)(L + pa) = f32x4{o[0], o[1], o[2], o[3]};
      *(LAS f32x4*)(L + pa + 16) = f32x4{o[4], o[5], o[6], o[7]};
      lds_set(FL + F_OPF + w, tg);
      STAMP(6);
      if (w < 4) {
        const int jl = r16 < ntile ? r16 : 0;
        lds_wait(FL + F_MW + jl, tg);
        const f32x2 ml = *(const LAS f32x2*)(L + L_MW + (w * 12 + jl) * 8);
        const float mg = row_max(ml.x);
        const float wt = r16 < ntile ? __builtin_amdgcn_exp2f(ml.x - mg) : 0.f;
        const float lt = row_sum(wt * ml.y);
        float wj[11];
#pragma unroll
        for (int j = 0; j < 11; ++j) { wj[j] = __builtin_bit_cast(float, __builtin_amdgcn_readlane(__builtin_bit_cast(int, wt), j)); PINS(wj[j]); }
        if (lane == 0) ST64((v2u{nosent(__float_as_uint(mg)), nosent(__float_as_uint(lt))}), orec + 1024 + w * 8);
        unsigned ra = L_PART + w * 512 + lane * 8, rl = ra + (ntile - 1) * 2048, so = orec + w * 256 + lane * 4;
        PINV(ra); PINV(rl); PINV(so);
        lds_wait(FL + F_OPF + jl, tg);
        f32x2 pv[11];
#pragma unroll
        for (int j = 0; j < 10; ++j) pv[j] = *(const LAS f32x2*)(L + ra + j * 2048);
        pv[10] = *(const LAS f32x2*)(L + rl);
        SCHED();
        f32x2 a = pv[0] * wj[0], a2 = pv[1] * wj[1];
#pragma unroll
        for (int j = 2; j < 10; j += 2) { a += pv[j] * wj[j]; a2 += pv[j + 1] * wj[j + 1]; }
        a += pv[10] * wj[10];
        a += a2;
        const unsigned v = nosent(pk(a.x, a.y));
        const v4u o4 = {dppu<0x00>(v), dppu<0x55>(v), dppu<0xAA>(v), dppu<0xFF>(v)};
        if ((lane & 3) == 0) ST128(o4, so);
        STAMP(7);
      }
    } else if (w == 11) {
      __builtin_amdgcn_s_setprio(3);
      ST128(SV, orec + lane * 16);
      if (lane < 8) ST128(SV, orec + 1024 + lane * 16);
      const u64 tag = load_tag(ws);
      const unsigned taghi = (unsigned)(tag >> 16), taglo = (unsigned)(tag << 16), tg = (unsigned)tag;
      const bool isR = i < 8;
      const int ln = g * 8 + i, rb = ln >> 4, pg = lane >> 3, q4 = lane & 7, col = 32 * (ln & 15) + 4 * q4;
      v2u xv = {0u, 0u};
      if (isR) xv = __builtin_amdgcn_raw_buffer_load_b64(rx, (rb * DM + col) * 2, 0, 0);
      DRAIN();
      lds_set(FL + F_CL, tg);
      STAMP(3);
      lds_wait(FL + F_VL + (lane < ntile ? lane : 0), tg);
      if (lane == 0) ST64(hint(taglo, taghi), WS_HA + (g * 32 + i) * 128);
      STAMPE(4);
      if (isR) {
        const rsrc_t ro = mk(out, NB * DM * 2u);
        unsigned pf = __builtin_amdgcn_raw_buffer_load_b32(ro, 0, 0, AUX_SC1);
        asm volatile("" :: "v"(pf));
        v2u hb;
        GSPIN {
          CBAR();
          hb = LD64(WS_HB + ((lane & 7) * 4 + ((ln >> 2) & 3)) * 128, 0);
          if (__all(hint_ok(hb, taglo, taghi))) break;
          __builtin_amdgcn_s_sleep(2);
        }
        STAMP(5);
        wait_age(hb.x, DLY3 + 20);
        v2u hv;
        GSPIN {
          CBAR();
          hv = LD64(WS_HP + (pg * NB + rb) * 1024 + col * 2, 0);
          if (__all(max(hv.x, hv.y) != SENT)) break;
        }
        STAMP(6);
        float h[4] = {blo(hv.x), bhi(hv.x), blo(hv.y), bhi(hv.y)};
#pragma unroll
        for (int d = 0; d < 4; ++d) { h[d] += dpp<ROR8>(h[d]); h[d] = x32_sum(x16_sum(h[d])); }
        {
          const unsigned a0 = pk(h[0], h[1]), a1 = pk(h[2], h[3]);
          const unsigned h0 = pk(blo(xv.x) + blo(a0), bhi(xv.x) + bhi(a0)), h1 = pk(blo(xv.y) + blo(a1), bhi(xv.y) + bhi(a1));
          h[0] = blo(h0); h[1] = bhi(h0); h[2] = blo(h1); h[3] = bhi(h1);
        }
        unsigned po = WS_OP + pg * 8192 + ln * 128 + q4 * 16, po3 = pg < 4 ? po + 24 * 8192 : po, oo = (rb * DM + col) * 2;
        PINV(po); PINV(po3); PINV(oo); PINV(h[0]); PINV(h[1]); PINV(h[2]); PINV(h[3]);
        v2u hc;
        GSPIN {
          CBAR();
          hc = LD64(WS_HC + (lane < NDCU ? lane : 0) * 128, 0);
          if (__all(hint_ok(hc, taglo, taghi))) break;
        }
        wait_age(hc.x, DLY5);
        STAMP(7);
        v4u pv[4];
        GSPIN {
          CBAR();
#pragma unroll
          for (int t = 0; t < 3; ++t) pv[t] = LD128(po, t * 8 * 8192);
          pv[3] = LD128(po3, 0);
          if (__all(okv(pv[0]) & okv(pv[1]) & okv(pv[2]) & okv(pv[3]))) break;
          RETRY();
        }
        STAMP(8);
        float y[4];
#pragma unroll
        for (int d = 0; d < 4; ++d) {
          float t = (__uint_as_float(pv[0][d]) + __uint_as_float(pv[1][d])) + (__uint_as_float(pv[2][d]) + (pg < 4 ? __uint_as_float(pv[3][d]) : 0.f));
          t += dpp<ROR8>(t);
          y[d] = x32_sum(x16_sum(t));
        }
        const unsigned y0 = pk(y[0], y[1]), y1 = pk(y[2], y[3]);
        if (lane < 8) __builtin_amdgcn_raw_buffer_store_b64(v2u{pk(h[0] + blo(y0), h[1] + bhi(y0)), pk(h[2] + blo(y1), h[3] + bhi(y1))}, ro, oo, 0, AUX_SC1);
        if (ln == 0 && lane == 0) ST64((v2u{(unsigned)tag, (unsigned)(tag >> 32)}), WS_EPOCH);
        STAMPE(9);
      }
    }
    PDUMP();
    return;
  }

  const int jq = i - 24;
  const bool isC = jq < 4;
  const int dI = g + 8 * (jq - 4);
  if (!isC && dI >= NDCU) { PDUMP(); return; }
  const u64 tag = load_tag(ws);
  const unsigned taghi = (unsigned)(tag >> 16), taglo = (unsigned)(tag << 16), tg = (unsigned)tag;
#define GATHER_H() do { \
    unsigned klo = 0x00003F80u, khi = 0x3F800000u, ho = WS_HP + w * 256 + lane * 4, oa = L_OA + w * 256 + lane * 4; \
    unsigned hp[8] = {0u, 0u, 0u, 0u, 0u, 0u, 0u, 0u}; \
    float x0 = blo(xp), x1 = bhi(xp); \
    PINS(klo); PINS(khi); PINV(oa); PINV(x0); PINV(x1); \
    ask8(hp, (unsigned)(size_t)(FL + F_GO + (lane & 7)), tg, ho, mkd(ws, WS_END)); \
    if (!__all(max(max(max(hp[0], hp[1]), max(hp[2], hp[3])), max(max(hp[4], hp[5]), max(hp[6], hp[7]))) != SENT)) { \
      GSPIN { \
        RETRY(); \
        CBAR(); \
        _Pragma("unroll") for (int j = 0; j < 8; ++j) hp[j] = LD32(ho, j * 4096); \
        if (__all(max(max(max(hp[0], hp[1]), max(hp[2], hp[3])), max(max(hp[4], hp[5]), max(hp[6], hp[7]))) != SENT)) break; \
      } \
      DRAIN(); \
    } \
    STAMP(7); \
    float a0 = 0.f, a1 = 0.f, a2 = 0.f, a3 = 0.f; \
    _Pragma("unroll") for (int j = 0; j < 4; ++j) { a0 = dot2(hp[j], klo, a0); a1 = dot2(hp[j], khi, a1); a2 = dot2(hp[4 + j], klo, a2); a3 = dot2(hp[4 + j], khi, a3); } \
    const unsigned t0 = pk(a0 + a2, a1 + a3); \
    *(LAS unsigned*)(L + oa) = pk(x0 + blo(t0), x1 + bhi(t0)); \
  } while (0)
  unsigned xp = 0;
  if (!isC) xp = __builtin_amdgcn_raw_buffer_load_b32(rx, w * 256 + lane * 4, 0, 0);
  if (w < 8) {
    const unsigned stride = isC ? DM * 2 : FF * 2;
    const GAS unsigned char* p = isC ? (const GAS unsigned char*)wo + (size_t)(512 * g) * (DM * 2) + (128 * jq + 8 * r16) * 2
                                     : (const GAS unsigned char*)(r16 < 8 ? wg : wu) + (64 * dI + 8 * (r16 & 7)) * 2;
    p += (size_t)(64 * w + 8 * gq) * stride;
    unsigned aa = L_OA + (r16 >> 2) * 1024 + w * 128 + gq * 16, pa = L_PART + w * 2048 + gq * 512 + r16 * 32;
    PINV(aa); PINV(pa);
    v4u src[16], bt[16];
    until(tstart, isC ? TC0 : TD0);
#pragma unroll
    for (int j = 0; j < WPRE; ++j) src[j] = WNT ? __builtin_nontemporal_load((const GAS v4u*)(p + ((j >> 3) * 32 + (j & 7)) * stride)) : *(const GAS v4u*)(p + ((j >> 3) * 32 + (j & 7)) * stride);
    lds_wait(FL + F_QPUB + (lane & 3), tg);
#pragma unroll
    for (int j = WPRE; j < 16; ++j) src[j] = WNT ? __builtin_nontemporal_load((const GAS v4u*)(p + ((j >> 3) * 32 + (j & 7)) * stride)) : *(const GAS v4u*)(p + ((j >> 3) * 32 + (j & 7)) * stride);
    SCHED();
    STAMP(4);
    tr8(src, bt);
    tr8(src + 8, bt + 8);
    if (!isC) {
      const v4u ones = {ONE2, ONE2, ONE2, ONE2};
      float cs[8];
#pragma unroll
      for (int e = 0; e < 8; ++e) cs[e] = mfma(ones, bt[8 + e], mfma(ones, bt[e], Z4))[0];
      if (lane < 16) {
        *(LAS f32x4*)(L + L_SS + w * 512 + lane * 32) = f32x4{cs[0], cs[1], cs[2], cs[3]};
        *(LAS f32x4*)(L + L_SS + w * 512 + lane * 32 + 16) = f32x4{cs[4], cs[5], cs[6], cs[7]};
      }
      lds_set(FL + F_SR + w, tg);
    }
    DRAIN();
    STAMP(5);
    if (w == 0) {
      if (isC) {
        const int hl = lane < 24 ? lane : 0;
        watch(rws, WS_HA + (g * 32 + hl) * 128, taglo, taghi, (LAS v2u*)(L + L_HINT + hl * 8), tg);
      } else {
        watch8(rws, WS_HB + (lane & 31) * 128, taglo, taghi, DLY3, FL + F_GO, tg, lane);
      }
      STAMP(10);
      DRAIN();
    } else if (w == 1 && isC) {
      lds_wait(FL + F_SR + (lane & 7), tg);
      if (lane == 0) ST64(hint(taglo, taghi), WS_HB + (g * 4 + jq) * 128);
    }
    if (!isC) GATHER_H();
    WGBAR();
    STAMP(8);
    {
      const v4u a0 = *(const LAS v4u*)(L + aa), a1 = *(const LAS v4u*)(L + aa + 64);
#pragma unroll
      for (int h = 0; h < 2; ++h) {
        f32x4 acc[4];
#pragma unroll
        for (int e = 0; e < 4; ++e) acc[e] = mfma(a0, bt[4 * h + e], Z4);
#pragma unroll
        for (int e = 0; e < 4; ++e) acc[e] = mfma(a1, bt[8 + 4 * h + e], acc[e]);
        *(LAS f32x4*)(L + pa + 16 * h) = f32x4{acc[0][0], acc[1][0], acc[2][0], acc[3][0]};
      }
    }
    STAMP(9);
    WGBAR();
    if (!isC) WGBAR();
  } else if (isC) {
    const int v = w & 7, bb = v >> 1, head = 2 * (v & 1) + (lane >> 5), sg = gq & 1;
    unsigned ra = L_PART + (w & 3) * 512 + lane * 8, so = WS_HP + (g * NB + (w & 3)) * 1024 + jq * 256 + lane * 4;
    unsigned rd = WS_OREC + (g * 24 + bb * NSPL + sg) * OREC_SZ + head * 256 + r16 * 16, rs = WS_OREC + (g * 24 + bb * NSPL + sg) * OREC_SZ + 1024 + head * 8;
    unsigned oa = L_OA + bb * 1024 + head * 256 + r16 * 16 + sg * 8;
    PINV(ra); PINV(so); PINV(rd); PINV(rs); PINV(oa);
    if (w == 15) ST128(SV, WS_HP + (g * NB + gq) * 1024 + jq * 256 + r16 * 16);
    DRAIN();
    STAMP(5);
    {
      v2u e;
#pragma nounroll
      for (unsigned sp = 0; sp < SPIN_L; ++sp) { e = *(volatile LAS v2u*)(L + L_HINT + (bb * NSPL + (lane < NSPL ? lane : 0)) * 8); if (__all(e.x == tg)) break; }
      wait_age(e.y, DLY2);
    }
    STAMP(6);
    GSPIN {
      v4u od[3];
      v2u st[3];
      CBAR();
#pragma unroll
      for (int u = 0; u < 3; ++u) st[u] = LD64(rs, 2 * u * OREC_SZ);
      SCHED();
#pragma unroll
      for (int u = 0; u < 3; ++u) od[u] = LD128(rd, 2 * u * OREC_SZ);
      SCHED();
      lds_set(FL + F_SR + v, tg);
      SCHED();
      unsigned bad = max(max(max(st[0].x, st[0].y), max(st[1].x, st[1].y)), max(st[2].x, st[2].y));
      const float m0 = __uint_as_float(st[0].x), m1 = __uint_as_float(st[1].x), m2 = __uint_as_float(st[2].x);
      const float mg = x16_max(fmaxf(fmaxf(m0, m1), m2));
      float wt[3] = {__builtin_amdgcn_exp2f(m0 - mg), __builtin_amdgcn_exp2f(m1 - mg), __builtin_amdgcn_exp2f(m2 - mg)};
      const float inv = __builtin_amdgcn_rcpf(x16_sum(fmaf(wt[0], __uint_as_float(st[0].y), fmaf(wt[1], __uint_as_float(st[1].y), wt[2] * __uint_as_float(st[2].y)))));
#pragma unroll
      for (int u = 0; u < 3; ++u) { wt[u] *= inv; PINV(wt[u]); }
      PINV(bad);
      SCHED();
      f32x2 a[4];
#pragma unroll
      for (int u = 0; u < 3; ++u) {
        f32x2 t[4];
#pragma unroll
        for (int d = 0; d < 4; ++d) { t[d] = f32x2{blo(od[u][d]), bhi(od[u][d])}; PINV(t[d]); }
        bad = max(bad, max(max(od[u].x, od[u].y), max(od[u].z, od[u].w)));
#pragma unroll
        for (int d = 0; d < 4; ++d) a[d] = u == 0 ? t[d] * wt[0] : t[d] * wt[u] + a[d];
      }
      float r[4];
#pragma unroll
      for (int d = 0; d < 4; ++d) {
        const auto sw = __builtin_amdgcn_permlane16_swap(__float_as_uint(a[d >> 1][d & 1]), __float_as_uint(a[2 + (d >> 1)][d & 1]), false, false);
        r[d] = __uint_as_float(sw[0]) + __uint_as_float(sw[1]);
      }
      *(LAS v2u*)(L + oa) = v2u{pk(r[0], r[1]), pk(r[2], r[3])};
      STAMP(7);
      if (__all(bad != SENT)) break;
      RETRY();
    }
    DRAIN();
    STAMP(8);
    WGBAR();
    WGBAR();
    if (w >= 12) {
      f32x2 pv[8];
#pragma unroll
      for (int j = 0; j < 8; ++j) pv[j] = *(const LAS f32x2*)(L + ra + j * 2048);
      SCHED();
      const f32x2 t = ((pv[0] + pv[1]) + (pv[2] + pv[3])) + ((pv[4] + pv[5]) + (pv[6] + pv[7]));
      const unsigned vv = nosent(pk(t.x, t.y));
      const v4u o4 = {dppu<0x00>(vv), dppu<0x55>(vv), dppu<0xAA>(vv), dppu<0xFF>(vv)};
      if ((lane & 3) == 0) ST128(o4, so);
      STAMPE(9);
    }
  } else {
    unsigned ra = L_PART + (w & 3) * 512 + lane * 4, hw = L_HH + (w & 3) * 128 + lane * 2, ha = L_HH + (r16 >> 2) * 128 + gq * 16;
    unsigned so = WS_OP + dI * 8192 + gq * 2048 + (w & 7) * 256 + r16 * 16, sa = L_OA + (w & 3) * 1024 + lane * 16;
    PINV(ra); PINV(hw); PINV(ha); PINV(so); PINV(sa);
    ST128(SV, WS_OP + dI * 8192 + (w - 8) * 1024 + lane * 16);
    until(tstart, TWD);
    const rsrc_t rw = mk(wd, FF * DM * 2u);
    v2u src[16];
    v4u bt[8];
#pragma unroll
    for (int j = 0; j < 16; ++j) src[j] = __builtin_amdgcn_raw_buffer_load_b64(rw, (unsigned)(8 * gq + (j & 3)) * 1024 + (w - 8) * 128 + r16 * 8, (64 * dI + (j >> 3) * 32 + (j & 4)) * 1024, WNT ? AUX_NT : 0);
    SCHED();
    tr4(src, bt);
    tr4(src + 8, bt + 4);
    float sgs = 0.f, sus = 0.f;
    if (w >= 12) {
      lds_wait(FL + F_SR + (lane & 7), tg);
#pragma unroll
      for (int j = 0; j < 8; ++j) { sgs += *(const LAS float*)(L + L_SS + j * 512 + lane * 4); sus += *(const LAS float*)(L + L_SS + j * 512 + 256 + lane * 4); }
    }
    PINV(sgs); PINV(sus);
    DRAIN();
    STAMP(5);
    GATHER_H();
    WGBAR();
    if (w >= 12) {
      const f32x2 st = ln_stats(*(const LAS v4u*)(L + sa));
      float kg = st.y * -1.4426950408889634f, cg = st.x * sgs, cu = st.x * sus, ku = st.y;
      PINV(kg); PINV(cg); PINV(cu); PINV(ku);
      WGBAR();
      float gp[8], up[8];
#pragma unroll
      for (int j = 0; j < 8; ++j) { gp[j] = *(const LAS float*)(L + ra + j * 2048); up[j] = *(const LAS float*)(L + ra + j * 2048 + 256); }
      SCHED();
      const float gg = ((gp[0] + gp[1]) + (gp[2] + gp[3])) + ((gp[4] + gp[5]) + (gp[6] + gp[7]));
      const float uu = ((up[0] + up[1]) + (up[2] + up[3])) + ((up[4] + up[5]) + (up[6] + up[7]));
      const float hh = ku * (uu - cu) * __builtin_amdgcn_rcpf(1.f + __builtin_amdgcn_exp2f(kg * (gg - cg)));
      *(LAS u16*)(L + hw) = (u16)pk(hh, hh);
    } else {
      if (w == 8 && lane == 0) ST64(hint(taglo, taghi), WS_HC + dI * 128);
      WGBAR();
    }
    WGBAR();
    STAMP(8);
    const v4u a0 = *(const LAS v4u*)(L + ha), a1 = *(const LAS v4u*)(L + ha + 64);
    f32x4 acc[4];
#pragma unroll
    for (int e = 0; e < 4; ++e) acc[e] = mfma(a0, bt[e], Z4);
#pragma unroll
    for (int e = 0; e < 4; ++e) acc[e] = mfma(a1, bt[4 + e], acc[e]);
    ST128((v4u{nosent(__float_as_uint(acc[0][0])), nosent(__float_as_uint(acc[1][0])), nosent(__float_as_uint(acc[2][0])), nosent(__float_as_uint(acc[3][0]))}), so);
    STAMPE(9);
  }
  PDUMP();
}
}

extern "C" void kernel_launch(void* const* d_in, const int* in_sizes, int n_in, void* d_out, int out_size, void* d_ws, size_t ws_size, hipStream_t stream) {
  (void)in_sizes; (void)n_in; (void)out_size; (void)ws_size;
  fused_decode<<<dim3(256), dim3(1024), 0, stream>>>((const u16*)d_in[0], (const u16*)d_in[1], (const u16*)d_in[2], (const u16*)d_in[3], (const u16*)d_in[4],
                                                     (const u16*)d_in[6], (const u16*)d_in[7], (const u16*)d_in[8], (u16*)d_out, (unsigned char*)d_ws);
}
```

```cpp
#include <hip/hip_runtime.h>
#include <stdint.h>

#define PROF 0
#define TK0 80
#define KPRE 4
#define QDELTA 70
#define TC0 300
#define TD0 220
#define NEAR 1200
#define WMODE 2
#define QNT 0
#define K0NT 0
#define WPRE 0
#define TWD 470
#define DLY2 60
#define DLY3 90
#define DLY5 76

namespace {
typedef unsigned short u16;
typedef unsigned long long u64;
typedef unsigned v4u __attribute__((ext_vector_type(4)));
typedef unsigned v2u __attribute__((ext_vector_type(2)));
typedef int v4i __attribute__((ext_vector_type(4)));
typedef float f32x4 __attribute__((ext_vector_type(4)));
typedef float f32x2 __attribute__((ext_vector_type(2)));
typedef short bf16x8 __attribute__((ext_vector_type(8)));
typedef __bf16 bf16x2 __attribute__((ext_vector_type(2)));
#define GAS __attribute__((address_space(1)))
#define LAS __attribute__((address_space(3)))
typedef __amdgpu_buffer_rsrc_t rsrc_t;

constexpr int NB = 4, DM = 512, HKV = 8, SK = 8192, WIN = 2048, FF = 1792, NQ = 4096;
constexpr int NSPL = 6, NDCU = 28;
constexpr int AUX_NT = 2, AUX_SC1 = 16;
constexpr unsigned SENT = 0xFFFFFFFFu, ONE2 = 0x3F803F80u;
constexpr int WS_EPOCH = 0, WS_HB = 4096, WS_HC = WS_HB + 32 * 128, WS_HA = WS_HC + 32 * 128, WS_QG = WS_HA + 8 * 32 * 128;
constexpr int WS_OREC = WS_QG + 8 * 4 * 4 * 512 * 8, OREC_SZ = 1152;
constexpr int WS_HP = WS_OREC + 8 * 24 * OREC_SZ, WS_OP = WS_HP + 8 * 4 * 512 * 2, WS_END = WS_OP + NDCU * 8192;
constexpr int WS_PROF = 2 << 20;
constexpr int L_FLAG = 0, L_MW = 512, L_HINT = 512, L_QS = 1024, L_OA = L_QS + 1024, L_HH = L_OA + 4096, L_SS = L_HH + 512;
constexpr int L_QRED = L_SS + 4096, L_PART = L_QRED + 5120, L_USED = L_PART + 11 * 2048;
constexpr int L_TOTAL = 84 * 1024;
static_assert(L_USED <= L_TOTAL, "lds");
enum { F_QIS = 0, F_QS = 4, F_NEAR = 8, F_QPUB = 12, F_QR = 16, F_CL = 24, F_GO = 32, F_SR = 72, F_MW = 80, F_VL = 96, F_OPF = 112 };
constexpr int CODE_BYTES = 16896;
constexpr unsigned SPIN_G = 1u << 17, SPIN_L = 1u << 21;

__device__ __forceinline__ u64 rt() { return __builtin_amdgcn_s_memrealtime(); }
__device__ __forceinline__ rsrc_t mk(const void* p, unsigned bytes) { return __builtin_amdgcn_make_buffer_rsrc((void*)p, 0, bytes, 0x00020000); }
__device__ __forceinline__ f32x4 mfma(v4u a, v4u b, f32x4 c) { return __builtin_amdgcn_mfma_f32_16x16x32_bf16(__builtin_bit_cast(bf16x8, a), __builtin_bit_cast(bf16x8, b), c, 0, 0, 0); }
__device__ __forceinline__ unsigned pk(float lo, float hi) { return __builtin_bit_cast(unsigned, __builtin_convertvector(f32x2{lo, hi}, bf16x2)); }
__device__ __forceinline__ float blo(unsigned v) { return __builtin_bit_cast(float, v << 16); }
__device__ __forceinline__ float bhi(unsigned v) { return __builtin_bit_cast(float, v & 0xffff0000u); }
__device__ __forceinline__ unsigned plo(unsigned a, unsigned b) { return __builtin_amdgcn_perm(b, a, 0x05040100u); }
__device__ __forceinline__ unsigned phi(unsigned a, unsigned b) { return __builtin_amdgcn_perm(b, a, 0x07060302u); }
__device__ __forceinline__ unsigned nosent(unsigned v) { return v < 0xFFFFFFFEu ? v : 0xFFFFFFFEu; }
template <int C> __device__ __forceinline__ float dpp(float x) { return __builtin_bit_cast(float, __builtin_amdgcn_mov_dpp(__builtin_bit_cast(int, x), C, 0xf, 0xf, true)); }
template <int C> __device__ __forceinline__ unsigned dppu(unsigned x) { return (unsigned)__builtin_amdgcn_mov_dpp((int)x, C, 0xf, 0xf, true); }
constexpr int XOR1 = 0xB1, XOR2 = 0x4E, ROR4 = 0x124, ROR8 = 0x128;
__device__ __forceinline__ float x16_sum(float x) { auto s = __builtin_amdgcn_permlane16_swap(__float_as_uint(x), __float_as_uint(x), false, false); return __uint_as_float(s[0]) + __uint_as_float(s[1]); }
__device__ __forceinline__ float x32_sum(float x) { auto s = __builtin_amdgcn_permlane32_swap(__float_as_uint(x), __float_as_uint(x), false, false); return __uint_as_float(s[0]) + __uint_as_float(s[1]); }
__device__ __forceinline__ float x16_max(float x) { auto s = __builtin_amdgcn_permlane16_swap(__float_as_uint(x), __float_as_uint(x), false, false); return fmaxf(__uint_as_float(s[0]), __uint_as_float(s[1])); }
__device__ __forceinline__ float x32_max(float x) { auto s = __builtin_amdgcn_permlane32_swap(__float_as_uint(x), __float_as_uint(x), false, false); return fmaxf(__uint_as_float(s[0]), __uint_as_float(s[1])); }
__device__ __forceinline__ float row_sum(float x) { x += dpp<XOR1>(x); x += dpp<XOR2>(x); x += dpp<ROR4>(x); x += dpp<ROR8>(x); return x; }
__device__ __forceinline__ float row_max(float x) { x = fmaxf(x, dpp<XOR1>(x)); x = fmaxf(x, dpp<XOR2>(x)); x = fmaxf(x, dpp<ROR4>(x)); x = fmaxf(x, dpp<ROR8>(x)); return x; }
__device__ __forceinline__ float dot2(unsigned a, unsigned b, float c) { return __builtin_amdgcn_fdot2_f32_bf16(__builtin_bit_cast(bf16x2, a), __builtin_bit_cast(bf16x2, b), c, false); }
#define CBAR() asm volatile("" ::: "memory")
#define SCHED() __builtin_amdgcn_sched_barrier(0)
#define DRAIN() do { asm volatile("s_waitcnt vmcnt(0)" ::: "memory"); __builtin_amdgcn_s_waitcnt(0x0F70); } while (0)
#define PINV(x) asm volatile("" : "+v"(x))
#define PINS(x) asm volatile("" : "+s"(x))
#define WGBAR() do { SCHED(); asm volatile("s_waitcnt lgkmcnt(0)" ::: "memory"); __builtin_amdgcn_s_barrier(); CBAR(); SCHED(); } while (0)
#define GSPIN _Pragma("nounroll") for (unsigned sp = 0; sp < SPIN_G; ++sp)
#define LD32(o, so) __builtin_amdgcn_raw_buffer_load_b32(rws, (o), (so), AUX_SC1)
#define LD64(o, so) __builtin_amdgcn_raw_buffer_load_b64(rws, (o), (so), AUX_SC1)
#define LD128(o, so) __builtin_amdgcn_raw_buffer_load_b128(rws, (o), (so), AUX_SC1)
#define ST64(v, o) __builtin_amdgcn_raw_buffer_store_b64((v), rws, (o), 0, AUX_SC1)
#define ST128(v, o) __builtin_amdgcn_raw_buffer_store_b128((v), rws, (o), 0, AUX_SC1)
__device__ __forceinline__ bool okv(v4u r) { return max(max(r.x, r.y), max(r.z, r.w)) != SENT; }
__device__ __forceinline__ void lds_wait(volatile LAS unsigned* f, unsigned tg) {
  SCHED();
#pragma nounroll
  for (unsigned s = 0; s < SPIN_L; ++s) { if (__all(*f == tg)) break; }
  CBAR();
  SCHED();
}
__device__ __forceinline__ void lds_set(volatile LAS unsigned* f, unsigned tg) { CBAR(); *f = tg; CBAR(); }
__device__ __forceinline__ void until(u64 t0, unsigned ticks) { while (rt() - t0 < ticks) __builtin_amdgcn_s_sleep(1); }
__device__ __forceinline__ v2u hint(unsigned taglo, unsigned taghi) { return v2u{taglo | ((unsigned)rt() & 0xFFFFu), taghi}; }
__device__ __forceinline__ bool hint_ok(v2u h, unsigned taglo, unsigned taghi) { return (h.y == taghi) & ((h.x ^ taglo) < 65536u); }
__device__ __forceinline__ void wait_age(unsigned t16, unsigned dly) {
#pragma nounroll
  for (unsigned s = 0; s < 4096u; ++s) { if (__all((((unsigned)rt() - t16) & 0xFFFFu) >= dly)) break; }
}
__device__ __forceinline__ void watch(rsrc_t rws, unsigned ho, unsigned taglo, unsigned taghi, LAS v2u* slot, unsigned tg) {
  GSPIN {
    CBAR();
    const v2u h = LD64(ho, 0);
    const bool ok = hint_ok(h, taglo, taghi);
    if (ok) *slot = v2u{tg, h.x};
    CBAR();
    if (__all(ok)) break;
  }
}
__device__ __forceinline__ void watch8(rsrc_t rws, unsigned ho, unsigned taglo, unsigned taghi, unsigned dly, volatile LAS unsigned* go, unsigned tg, int lane) {
  v2u h;
  unsigned seen = 0, done = 0;
#pragma nounroll
  for (unsigned sp = 0; sp < SPIN_G && done != 0x11111111u; ++sp) {
    if (seen != ~0u) {
      CBAR();
      h = LD64(ho, 0);
      seen = (unsigned)__builtin_amdgcn_ballot_w64(hint_ok(h, taglo, taghi));
    }
    unsigned m = (unsigned)__builtin_amdgcn_ballot_w64((((unsigned)rt() - h.x) & 0xFFFFu) >= dly) & seen;
    m &= m >> 1; m &= m >> 2; m &= 0x11111111u;
    if (m != done) { if ((m >> (4 * (lane & 7))) & 1u) go[lane & 7] = tg; done = m; }
  }
  CBAR();
}
__device__ __forceinline__ u64 load_tag(const unsigned char* ws) {
  u64 e;
  asm volatile("s_load_dwordx2 %0, %1, 0x0\n\ts_waitcnt lgkmcnt(0)" : "=s"(e) : "s"(ws) : "memory");
  return (e + 1ull) & 0xFFFFFFFFFFFFull;
}
__device__ __forceinline__ void code_fetch(int part) {
  u64 pc0;
  asm volatile("s_getpc_b64 %0" : "=s"(pc0));
  const u64 a0 = (pc0 & ~127ull) + (u64)(part * 576 < CODE_BYTES - 576 ? part * 576 : CODE_BYTES - 576);
  unsigned t0, t1, t2, t3, t4, t5, t6, t7, t8;
  asm volatile("s_load_dword %0, %9, 0x0\n\ts_load_dword %1, %9, 0x40\n\ts_load_dword %2, %9, 0x80\n\ts_load_dword %3, %9, 0xc0\n\ts_load_dword %4, %9, 0x100\n\t"
               "s_load_dword %5, %9, 0x140\n\ts_load_dword %6, %9, 0x180\n\ts_load_dword %7, %9, 0x1c0\n\ts_load_dword %8, %9, 0x200\n\ts_waitcnt lgkmcnt(0)"
               : "=&s"(t0), "=&s"(t1), "=&s"(t2), "=&s"(t3), "=&s"(t4), "=&s"(t5), "=&s"(t6), "=&s"(t7), "=&s"(t8) : "s"(a0) : "memory");
}
__device__ __forceinline__ v4i mkd(const void* p, unsigned bytes) { return v4i{(int)(u64)p, (int)((u64)p >> 32) & 0xFFFF, (int)bytes, 0x00020000}; }
template <int OFF> __device__ __forceinline__ void ldnt(v4u& d, unsigned vo, v4i rs, unsigned so) {
  asm volatile("buffer_load_dwordx4 %0, %1, %2, %3 offen offset:%4 nt" : "=v"(d) : "v"(vo), "s"(rs), "s"(so), "i"(OFF) : "memory");
}
template <int LO, int HI> __device__ __forceinline__ void ldtile(v4u* d, unsigned vo, v4i rs, unsigned so, unsigned so2) {
  if constexpr (LO < HI) { ldnt<(LO & 3) * 1024>(d[LO], vo, rs, LO < 4 ? so : so2); ldtile<LO + 1, HI>(d, vo, rs, so, so2); }
}
__device__ __forceinline__ void ld4(v4u* d, unsigned vo, v4i rs, unsigned so, unsigned near) {
  asm volatile("s_cmp_lg_u32 %7, 0\n\ts_cbranch_scc1 1f\n\t"
               "buffer_load_dwordx4 %0, %4, %5, %6 offen nt\n\tbuffer_load_dwordx4 %1, %4, %5, %6 offen offset:1024 nt\n\t"
               "buffer_load_dwordx4 %2, %4, %5, %6 offen offset:2048 nt\n\tbuffer_load_dwordx4 %3, %4, %5, %6 offen offset:3072 nt\n\ts_branch 2f\n"
               "1:\n\tbuffer_load_dwordx4 %0, %4, %5, %6 offen\n\tbuffer_load_dwordx4 %1, %4, %5, %6 offen offset:1024\n\t"
               "buffer_load_dwordx4 %2, %4, %5, %6 offen offset:2048\n\tbuffer_load_dwordx4 %3, %4, %5, %6 offen offset:3072\n2:"
               : "=&v"(d[0]), "=&v"(d[1]), "=&v"(d[2]), "=&v"(d[3]) : "v"(vo), "s"(rs), "s"(so), "s"(near) : "scc", "memory");
}
__device__ __forceinline__ unsigned probe(const void* p) {
  u64 t0, t1;
  unsigned d;
  asm volatile("s_load_dword %2, %3, 0x0\n\ts_waitcnt lgkmcnt(0)\n\ts_memtime %0\n\ts_load_dword %2, %3, 0x100\n\ts_waitcnt lgkmcnt(0)\n\ts_memtime %1\n\ts_waitcnt lgkmcnt(0)"
               : "=&s"(t0), "=&s"(t1), "=&s"(d) : "s"(p) : "memory");
  return (unsigned)(t1 - t0);
}
__device__ __forceinline__ void ldnt2(v2u& d, unsigned vo, v4i rs, unsigned so) {
  if (QNT) asm volatile("buffer_load_dwordx2 %0, %1, %2, %3 offen nt" : "=v"(d) : "v"(vo), "s"(rs), "s"(so) : "memory");
  else asm volatile("buffer_load_dwordx2 %0, %1, %2, %3 offen" : "=v"(d) : "v"(vo), "s"(rs), "s"(so) : "memory");
}
__device__ __forceinline__ void ldp4(v4u& d, unsigned vo, v4i rs) {
  asm volatile("buffer_load_dwordx4 %0, %1, %2, 0 offen" : "=v"(d) : "v"(vo), "s"(rs) : "memory");
}
#define VMWAIT2(n, a, b) asm volatile("s_waitcnt vmcnt(%2)" : "+v"(a), "+v"(b) : "i"(n) : "memory")
__device__ __forceinline__ void ask8(unsigned (&hp)[8], unsigned go, unsigned tg, unsigned ho, v4i rs) {
  unsigned t, done, n, cnt;
#define ASK1(j, off) "s_bitcmp1_b32 %10, " #j "\n\ts_cbranch_scc0 2" #j "f\n\ts_movk_i32 %11, " #off "\n\tbuffer_load_dword %" #j ", %14, %15, %11 offen sc1\n2" #j ":\n\t"
  asm volatile("s_mov_b32 %9, 0\n\ts_mov_b32 %11, 0x200000\n"
               "1:\n\tds_read_b32 %8, %12\n\ts_sub_u32 %11, %11, 1\n\ts_cbranch_scc1 3f\n\ts_waitcnt lgkmcnt(0)\n\t"
               "v_cmp_eq_u32 vcc, %13, %8\n\ts_andn2_b32 %10, vcc_lo, %9\n\ts_and_b32 %10, %10, 0xff\n\ts_cbranch_scc0 1b\n\t"
               ASK1(0, 0) ASK1(1, 0x1000) ASK1(2, 0x2000) ASK1(3, 0x3000) ASK1(4, 0x4000) ASK1(5, 0x5000) ASK1(6, 0x6000) ASK1(7, 0x7000)
               "s_mov_b32 %11, 0x200000\n\ts_or_b32 %9, %9, %10\n\ts_cmp_lg_u32 %9, 0xff\n\ts_cbranch_scc1 1b\n"
               "3:\n\ts_waitcnt vmcnt(0) lgkmcnt(0)"
               : "+v"(hp[0]), "+v"(hp[1]), "+v"(hp[2]), "+v"(hp[3]), "+v"(hp[4]), "+v"(hp[5]), "+v"(hp[6]), "+v"(hp[7]), "=&v"(t), "=&s"(done), "=&s"(n), "=&s"(cnt)
               : "v"(go), "s"(tg), "v"(ho), "s"(rs) : "vcc", "scc", "memory");
#undef ASK1
}
__device__ __forceinline__ f32x2 ln_stats(v4u hv) {
  const float c0 = blo((unsigned)__builtin_amdgcn_readfirstlane(hv[0]));
  float t[8];
#pragma unroll
  for (int d = 0; d < 4; ++d) { t[2 * d] = blo(hv[d]) - c0; t[2 * d + 1] = bhi(hv[d]) - c0; }
  float s1 = ((t[0] + t[1]) + (t[2] + t[3])) + ((t[4] + t[5]) + (t[6] + t[7]));
  float s2 = (fmaf(t[1], t[1], t[0] * t[0]) + fmaf(t[3], t[3], t[2] * t[2])) + (fmaf(t[5], t[5], t[4] * t[4]) + fmaf(t[7], t[7], t[6] * t[6]));
  s1 += dpp<XOR1>(s1); s2 += dpp<XOR1>(s2);
  s1 += dpp<XOR2>(s1); s2 += dpp<XOR2>(s2);
  s1 += dpp<ROR4>(s1); s2 += dpp<ROR4>(s2);
  s1 += dpp<ROR8>(s1); s2 += dpp<ROR8>(s2);
  s1 = x16_sum(s1); s2 = x16_sum(s2);
  s1 = x32_sum(s1); s2 = x32_sum(s2);
  const float d = s1 * (1.f / DM), var = fmaxf(fmaf(-d, d, s2 * (1.f / DM)), 0.f);
  return f32x2{c0 + d, __builtin_amdgcn_rsqf(var + 1e-5f)};
}
#define STAMP(k) do {} while (0)
#define STAMPE(k) do {} while (0)
#define PDUMP() do {} while (0)
#define RETRY() do {} while (0)

__device__ __forceinline__ void tr4(const v2u* s, v4u* o) {
#pragma unroll
  for (int m = 0; m < 4; ++m) {
    o[0][m] = plo(s[2 * m].x, s[2 * m + 1].x); o[1][m] = phi(s[2 * m].x, s[2 * m + 1].x);
    o[2][m] = plo(s[2 * m].y, s[2 * m + 1].y); o[3][m] = phi(s[2 * m].y, s[2 * m + 1].y);
  }
#pragma unroll
  for (int e = 0; e < 4; ++e) PINV(o[e]);
}
__device__ __forceinline__ void tr8(const v4u* s, v4u* o) {
#pragma unroll
  for (int m = 0; m < 4; ++m)
#pragma unroll
    for (int d = 0; d < 4; ++d) { o[2 * d][m] = plo(s[2 * m][d], s[2 * m + 1][d]); o[2 * d + 1][m] = phi(s[2 * m][d], s[2 * m + 1][d]); }
#pragma unroll
  for (int e = 0; e < 8; ++e) PINV(o[e]);
}

__global__ void __launch_bounds__(1024) fused_decode(const u16* __restrict__ x, const u16* __restrict__ kc, const u16* __restrict__ vc,
                                                     const u16* __restrict__ wq, const u16* __restrict__ wo, const u16* __restrict__ wg,
                                                     const u16* __restrict__ wu, const u16* __restrict__ wd, u16* __restrict__ out, unsigned char* ws) {
  const u64 tstart = rt();
  __shared__ __attribute__((aligned(16))) unsigned char smem[L_TOTAL];
  LAS unsigned char* L = (LAS unsigned char*)smem;
  volatile LAS unsigned* FL = (volatile LAS unsigned*)(L + L_FLAG);
  const int lane = threadIdx.x & 63;
  int w = (__builtin_amdgcn_readfirstlane(threadIdx.x >> 6) + 11) & 15;
  const int c = blockIdx.x, g = c & 7, i = c >> 3;
  if (w == 11) code_fetch(i);
  const int gq = lane >> 4, r16 = lane & 15;
  const int b = (i * 43) >> 8, s = i - b * NSPL;
  const int ntile = s < 4 ? 11 : 10, tile0 = 11 * s - (s > 4 ? 1 : 0);
  const int cw = w - 12, rq = i & 3, cq = i >> 2, R0 = 128 * rq + 32 * cw;
  asm volatile("" :: "s"(x), "s"(kc), "s"(vc), "s"(wq), "s"(wo), "s"(wg), "s"(wu), "s"(wd), "s"(out), "s"(ws));
  v4u kf[8], vf[8], xa, xr;
  v2u wsrc[8];
  const unsigned kvbase = ((unsigned)(b * HKV + g) * SK + (SK - WIN) + 32u * tile0 + 16u * w) * 256u, kvbase2 = kvbase + ntile * 4096u;
  const v4i rk = mkd(kc, NB * HKV * SK * 256u), rv = mkd(vc, NB * HKV * SK * 256u);
  if (w >= 12) {
    __builtin_amdgcn_s_setprio(3);
    const v4i rw = mkd(wq, DM * NQ * 2u), rxd = mkd(x, NB * DM * 2u);
    const unsigned o = (unsigned)(8 * gq) * (NQ * 2) + r16 * 8, so = R0 * (NQ * 2) + (512 * g + 64 * cq) * 2;
#pragma unroll
    for (int j = 0; j < 8; ++j) ldnt2(wsrc[j], o, rw, so + j * (NQ * 2));
    ldp4(xa, (r16 >> 2) * 1024 + (R0 + 8 * gq) * 2, rxd);
    ldp4(xr, cw * 1024 + lane * 16, rxd);
  } else if (i < 24 && w < ntile) {
    __builtin_amdgcn_s_setprio(2);
    until(tstart, TK0);
    ld4(kf, r16 * 64 + gq * 16, rk, kvbase, K0NT ? 0u : 1u);
  }
  STAMP(1);
  PINS(w);
  const rsrc_t rws = mk(ws, WS_END), rx = mk(x, NB * DM * 2u);
  const f32x4 Z4 = {0.f, 0.f, 0.f, 0.f};
  const v4u SV = {SENT, SENT, SENT, SENT};
  const unsigned orec = WS_OREC + (g * 24 + i) * OREC_SZ;

  if (__builtin_expect(w >= 12, 1)) {
    const u64 tag = load_tag(ws);
    const unsigned tg = (unsigned)tag, qhi = (unsigned)(tag >> 8), qlo = (unsigned)(tag << 24);
    asm volatile("s_waitcnt vmcnt(0)" : "+v"(wsrc[0]), "+v"(wsrc[1]), "+v"(wsrc[2]), "+v"(wsrc[3]), "+v"(wsrc[4]), "+v"(wsrc[5]), "+v"(wsrc[6]), "+v"(wsrc[7]), "+v"(xa), "+v"(xr) :: "memory");
    {
      v4u bt[4];
      tr4(wsrc, bt);
      if ((lane & 3) == 1) xa = v4u{ONE2, ONE2, ONE2, ONE2};
      f32x4 qp, cs;
#pragma unroll
      for (int e = 0; e < 4; ++e) { const f32x4 d = mfma(xa, bt[e], Z4); qp[e] = d[0]; cs[e] = d[1]; }
      *(LAS f32x4*)(L + L_QRED + cw * 1024 + lane * 16) = qp;
      if (lane < 16) *(LAS f32x4*)(L + L_QRED + 4096 + cw * 256 + lane * 16) = cs;
    }
    lds_set(FL + F_QR + cw, tg);
    f32x2 xst = ln_stats(xr);
    PINV(xst);
    STAMP(2);
    lds_wait(FL + F_QR + (lane & 3), tg);
    {
      float a[4], cs[4];
#pragma unroll
      for (int j = 0; j < 4; ++j) { a[j] = *(const LAS float*)(L + L_QRED + j * 1024 + cw * 256 + lane * 4); cs[j] = *(const LAS float*)(L + L_QRED + 4096 + j * 256 + lane * 4); }
      const float q = xst.y * (((a[0] + a[1]) + (a[2] + a[3])) - xst.x * ((cs[0] + cs[1]) + (cs[2] + cs[3])));
      ST64((v2u{qlo | ((__float_as_uint(q) + 0x80u) >> 8), qhi}), WS_QG + (((g * NB + cw) * 4 + rq) * 512 + 64 * cq + lane) * 8);
    }
    lds_set(FL + F_QPUB + cw, tg);
    STAMP(3);
    if (i < 24) {
      until(rt(), QDELTA);
      const unsigned qo = WS_QG + ((g * NB + b) * 4 * 512 + 2 * (cw * 64 + lane)) * 8;
      v4u qg[4];
      GSPIN {
        CBAR();
        bool ok = true;
#pragma unroll
        for (int j = 0; j < 4; ++j) qg[j] = LD128(qo, j * 4096);
        SCHED();
        lds_set(FL + F_QIS + cw, tg);
        SCHED();
#pragma unroll
        for (int j = 0; j < 4; ++j) ok &= (qg[j].y == qhi) & (qg[j].w == qhi) & ((qg[j].x ^ qlo) < (1u << 24)) & ((qg[j].z ^ qlo) < (1u << 24));
        if (__all(ok)) break;
        RETRY();
      }
      const float q0 = (__uint_as_float(qg[0].x << 8) + __uint_as_float(qg[1].x << 8)) + (__uint_as_float(qg[2].x << 8) + __uint_as_float(qg[3].x << 8));
      const float q1 = (__uint_as_float(qg[0].z << 8) + __uint_as_float(qg[1].z << 8)) + (__uint_as_float(qg[2].z << 8) + __uint_as_float(qg[3].z << 8));
      *(LAS unsigned*)(L + L_QS + (cw * 64 + lane) * 4) = pk(q0, q1);
      lds_set(FL + F_QS + cw, tg);
      STAMP(4);
      PDUMP();
      return;
    }
    __builtin_amdgcn_s_setprio(0);
  } else if (i < 24) {
    if (w < ntile) {
      const unsigned tg = (unsigned)load_tag(ws);
      if (w < 2) {
        const unsigned cy = probe((const unsigned char*)(w == 0 ? kc : vc) + kvbase2 + 2048);
        if (cy < NEAR) lds_set(FL + F_NEAR + w, tg);
      }
      lds_wait(FL + F_QPUB + (lane & 3), tg);
      static_assert(KPRE == 4, "");
      ld4(kf + 4, r16 * 64 + gq * 16, rk, kvbase2, __builtin_amdgcn_readfirstlane(FL[F_NEAR] == tg));
      __builtin_amdgcn_s_setprio(0);
      lds_wait(FL + F_QIS + (lane & 3), tg);
      {
        const unsigned near = __builtin_amdgcn_readfirstlane(FL[F_NEAR + 1] == tg);
        ld4(vf, lane * 16, rv, kvbase, near);
        ld4(vf + 4, lane * 16, rv, kvbase2, near);
      }
      STAMP(2);
      lds_wait(FL + (lane < 4 ? F_QS + lane : F_CL), tg);
      asm volatile("s_waitcnt vmcnt(8)" : "+v"(kf[0]), "+v"(kf[1]), "+v"(kf[2]), "+v"(kf[3]), "+v"(kf[4]), "+v"(kf[5]), "+v"(kf[6]), "+v"(kf[7]) :: "memory");
      STAMP(3);
      const v4u qf = *(const LAS v4u*)(L + L_QS + r16 * 64 + gq * 16);
      float sc[8];
#pragma unroll
      for (int t = 0; t < 8; ++t) {
        const f32x4 d = mfma(kf[t], qf, Z4);
        sc[t] = (dpp<0x00>(d[0]) + dpp<0x55>(d[1])) + (dpp<0xAA>(d[2]) + dpp<0xFF>(d[3]));
      }
      float m = fmaxf(fmaxf(fmaxf(sc[0], sc[1]), fmaxf(sc[2], sc[3])), fmaxf(fmaxf(sc[4], sc[5]), fmaxf(sc[6], sc[7])));
      m = x32_max(x16_max(m));
      constexpr float C2 = 0.08838834764831845f * 1.4426950408889634f;
      const float ms = m * C2;
      float p[8], l = 0.f;
#pragma unroll
      for (int t = 0; t < 8; ++t) { p[t] = __builtin_amdgcn_exp2f(fmaf(sc[t], C2, -ms)); l += p[t]; }
      l = x32_sum(x16_sum(l));
      if ((lane & 0x33) == 0) *(LAS f32x2*)(L + L_MW + ((lane >> 2) * 12 + w) * 8) = f32x2{ms, l};
      lds_set(FL + F_MW + w, tg);
      v4u pf = {pk(p[0], p[1]), pk(p[2], p[3]), pk(p[4], p[5]), pk(p[6], p[7])};
      unsigned pa = L_PART + w * 2048 + gq * 512 + r16 * 32;
      PINV(pf); PINV(pa);
      STAMP(4);
      v4u vt[8];
#pragma unroll
      for (int mm = 0; mm < 4; ++mm) {
        if (mm == 0) VMWAIT2(6, vf[0], vf[1]);
        if (mm == 1) VMWAIT2(4, vf[2], vf[3]);
        if (mm == 2) { lds_set(FL + F_VL + w, tg); VMWAIT2(2, vf[4], vf[5]); }
        if (mm == 3) { VMWAIT2(0, vf[6], vf[7]); STAMP(5); }
#pragma unroll
        for (int d = 0; d < 4; ++d) { vt[2 * d][mm] = plo(vf[2 * mm][d], vf[2 * mm + 1][d]); vt[2 * d + 1][mm] = phi(vf[2 * mm][d], vf[2 * mm + 1][d]); }
        if (mm < 3) {
#pragma unroll
          for (int e = 0; e < 8; ++e) PINV(vt[e]);
        }
        SCHED();
      }
      float o[8];
#pragma unroll
      for (int e = 0; e < 8; ++e) o[e] = mfma(pf, vt[e], Z4)[0];
      *(LAS f32x4*)(L + pa) = f32x4{o[0], o[1], o[2], o[3]};
      *(LAS f32x4*)(L + pa + 16) = f32x4{o[4], o[5], o[6], o[7]};
      lds_set(FL + F_OPF + w, tg);
      STAMP(6);
      if (w < 4) {
        const int jl = r16 < ntile ? r16 : 0;
        lds_wait(FL + F_MW + jl, tg);
        const f32x2 ml = *(const LAS f32x2*)(L + L_MW + (w * 12 + jl) * 8);
        const float mg = row_max(ml.x);
        const float wt = r16 < ntile ? __builtin_amdgcn_exp2f(ml.x - mg) : 0.f;
        const float lt = row_sum(wt * ml.y);
        float wj[11];
#pragma unroll
        for (int j = 0; j < 11; ++j) { wj[j] = __builtin_bit_cast(float, __builtin_amdgcn_readlane(__builtin_bit_cast(int, wt), j)); PINS(wj[j]); }
        if (lane == 0) ST64((v2u{nosent(__float_as_uint(mg)), nosent(__float_as_uint(lt))}), orec + 1024 + w * 8);
        unsigned ra = L_PART + w * 512 + lane * 8, rl = ra + (ntile - 1) * 2048, so = orec + w * 256 + lane * 4;
        PINV(ra); PINV(rl); PINV(so);
        lds_wait(FL + F_OPF + jl, tg);
        f32x2 pv[11];
#pragma unroll
        for (int j = 0; j < 10; ++j) pv[j] = *(const LAS f32x2*)(L + ra + j * 2048);
        pv[10] = *(const LAS f32x2*)(L + rl);
        SCHED();
        f32x2 a = pv[0] * wj[0], a2 = pv[1] * wj[1];
#pragma unroll
        for (int j = 2; j < 10; j += 2) { a += pv[j] * wj[j]; a2 += pv[j + 1] * wj[j + 1]; }
        a += pv[10] * wj[10];
        a += a2;
        const unsigned v = nosent(pk(a.x, a.y));
        const v4u o4 = {dppu<0x00>(v), dppu<0x55>(v), dppu<0xAA>(v), dppu<0xFF>(v)};
        if ((lane & 3) == 0) ST128(o4, so);
        STAMP(7);
      }
    } else if (w == 11) {
      __builtin_amdgcn_s_setprio(3);
      ST128(SV, orec + lane * 16);
      if (lane < 8) ST128(SV, orec + 1024 + lane * 16);
      const u64 tag = load_tag(ws);
      const unsigned taghi = (unsigned)(tag >> 16), taglo = (unsigned)(tag << 16), tg = (unsigned)tag;
      const bool isR = i < 8;
      const int ln = g * 8 + i, rb = ln >> 4, pg = lane >> 3, q4 = lane & 7, col = 32 * (ln & 15) + 4 * q4;
      v2u xv = {0u, 0u};
      if (isR) xv = __builtin_amdgcn_raw_buffer_load_b64(rx, (rb * DM + col) * 2, 0, 0);
      DRAIN();
      lds_set(FL + F_CL, tg);
      STAMP(3);
      lds_wait(FL + F_VL + (lane < ntile ? lane : 0), tg);
      if (lane == 0) ST64(hint(taglo, taghi), WS_HA + (g * 32 + i) * 128);
      STAMPE(4);
      if (isR) {
        const rsrc_t ro = mk(out, NB * DM * 2u);
        unsigned pf = __builtin_amdgcn_raw_buffer_load_b32(ro, 0, 0, AUX_SC1);
        asm volatile("" :: "v"(pf));
        v2u hb;
        GSPIN {
          CBAR();
          hb = LD64(WS_HB + ((lane & 7) * 4 + ((ln >> 2) & 3)) * 128, 0);
          if (__all(hint_ok(hb, taglo, taghi))) break;
          __builtin_amdgcn_s_sleep(2);
        }
        STAMP(5);
        wait_age(hb.x, DLY3 + 20);
        v2u hv;
        GSPIN {
          CBAR();
          hv = LD64(WS_HP + (pg * NB + rb) * 1024 + col * 2, 0);
          if (__all(max(hv.x, hv.y) != SENT)) break;
        }
        STAMP(6);
        float h[4] = {blo(hv.x), bhi(hv.x), blo(hv.y), bhi(hv.y)};
#pragma unroll
        for (int d = 0; d < 4; ++d) { h[d] += dpp<ROR8>(h[d]); h[d] = x32_sum(x16_sum(h[d])); }
        {
          const unsigned a0 = pk(h[0], h[1]), a1 = pk(h[2], h[3]);
          const unsigned h0 = pk(blo(xv.x) + blo(a0), bhi(xv.x) + bhi(a0)), h1 = pk(blo(xv.y) + blo(a1), bhi(xv.y) + bhi(a1));
          h[0] = blo(h0); h[1] = bhi(h0); h[2] = blo(h1); h[3] = bhi(h1);
        }
        unsigned po = WS_OP + pg * 8192 + ln * 128 + q4 * 16, po3 = pg < 4 ? po + 24 * 8192 : po, oo = (rb * DM + col) * 2;
        PINV(po); PINV(po3); PINV(oo); PINV(h[0]); PINV(h[1]); PINV(h[2]); PINV(h[3]);
        v2u hc;
        GSPIN {
          CBAR();
          hc = LD64(WS_HC + (lane < NDCU ? lane : 0) * 128, 0);
          if (__all(hint_ok(hc, taglo, taghi))) break;
        }
        wait_age(hc.x, DLY5);
        STAMP(7);
        v4u pv[4];
        GSPIN {
          CBAR();
#pragma unroll
          for (int t = 0; t < 3; ++t) pv[t] = LD128(po, t * 8 * 8192);
          pv[3] = LD128(po3, 0);
          if (__all(okv(pv[0]) & okv(pv[1]) & okv(pv[2]) & okv(pv[3]))) break;
          RETRY();
        }
        STAMP(8);
        float y[4];
#pragma unroll
        for (int d = 0; d < 4; ++d) {
          float t = (__uint_as_float(pv[0][d]) + __uint_as_float(pv[1][d])) + (__uint_as_float(pv[2][d]) + (pg < 4 ? __uint_as_float(pv[3][d]) : 0.f));
          t += dpp<ROR8>(t);
          y[d] = x32_sum(x16_sum(t));
        }
        const unsigned y0 = pk(y[0], y[1]), y1 = pk(y[2], y[3]);
        if (lane < 8) __builtin_amdgcn_raw_buffer_store_b64(v2u{pk(h[0] + blo(y0), h[1] + bhi(y0)), pk(h[2] + blo(y1), h[3] + bhi(y1))}, ro, oo, 0, AUX_SC1);
        if (ln == 0 && lane == 0) ST64((v2u{(unsigned)tag, (unsigned)(tag >> 32)}), WS_EPOCH);
        STAMPE(9);
      }
    }
    PDUMP();
    return;
  }

  const int jq = i - 24;
  const bool isC = jq < 4;
  const int dI = g + 8 * (jq - 4);
  if (!isC && dI >= NDCU) { PDUMP(); return; }
  const u64 tag = load_tag(ws);
  const unsigned taghi = (unsigned)(tag >> 16), taglo = (unsigned)(tag << 16), tg = (unsigned)tag;
#define GATHER_H() do { \
    unsigned klo = 0x00003F80u, khi = 0x3F800000u, ho = WS_HP + w * 256 + lane * 4, oa = L_OA + w * 256 + lane * 4; \
    unsigned hp[8] = {0u, 0u, 0u, 0u, 0u, 0u, 0u, 0u}; \
    float x0 = blo(xp), x1 = bhi(xp); \
    PINS(klo); PINS(khi); PINV(oa); PINV(x0); PINV(x1); \
    ask8(hp, (unsigned)(size_t)(FL + F_GO + (lane & 7)), tg, ho, mkd(ws, WS_END)); \
    if (!__all(max(max(max(hp[0], hp[1]), max(hp[2], hp[3])), max(max(hp[4], hp[5]), max(hp[6], hp[7]))) != SENT)) { \
      GSPIN { \
        RETRY(); \
        CBAR(); \
        _Pragma("unroll") for (int j = 0; j < 8; ++j) hp[j] = LD32(ho, j * 4096); \
        if (__all(max(max(max(hp[0], hp[1]), max(hp[2], hp[3])), max(max(hp[4], hp[5]), max(hp[6], hp[7]))) != SENT)) break; \
      } \
      DRAIN(); \
    } \
    STAMP(7); \
    float a0 = 0.f, a1 = 0.f, a2 = 0.f, a3 = 0.f; \
    _Pragma("unroll") for (int j = 0; j < 4; ++j) { a0 = dot2(hp[j], klo, a0); a1 = dot2(hp[j], khi, a1); a2 = dot2(hp[4 + j], klo, a2); a3 = dot2(hp[4 + j], khi, a3); } \
    const unsigned t0 = pk(a0 + a2, a1 + a3); \
    *(LAS unsigned*)(L + oa) = pk(x0 + blo(t0), x1 + bhi(t0)); \
  } while (0)
  unsigned xp = 0;
  if (!isC) xp = __builtin_amdgcn_raw_buffer_load_b32(rx, w * 256 + lane * 4, 0, 0);
  if (w < 8) {
    const unsigned stride = isC ? DM * 2 : FF * 2;
    const GAS unsigned char* p = isC ? (const GAS unsigned char*)wo + (size_t)(512 * g) * (DM * 2) + (128 * jq + 8 * r16) * 2
                                     : (const GAS unsigned char*)(r16 < 8 ? wg : wu) + (64 * dI + 8 * (r16 & 7)) * 2;
    p += (size_t)(64 * w + 8 * gq) * stride;
    unsigned aa = L_OA + (r16 >> 2) * 1024 + w * 128 + gq * 16, pa = L_PART + w * 2048 + gq * 512 + r16 * 32;
    PINV(aa); PINV(pa);
    v4u src[16], bt[16];
    const unsigned wcy = WMODE == 2 ? probe(isC ? (const unsigned char*)wo + (size_t)(512 * g + 64 * w + 63) * (DM * 2) + (jq & 2) * 256 : (const unsigned char*)wg + (size_t)(64 * w + 63) * (FF * 2) + (dI < 26 ? dI : 25) * 128) : 0u;
    const bool wnear = WMODE == 0 || (WMODE == 2 && wcy < NEAR);
    until(tstart, isC ? TC0 : TD0);
    lds_wait(FL + F_QPUB + (lane & 3), tg);
    if (wnear) {
#pragma unroll
      for (int j = 0; j < 16; ++j) src[j] = *(const GAS v4u*)(p + ((j >> 3) * 32 + (j & 7)) * stride);
    } else {
#pragma unroll
      for (int j = 0; j < 16; ++j) src[j] = __builtin_nontemporal_load((const GAS v4u*)(p + ((j >> 3) * 32 + (j & 7)) * stride));
    }
    SCHED();
    STAMP(4);
    tr8(src, bt);
    tr8(src + 8, bt + 8);
    if (!isC) {
      const v4u ones = {ONE2, ONE2, ONE2, ONE2};
      float cs[8];
#pragma unroll
      for (int e = 0; e < 8; ++e) cs[e] = mfma(ones, bt[8 + e], mfma(ones, bt[e], Z4))[0];
      if (lane < 16) {
        *(LAS f32x4*)(L + L_SS + w * 512 + lane * 32) = f32x4{cs[0], cs[1], cs[2], cs[3]};
        *(LAS f32x4*)(L + L_SS + w * 512 + lane * 32 + 16) = f32x4{cs[4], cs[5], cs[6], cs[7]};
      }
      lds_set(FL + F_SR + w, tg);
    }
    DRAIN();
    STAMP(5);
    if (w == 0) {
      if (isC) {
        const int hl = lane < 24 ? lane : 0;
        watch(rws, WS_HA + (g * 32 + hl) * 128, taglo, taghi, (LAS v2u*)(L + L_HINT + hl * 8), tg);
      } else {
        watch8(rws, WS_HB + (lane & 31) * 128, taglo, taghi, DLY3, FL + F_GO, tg, lane);
      }
      STAMP(10);
      DRAIN();
    } else if (w == 1 && isC) {
      lds_wait(FL + F_SR + (lane & 7), tg);
      if (lane == 0) ST64(hint(taglo, taghi), WS_HB + (g * 4 + jq) * 128);
    }
    if (!isC) GATHER_H();
    WGBAR();
    STAMP(8);
    {
      const v4u a0 = *(const LAS v4u*)(L + aa), a1 = *(const LAS v4u*)(L + aa + 64);
#pragma unroll
      for (int h = 0; h < 2; ++h) {
        f32x4 acc[4];
#pragma unroll
        for (int e = 0; e < 4; ++e) acc[e] = mfma(a0, bt[4 * h + e], Z4);
#pragma unroll
        for (int e = 0; e < 4; ++e) acc[e] = mfma(a1, bt[8 + 4 * h + e], acc[e]);
        *(LAS f32x4*)(L + pa + 16 * h) = f32x4{acc[0][0], acc[1][0], acc[2][0], acc[3][0]};
      }
    }
    STAMP(9);
    WGBAR();
    if (!isC) WGBAR();
  } else if (isC) {
    const int v = w & 7, bb = v >> 1, head = 2 * (v & 1) + (lane >> 5), sg = gq & 1;
    unsigned ra = L_PART + (w & 3) * 512 + lane * 8, so = WS_HP + (g * NB + (w & 3)) * 1024 + jq * 256 + lane * 4;
    unsigned rd = WS_OREC + (g * 24 + bb * NSPL + sg) * OREC_SZ + head * 256 + r16 * 16, rs = WS_OREC + (g * 24 + bb * NSPL + sg) * OREC_SZ + 1024 + head * 8;
    unsigned oa = L_OA + bb * 1024 + head * 256 + r16 * 16 + sg * 8;
    PINV(ra); PINV(so); PINV(rd); PINV(rs); PINV(oa);
    if (w == 15) ST128(SV, WS_HP + (g * NB + gq) * 1024 + jq * 256 + r16 * 16);
    DRAIN();
    STAMP(5);
    {
      v2u e;
#pragma nounroll
      for (unsigned sp = 0; sp < SPIN_L; ++sp) { e = *(volatile LAS v2u*)(L + L_HINT + (bb * NSPL + (lane < NSPL ? lane : 0)) * 8); if (__all(e.x == tg)) break; }
      wait_age(e.y, DLY2);
    }
    STAMP(6);
    GSPIN {
      v4u od[3];
      v2u st[3];
      CBAR();
#pragma unroll
      for (int u = 0; u < 3; ++u) st[u] = LD64(rs, 2 * u * OREC_SZ);
      SCHED();
#pragma unroll
      for (int u = 0; u < 3; ++u) od[u] = LD128(rd, 2 * u * OREC_SZ);
      SCHED();
      lds_set(FL + F_SR + v, tg);
      SCHED();
      unsigned bad = max(max(max(st[0].x, st[0].y), max(st[1].x, st[1].y)), max(st[2].x, st[2].y));
      const float m0 = __uint_as_float(st[0].x), m1 = __uint_as_float(st[1].x), m2 = __uint_as_float(st[2].x);
      const float mg = x16_max(fmaxf(fmaxf(m0, m1), m2));
      float wt[3] = {__builtin_amdgcn_exp2f(m0 - mg), __builtin_amdgcn_exp2f(m1 - mg), __builtin_amdgcn_exp2f(m2 - mg)};
      const float inv = __builtin_amdgcn_rcpf(x16_sum(fmaf(wt[0], __uint_as_float(st[0].y), fmaf(wt[1], __uint_as_float(st[1].y), wt[2] * __uint_as_float(st[2].y)))));
#pragma unroll
      for (int u = 0; u < 3; ++u) { wt[u] *= inv; PINV(wt[u]); }
      PINV(bad);
      SCHED();
      f32x2 a[4];
#pragma unroll
      for (int u = 0; u < 3; ++u) {
        f32x2 t[4];
#pragma unroll
        for (int d = 0; d < 4; ++d) { t[d] = f32x2{blo(od[u][d]), bhi(od[u][d])}; PINV(t[d]); }
        bad = max(bad, max(max(od[u].x, od[u].y), max(od[u].z, od[u].w)));
#pragma unroll
        for (int d = 0; d < 4; ++d) a[d] = u == 0 ? t[d] * wt[0] : t[d] * wt[u] + a[d];
      }
      float r[4];
#pragma unroll
      for (int d = 0; d < 4; ++d) {
        const auto sw = __builtin_amdgcn_permlane16_swap(__float_as_uint(a[d >> 1][d & 1]), __float_as_uint(a[2 + (d >> 1)][d & 1]), false, false);
        r[d] = __uint_as_float(sw[0]) + __uint_as_float(sw[1]);
      }
      *(LAS v2u*)(L + oa) = v2u{pk(r[0], r[1]), pk(r[2], r[3])};
      STAMP(7);
      if (__all(bad != SENT)) break;
      RETRY();
    }
    DRAIN();
    STAMP(8);
    WGBAR();
    WGBAR();
    if (w >= 12) {
      f32x2 pv[8];
#pragma unroll
      for (int j = 0; j < 8; ++j) pv[j] = *(const LAS f32x2*)(L + ra + j * 2048);
      SCHED();
      const f32x2 t = ((pv[0] + pv[1]) + (pv[2] + pv[3])) + ((pv[4] + pv[5]) + (pv[6] + pv[7]));
      const unsigned vv = nosent(pk(t.x, t.y));
      const v4u o4 = {dppu<0x00>(vv), dppu<0x55>(vv), dppu<0xAA>(vv), dppu<0xFF>(vv)};
      if ((lane & 3) == 0) ST128(o4, so);
      STAMPE(9);
    }
  } else {
    unsigned ra = L_PART + (w & 3) * 512 + lane * 4, hw = L_HH + (w & 3) * 128 + lane * 2, ha = L_HH + (r16 >> 2) * 128 + gq * 16;
    unsigned so = WS_OP + dI * 8192 + gq * 2048 + (w & 7) * 256 + r16 * 16, sa = L_OA + (w & 3) * 1024 + lane * 16;
    PINV(ra); PINV(hw); PINV(ha); PINV(so); PINV(sa);
    ST128(SV, WS_OP + dI * 8192 + (w - 8) * 1024 + lane * 16);
    const bool wnear = WMODE == 0 || (WMODE == 2 && probe((const unsigned char*)wd + (size_t)(64 * dI + 8 * (w - 8)) * 1024) < NEAR);
    until(tstart, TWD);
    const rsrc_t rw = mk(wd, FF * DM * 2u);
    v2u src[16];
    v4u bt[8];
    if (wnear) {
#pragma unroll
      for (int j = 0; j < 16; ++j) src[j] = __builtin_amdgcn_raw_buffer_load_b64(rw, (unsigned)(8 * gq + (j & 3)) * 1024 + (w - 8) * 128 + r16 * 8, (64 * dI + (j >> 3) * 32 + (j & 4)) * 1024, 0);
    } else {
#pragma unroll
      for (int j = 0; j < 16; ++j) src[j] = __builtin_amdgcn_raw_buffer_load_b64(rw, (unsigned)(8 * gq + (j & 3)) * 1024 + (w - 8) * 128 + r16 * 8, (64 * dI + (j >> 3) * 32 + (j & 4)) * 1024, AUX_NT);
    }
    SCHED();
    tr4(src, bt);
    tr4(src + 8, bt + 4);
    float sgs = 0.f, sus = 0.f;
    if (w >= 12) {
      lds_wait(FL + F_SR + (lane & 7), tg);
#pragma unroll
      for (int j = 0; j < 8; ++j) { sgs += *(const LAS float*)(L + L_SS + j * 512 + lane * 4); sus += *(const LAS float*)(L + L_SS + j * 512 + 256 + lane * 4); }
    }
    PINV(sgs); PINV(sus);
    DRAIN();
    STAMP(5);
    GATHER_H();
    WGBAR();
    if (w >= 12) {
      const f32x2 st = ln_stats(*(const LAS v4u*)(L + sa));
      float kg = st.y * -1.4426950408889634f, cg = st.x * sgs, cu = st.x * sus, ku = st.y;
      PINV(kg); PINV(cg); PINV(cu); PINV(ku);
      WGBAR();
      float gp[8], up[8];
#pragma unroll
      for (int j = 0; j < 8; ++j) { gp[j] = *(const LAS float*)(L + ra + j * 2048); up[j] = *(const LAS float*)(L + ra + j * 2048 + 256); }
      SCHED();
      const float gg = ((gp[0] + gp[1]) + (gp[2] + gp[3])) + ((gp[4] + gp[5]) + (gp[6] + gp[7]));
      const float uu = ((up[0] + up[1]) + (up[2] + up[3])) + ((up[4] + up[5]) + (up[6] + up[7]));
      const float hh = ku * (uu - cu) * __builtin_amdgcn_rcpf(1.f + __builtin_amdgcn_exp2f(kg * (gg - cg)));
      *(LAS u16*)(L + hw) = (u16)pk(hh, hh);
    } else {
      if (w == 8 && lane == 0) ST64(hint(taglo, taghi), WS_HC + dI * 128);
      WGBAR();
    }
    WGBAR();
    STAMP(8);
    const v4u a0 = *(const LAS v4u*)(L + ha), a1 = *(const LAS v4u*)(L + ha + 64);
    f32x4 acc[4];
#pragma unroll
    for (int e = 0; e < 4; ++e) acc[e] = mfma(a0, bt[e], Z4);
#pragma unroll
    for (int e = 0; e < 4; ++e) acc[e] = mfma(a1, bt[4 + e], acc[e]);
    ST128((v4u{nosent(__float_as_uint(acc[0][0])), nosent(__float_as_uint(acc[1][0])), nosent(__float_as_uint(acc[2][0])), nosent(__float_as_uint(acc[3][0]))}), so);
    STAMPE(9);
  }
  PDUMP();
}
}

extern "C" void kernel_launch(void* const* d_in, const int* in_sizes, int n_in, void* d_out, int out_size, void* d_ws, size_t ws_size, hipStream_t stream) {
  (void)in_sizes; (void)n_in; (void)out_size; (void)ws_size;
  fused_decode<<<dim3(256), dim3(1024), 0, stream>>>((const u16*)d_in[0], (const u16*)d_in[1], (const u16*)d_in[2], (const u16*)d_in[3], (const u16*)d_in[4],
                                                     (const u16*)d_in[6], (const u16*)d_in[7], (const u16*)d_in[8], (u16*)d_out, (unsigned char*)d_ws);
}
```

```cpp
#include <hip/hip_runtime.h>
#include <stdint.h>

#define PROF 0
#define TK0 80
#define KPRE 4
#define QDELTA 70
#define TC0 245
#define TD0 100
#define KMID 1
#define KLEFT 2
#define VPRE 0
#define WPRE 3
#define TWD 450
#define DLY2 60
#define DLY3 85
#define DLY5 76

namespace {
typedef unsigned short u16;
typedef unsigned long long u64;
typedef unsigned v4u __attribute__((ext_vector_type(4)));
typedef unsigned v2u __attribute__((ext_vector_type(2)));
typedef int v4i __attribute__((ext_vector_type(4)));
typedef float f32x4 __attribute__((ext_vector_type(4)));
typedef float f32x2 __attribute__((ext_vector_type(2)));
typedef short bf16x8 __attribute__((ext_vector_type(8)));
typedef __bf16 bf16x2 __attribute__((ext_vector_type(2)));
#define GAS __attribute__((address_space(1)))
#define LAS __attribute__((address_space(3)))
typedef __amdgpu_buffer_rsrc_t rsrc_t;

constexpr int NB = 4, DM = 512, HKV = 8, SK = 8192, WIN = 2048, FF = 1792, NQ = 4096;
constexpr int NSPL = 6, NDCU = 28;
constexpr int AUX_NT = 2, AUX_SC1 = 16;
constexpr unsigned SENT = 0xFFFFFFFFu, ONE2 = 0x3F803F80u;
constexpr int WS_EPOCH = 0, WS_HB = 4096, WS_HC = WS_HB + 32 * 128, WS_HA = WS_HC + 32 * 128, WS_QG = WS_HA + 8 * 32 * 128;
constexpr int WS_OREC = WS_QG + 8 * 4 * 4 * 256 * 8, OREC_SZ = 1152;
constexpr int WS_HP = WS_OREC + 8 * 24 * OREC_SZ, WS_OP = WS_HP + 8 * 4 * 512 * 2, WS_END = WS_OP + NDCU * 8192;
constexpr int WS_PROF = 2 << 20;
constexpr int L_FLAG = 0, L_MW = 512, L_HINT = 512, L_QS = 1024, L_OA = L_QS + 1024, L_HH = L_OA + 4096, L_SS = L_HH + 512;
constexpr int L_QRED = L_SS + 4096, L_PART = L_QRED + 5120, L_USED = L_PART + 11 * 2048;
constexpr int L_TOTAL = 84 * 1024;
static_assert(L_USED <= L_TOTAL, "lds");
enum { F_QIS = 0, F_QS = 4, F_QPUB = 12, F_QR = 16, F_CL = 24, F_GO = 32, F_SR = 72, F_MW = 80, F_VL = 96, F_OPF = 112 };
constexpr int CODE_BYTES = 15360;
constexpr unsigned SPIN_G = 1u << 17, SPIN_L = 1u << 21;

__device__ __forceinline__ u64 rt() { return __builtin_amdgcn_s_memrealtime(); }
__device__ __forceinline__ rsrc_t mk(const void* p, unsigned bytes) { return __builtin_amdgcn_make_buffer_rsrc((void*)p, 0, bytes, 0x00020000); }
__device__ __forceinline__ f32x4 mfma(v4u a, v4u b, f32x4 c) { return __builtin_amdgcn_mfma_f32_16x16x32_bf16(__builtin_bit_cast(bf16x8, a), __builtin_bit_cast(bf16x8, b), c, 0, 0, 0); }
__device__ __forceinline__ unsigned pk(float lo, float hi) { return __builtin_bit_cast(unsigned, __builtin_convertvector(f32x2{lo, hi}, bf16x2)); }
__device__ __forceinline__ float blo(unsigned v) { return __builtin_bit_cast(float, v << 16); }
__device__ __forceinline__ float bhi(unsigned v) { return __builtin_bit_cast(float, v & 0xffff0000u); }
__device__ __forceinline__ unsigned plo(unsigned a, unsigned b) { return __builtin_amdgcn_perm(b, a, 0x05040100u); }
__device__ __forceinline__ unsigned phi(unsigned a, unsigned b) { return __builtin_amdgcn_perm(b, a, 0x07060302u); }
__device__ __forceinline__ unsigned nosent(unsigned v) { return v < 0xFFFFFFFEu ? v : 0xFFFFFFFEu; }
template <int C> __device__ __forceinline__ float dpp(float x) { return __builtin_bit_cast(float, __builtin_amdgcn_mov_dpp(__builtin_bit_cast(int, x), C, 0xf, 0xf, true)); }
template <int C> __device__ __forceinline__ unsigned dppu(unsigned x) { return (unsigned)__builtin_amdgcn_mov_dpp((int)x, C, 0xf, 0xf, true); }
constexpr int XOR1 = 0xB1, XOR2 = 0x4E, ROR4 = 0x124, ROR8 = 0x128;
__device__ __forceinline__ float x16_sum(float x) { auto s = __builtin_amdgcn_permlane16_swap(__float_as_uint(x), __float_as_uint(x), false, false); return __uint_as_float(s[0]) + __uint_as_float(s[1]); }
__device__ __forceinline__ float x32_sum(float x) { auto s = __builtin_amdgcn_permlane32_swap(__float_as_uint(x), __float_as_uint(x), false, false); return __uint_as_float(s[0]) + __uint_as_float(s[1]); }
__device__ __forceinline__ float x16_max(float x) { auto s = __builtin_amdgcn_permlane16_swap(__float_as_uint(x), __float_as_uint(x), false, false); return fmaxf(__uint_as_float(s[0]), __uint_as_float(s[1])); }
__device__ __forceinline__ float x32_max(float x) { auto s = __builtin_amdgcn_permlane32_swap(__float_as_uint(x), __float_as_uint(x), false, false); return fmaxf(__uint_as_float(s[0]), __uint_as_float(s[1])); }
__device__ __forceinline__ float row_sum(float x) { x += dpp<XOR1>(x); x += dpp<XOR2>(x); x += dpp<ROR4>(x); x += dpp<ROR8>(x); return x; }
__device__ __forceinline__ float row_max(float x) { x = fmaxf(x, dpp<XOR1>(x)); x = fmaxf(x, dpp<XOR2>(x)); x = fmaxf(x, dpp<ROR4>(x)); x = fmaxf(x, dpp<ROR8>(x)); return x; }
__device__ __forceinline__ float dot2(unsigned a, unsigned b, float c) { return __builtin_amdgcn_fdot2_f32_bf16(__builtin_bit_cast(bf16x2, a), __builtin_bit_cast(bf16x2, b), c, false); }
#define CBAR() asm volatile("" ::: "memory")
#define SCHED() __builtin_amdgcn_sched_barrier(0)
#define DRAIN() do { asm volatile("s_waitcnt vmcnt(0)" ::: "memory"); __builtin_amdgcn_s_waitcnt(0x0F70); } while (0)
#define PINV(x) asm volatile("" : "+v"(x))
#define PINS(x) asm volatile("" : "+s"(x))
#define WGBAR() do { SCHED(); asm volatile("s_waitcnt lgkmcnt(0)" ::: "memory"); __builtin_amdgcn_s_barrier(); CBAR(); SCHED(); } while (0)
#define GSPIN _Pragma("nounroll") for (unsigned sp = 0; sp < SPIN_G; ++sp)
#define LD32(o, so) __builtin_amdgcn_raw_buffer_load_b32(rws, (o), (so), AUX_SC1)
#define LD64(o, so) __builtin_amdgcn_raw_buffer_load_b64(rws, (o), (so), AUX_SC1)
#define LD128(o, so) __builtin_amdgcn_raw_buffer_load_b128(rws, (o), (so), AUX_SC1)
#define ST64(v, o) __builtin_amdgcn_raw_buffer_store_b64((v), rws, (o), 0, AUX_SC1)
#define ST128(v, o) __builtin_amdgcn_raw_buffer_store_b128((v), rws, (o), 0, AUX_SC1)
__device__ __forceinline__ bool okv(v4u r) { return max(max(r.x, r.y), max(r.z, r.w)) != SENT; }
__device__ __forceinline__ void lds_wait(volatile LAS unsigned* f, unsigned tg) {
  SCHED();
#pragma nounroll
  for (unsigned s = 0; s < SPIN_L; ++s) { if (__all(*f == tg)) break; }
  CBAR();
  SCHED();
}
__device__ __forceinline__ void lds_set(volatile LAS unsigned* f, unsigned tg) { CBAR(); *f = tg; CBAR(); }
__device__ __forceinline__ void until(u64 t0, unsigned ticks) { while (rt() - t0 < ticks) __builtin_amdgcn_s_sleep(1); }
__device__ __forceinline__ v2u hint(unsigned taglo, unsigned taghi) { return v2u{taglo | ((unsigned)rt() & 0xFFFFu), taghi}; }
__device__ __forceinline__ bool hint_ok(v2u h, unsigned taglo, unsigned taghi) { return (h.y == taghi) & ((h.x ^ taglo) < 65536u); }
__device__ __forceinline__ void wait_age(unsigned t16, unsigned dly) {
#pragma nounroll
  for (unsigned s = 0; s < 4096u; ++s) { if (__all((((unsigned)rt() - t16) & 0xFFFFu) >= dly)) break; }
}
__device__ __forceinline__ void watch(rsrc_t rws, unsigned ho, unsigned taglo, unsigned taghi, LAS v2u* slot, unsigned tg) {
  GSPIN {
    CBAR();
    const v2u h = LD64(ho, 0);
    const bool ok = hint_ok(h, taglo, taghi);
    if (ok) *slot = v2u{tg, h.x};
    CBAR();
    if (__all(ok)) break;
  }
}
__device__ __forceinline__ void watch8(rsrc_t rws, unsigned ho, unsigned taglo, unsigned taghi, unsigned dly, volatile LAS unsigned* go, unsigned tg, int lane) {
  v2u h;
  unsigned seen = 0, done = 0;
#pragma nounroll
  for (unsigned sp = 0; sp < SPIN_G && done != 0x11111111u; ++sp) {
    if (seen != ~0u) {
      CBAR();
      h = LD64(ho, 0);
      seen = (unsigned)__builtin_amdgcn_ballot_w64(hint_ok(h, taglo, taghi));
    }
    unsigned m = (unsigned)__builtin_amdgcn_ballot_w64((((unsigned)rt() - h.x) & 0xFFFFu) >= dly) & seen;
    m &= m >> 1; m &= m >> 2; m &= 0x11111111u;
    if (m != done) { if ((m >> (4 * (lane & 7))) & 1u) go[lane & 7] = tg; done = m; }
  }
  CBAR();
}
__device__ __forceinline__ u64 load_tag(const unsigned char* ws) {
  u64 e;
  asm volatile("s_load_dwordx2 %0, %1, 0x0\n\ts_waitcnt lgkmcnt(0)" : "=s"(e) : "s"(ws) : "memory");
  return (e + 1ull) & 0xFFFFFFFFFFFFull;
}
__device__ __forceinline__ void code_fetch(int part) {
  u64 pc0;
  asm volatile("s_getpc_b64 %0" : "=s"(pc0));
  const u64 a0 = (pc0 & ~127ull) + (u64)(part * 512 < CODE_BYTES - 512 ? part * 512 : CODE_BYTES - 512);
  unsigned t0, t1, t2, t3, t4, t5, t6, t7;
  asm volatile("s_load_dword %0, %8, 0x0\n\ts_load_dword %1, %8, 0x40\n\ts_load_dword %2, %8, 0x80\n\ts_load_dword %3, %8, 0xc0\n\t"
               "s_load_dword %4, %8, 0x100\n\ts_load_dword %5, %8, 0x140\n\ts_load_dword %6, %8, 0x180\n\ts_load_dword %7, %8, 0x1c0\n\ts_waitcnt lgkmcnt(0)"
               : "=&s"(t0), "=&s"(t1), "=&s"(t2), "=&s"(t3), "=&s"(t4), "=&s"(t5), "=&s"(t6), "=&s"(t7) : "s"(a0) : "memory");
}
__device__ __forceinline__ v4i mkd(const void* p, unsigned bytes) { return v4i{(int)(u64)p, (int)((u64)p >> 32) & 0xFFFF, (int)bytes, 0x00020000}; }
template <int OFF> __device__ __forceinline__ void ldnt(v4u& d, unsigned vo, v4i rs, unsigned so) {
  asm volatile("buffer_load_dwordx4 %0, %1, %2, %3 offen offset:%4 nt" : "=v"(d) : "v"(vo), "s"(rs), "s"(so), "i"(OFF) : "memory");
}
template <int LO, int HI> __device__ __forceinline__ void ldtile(v4u* d, unsigned vo, v4i rs, unsigned so, unsigned so2) {
  if constexpr (LO < HI) { ldnt<(LO & 3) * 1024>(d[LO], vo, rs, LO < 4 ? so : so2); ldtile<LO + 1, HI>(d, vo, rs, so, so2); }
}
__device__ __forceinline__ void ldnt2(v2u& d, unsigned vo, v4i rs, unsigned so) {
  asm volatile("buffer_load_dwordx2 %0, %1, %2, %3 offen nt" : "=v"(d) : "v"(vo), "s"(rs), "s"(so) : "memory");
}
__device__ __forceinline__ void ldp4(v4u& d, unsigned vo, v4i rs) {
  asm volatile("buffer_load_dwordx4 %0, %1, %2, 0 offen" : "=v"(d) : "v"(vo), "s"(rs) : "memory");
}
#define VMWAIT2(n, a, b) asm volatile("s_waitcnt vmcnt(%2)" : "+v"(a), "+v"(b) : "i"(n) : "memory")
__device__ __forceinline__ void ask8(unsigned (&hp)[8], unsigned go, unsigned tg, unsigned ho, v4i rs) {
  unsigned t, done, n, cnt;
#define ASK1(j, off) "s_bitcmp1_b32 %10, " #j "\n\ts_cbranch_scc0 2" #j "f\n\ts_movk_i32 %11, " #off "\n\tbuffer_load_dword %" #j ", %14, %15, %11 offen sc1\n2" #j ":\n\t"
  asm volatile("s_mov_b32 %9, 0\n\ts_mov_b32 %11, 0x200000\n"
               "1:\n\tds_read_b32 %8, %12\n\ts_sub_u32 %11, %11, 1\n\ts_cbranch_scc1 3f\n\ts_waitcnt lgkmcnt(0)\n\t"
               "v_cmp_eq_u32 vcc, %13, %8\n\ts_andn2_b32 %10, vcc_lo, %9\n\ts_and_b32 %10, %10, 0xff\n\ts_cbranch_scc0 1b\n\t"
               ASK1(0, 0) ASK1(1, 0x1000) ASK1(2, 0x2000) ASK1(3, 0x3000) ASK1(4, 0x4000) ASK1(5, 0x5000) ASK1(6, 0x6000) ASK1(7, 0x7000)
               "s_mov_b32 %11, 0x200000\n\ts_or_b32 %9, %9, %10\n\ts_cmp_lg_u32 %9, 0xff\n\ts_cbranch_scc1 1b\n"
               "3:\n\ts_waitcnt vmcnt(0) lgkmcnt(0)"
               : "+v"(hp[0]), "+v"(hp[1]), "+v"(hp[2]), "+v"(hp[3]), "+v"(hp[4]), "+v"(hp[5]), "+v"(hp[6]), "+v"(hp[7]), "=&v"(t), "=&s"(done), "=&s"(n), "=&s"(cnt)
               : "v"(go), "s"(tg), "v"(ho), "s"(rs) : "vcc", "scc", "memory");
#undef ASK1
}
__device__ __forceinline__ f32x2 ln_stats(v4u hv) {
  const float c0 = blo((unsigned)__builtin_amdgcn_readfirstlane(hv[0]));
  float t[8];
#pragma unroll
  for (int d = 0; d < 4; ++d) { t[2 * d] = blo(hv[d]) - c0; t[2 * d + 1] = bhi(hv[d]) - c0; }
  float s1 = ((t[0] + t[1]) + (t[2] + t[3])) + ((t[4] + t[5]) + (t[6] + t[7]));
  float s2 = (fmaf(t[1], t[1], t[0] * t[0]) + fmaf(t[3], t[3], t[2] * t[2])) + (fmaf(t[5], t[5], t[4] * t[4]) + fmaf(t[7], t[7], t[6] * t[6]));
  s1 += dpp<XOR1>(s1); s2 += dpp<XOR1>(s2);
  s1 += dpp<XOR2>(s1); s2 += dpp<XOR2>(s2);
  s1 += dpp<ROR4>(s1); s2 += dpp<ROR4>(s2);
  s1 += dpp<ROR8>(s1); s2 += dpp<ROR8>(s2);
  s1 = x16_sum(s1); s2 = x16_sum(s2);
  s1 = x32_sum(s1); s2 = x32_sum(s2);
  const float d = s1 * (1.f / DM), var = fmaxf(fmaf(-d, d, s2 * (1.f / DM)), 0.f);
  return f32x2{c0 + d, __builtin_amdgcn_rsqf(var + 1e-5f)};
}
#define STAMP(k) do {} while (0)
#define STAMPE(k) do {} while (0)
#define PDUMP() do {} while (0)
#define RETRY() do {} while (0)

__device__ __forceinline__ void tr4(const v2u* s, v4u* o) {
#pragma unroll
  for (int m = 0; m < 4; ++m) {
    o[0][m] = plo(s[2 * m].x, s[2 * m + 1].x); o[1][m] = phi(s[2 * m].x, s[2 * m + 1].x);
    o[2][m] = plo(s[2 * m].y, s[2 * m + 1].y); o[3][m] = phi(s[2 * m].y, s[2 * m + 1].y);
  }
#pragma unroll
  for (int e = 0; e < 4; ++e) PINV(o[e]);
}
__device__ __forceinline__ void tr8(const v4u* s, v4u* o) {
#pragma unroll
  for (int m = 0; m < 4; ++m)
#pragma unroll
    for (int d = 0; d < 4; ++d) { o[2 * d][m] = plo(s[2 * m][d], s[2 * m + 1][d]); o[2 * d + 1][m] = phi(s[2 * m][d], s[2 * m + 1][d]); }
#pragma unroll
  for (int e = 0; e < 8; ++e) PINV(o[e]);
}

__global__ void __launch_bounds__(1024) fused_decode(const u16* __restrict__ x, const u16* __restrict__ kc, const u16* __restrict__ vc,
                                                     const u16* __restrict__ wq, const u16* __restrict__ wo, const u16* __restrict__ wg,
                                                     const u16* __restrict__ wu, const u16* __restrict__ wd, u16* __restrict__ out, unsigned char* ws) {
  const u64 tstart = rt();
  __shared__ __attribute__((aligned(16))) unsigned char smem[L_TOTAL];
  LAS unsigned char* L = (LAS unsigned char*)smem;
  volatile LAS unsigned* FL = (volatile LAS unsigned*)(L + L_FLAG);
  const int lane = threadIdx.x & 63;
  int w = (__builtin_amdgcn_readfirstlane(threadIdx.x >> 6) + 11) & 15;
  const int c = blockIdx.x, g = c & 7, i = c >> 3;
  if (w == 11) code_fetch(i);
  const int gq = lane >> 4, r16 = lane & 15;
  const int b = (i * 43) >> 8, s = i - b * NSPL;
  constexpr int ntile = 11;
  const int row0 = 344 * s - (s > 4 ? 8 : 0);
  const int cw = w - 12, rq = i & 3, cq = i >> 2, R0 = 128 * rq + 32 * cw;
  asm volatile("" :: "s"(x), "s"(kc), "s"(vc), "s"(wq), "s"(wo), "s"(wg), "s"(wu), "s"(wd), "s"(out), "s"(ws));
  v4u kf[8], vf[8], xa, xr;
  v2u wsrc[8];
  const unsigned kvbase = ((unsigned)(b * HKV + g) * SK + (SK - WIN) + row0 + 16u * w) * 256u, kvbase2 = kvbase + ntile * 4096u;
  const v4i rk = mkd(kc, NB * HKV * SK * 256u), rv = mkd(vc, NB * HKV * SK * 256u);
  if (w >= 12) {
    __builtin_amdgcn_s_setprio(3);
    const v4i rw = mkd(wq, DM * NQ * 2u), rxd = mkd(x, NB * DM * 2u);
    const unsigned o = (unsigned)(8 * gq) * (NQ * 2) + r16 * 8, so = R0 * (NQ * 2) + (512 * g + 64 * cq) * 2;
#pragma unroll
    for (int j = 0; j < 8; ++j) ldnt2(wsrc[j], o, rw, so + j * (NQ * 2));
    ldp4(xa, (r16 >> 2) * 1024 + (R0 + 8 * gq) * 2, rxd);
    ldp4(xr, cw * 1024 + lane * 16, rxd);
  } else if (i < 24 && w < ntile) {
    __builtin_amdgcn_s_setprio(2);
    until(tstart, TK0);
    ldtile<0, KPRE>(kf, r16 * 64 + gq * 16, rk, kvbase, kvbase2);
  }
  STAMP(1);
  PINS(w);
  const rsrc_t rws = mk(ws, WS_END), rx = mk(x, NB * DM * 2u);
  const f32x4 Z4 = {0.f, 0.f, 0.f, 0.f};
  const v4u SV = {SENT, SENT, SENT, SENT};
  const unsigned orec = WS_OREC + (g * 24 + i) * OREC_SZ;

  if (__builtin_expect(w >= 12, 1)) {
    const unsigned tg = (unsigned)load_tag(ws);
    asm volatile("s_waitcnt vmcnt(0)" : "+v"(wsrc[0]), "+v"(wsrc[1]), "+v"(wsrc[2]), "+v"(wsrc[3]), "+v"(wsrc[4]), "+v"(wsrc[5]), "+v"(wsrc[6]), "+v"(wsrc[7]), "+v"(xa), "+v"(xr) :: "memory");
    {
      v4u bt[4];
      tr4(wsrc, bt);
      if ((lane & 3) == 1) xa = v4u{ONE2, ONE2, ONE2, ONE2};
      f32x4 qp, cs;
#pragma unroll
      for (int e = 0; e < 4; ++e) { const f32x4 d = mfma(xa, bt[e], Z4); qp[e] = d[0]; cs[e] = d[1]; }
      *(LAS f32x4*)(L + L_QRED + cw * 1024 + lane * 16) = qp;
      if (lane < 16) *(LAS f32x4*)(L + L_QRED + 4096 + cw * 256 + lane * 16) = cs;
    }
    lds_set(FL + F_QR + cw, tg);
    f32x2 xst = ln_stats(xr);
    PINV(xst);
    STAMP(2);
    lds_wait(FL + F_QR + (lane & 3), tg);
    {
      float a[4], cs[4];
#pragma unroll
      for (int j = 0; j < 4; ++j) { a[j] = *(const LAS float*)(L + L_QRED + j * 1024 + cw * 256 + lane * 4); cs[j] = *(const LAS float*)(L + L_QRED + 4096 + j * 256 + lane * 4); }
      const float q = xst.y * (((a[0] + a[1]) + (a[2] + a[3])) - xst.x * ((cs[0] + cs[1]) + (cs[2] + cs[3])));
      const unsigned q2 = pk(q, dpp<XOR1>(q));
      if ((lane & 1) == 0) ST64((v2u{q2, tg}), WS_QG + (((g * NB + cw) * 4 + rq) * 256 + 32 * cq) * 8 + lane * 4);
    }
    lds_set(FL + F_QPUB + cw, tg);
    STAMP(3);
    if (i < 24) {
      until(rt(), QDELTA);
      const unsigned qo = WS_QG + ((g * NB + b) * 4 * 256 + cw * 64 + lane) * 8;
      v2u qg[4];
      GSPIN {
        CBAR();
#pragma unroll
        for (int j = 0; j < 4; ++j) qg[j] = LD64(qo, j * 2048);
        SCHED();
        lds_set(FL + F_QIS + cw, tg);
        SCHED();
        if (__all((qg[0].y == tg) & (qg[1].y == tg) & (qg[2].y == tg) & (qg[3].y == tg))) break;
        RETRY();
      }
      const float q0 = (blo(qg[0].x) + blo(qg[1].x)) + (blo(qg[2].x) + blo(qg[3].x)), q1 = (bhi(qg[0].x) + bhi(qg[1].x)) + (bhi(qg[2].x) + bhi(qg[3].x));
      *(LAS unsigned*)(L + L_QS + (cw * 64 + lane) * 4) = pk(q0, q1);
      lds_set(FL + F_QS + cw, tg);
      STAMP(4);
      PDUMP();
      return;
    }
    __builtin_amdgcn_s_setprio(0);
  } else if (i < 24) {
    if (w < ntile) {
      const unsigned tg = (unsigned)load_tag(ws);
      const int nval = w < 10 ? 8 : s < 4 ? 6 : 4;
      const unsigned oob6 = nval < 6 ? 0x80000000u : 0u, oob8 = nval < 8 ? 0x80000000u : 0u;
      static_assert(KPRE == 4, "");
      if (KMID >= 1) { asm volatile("s_waitcnt vmcnt(%0)" :: "i"(KLEFT) : "memory"); ldtile<4, 6>(kf, (r16 * 64 + gq * 16) | oob6, rk, kvbase, kvbase2); }
      if (KMID >= 2) { asm volatile("s_waitcnt vmcnt(%0)" :: "i"(KLEFT) : "memory"); ldtile<6, 8>(kf, (r16 * 64 + gq * 16) | oob8, rk, kvbase, kvbase2); }
      lds_wait(FL + F_QPUB + (lane & 3), tg);
      if (KMID < 1) ldtile<4, 6>(kf, (r16 * 64 + gq * 16) | oob6, rk, kvbase, kvbase2);
      if (KMID < 2) ldtile<6, 8>(kf, (r16 * 64 + gq * 16) | oob8, rk, kvbase, kvbase2);
      if (VPRE) ldtile<0, VPRE>(vf, lane * 16, rv, kvbase, kvbase2);
      __builtin_amdgcn_s_setprio(0);
      lds_wait(FL + F_QIS + (lane & 3), tg);
      ldtile<VPRE, 4>(vf, lane * 16, rv, kvbase, kvbase2);
      ldtile<4, 6>(vf, (lane * 16) | oob6, rv, kvbase, kvbase2);
      ldtile<6, 8>(vf, (lane * 16) | oob8, rv, kvbase, kvbase2);
      STAMP(2);
      lds_wait(FL + (lane < 4 ? F_QS + lane : F_CL), tg);
      asm volatile("s_waitcnt vmcnt(8)" : "+v"(kf[0]), "+v"(kf[1]), "+v"(kf[2]), "+v"(kf[3]), "+v"(kf[4]), "+v"(kf[5]), "+v"(kf[6]), "+v"(kf[7]) :: "memory");
      STAMP(3);
      const v4u qf = *(const LAS v4u*)(L + L_QS + r16 * 64 + gq * 16);
      float sc[8];
#pragma unroll
      for (int t = 0; t < 8; ++t) {
        const f32x4 d = mfma(kf[t], qf, Z4);
        sc[t] = (dpp<0x00>(d[0]) + dpp<0x55>(d[1])) + (dpp<0xAA>(d[2]) + dpp<0xFF>(d[3]));
        if (t >= 4 && t >= nval) sc[t] = -__builtin_inff();
      }
      float m = fmaxf(fmaxf(fmaxf(sc[0], sc[1]), fmaxf(sc[2], sc[3])), fmaxf(fmaxf(sc[4], sc[5]), fmaxf(sc[6], sc[7])));
      m = x32_max(x16_max(m));
      constexpr float C2 = 0.08838834764831845f * 1.4426950408889634f;
      const float ms = m * C2;
      float p[8], l = 0.f;
#pragma unroll
      for (int t = 0; t < 8; ++t) { p[t] = __builtin_amdgcn_exp2f(fmaf(sc[t], C2, -ms)); l += p[t]; }
      l = x32_sum(x16_sum(l));
      if ((lane & 0x33) == 0) *(LAS f32x2*)(L + L_MW + ((lane >> 2) * 12 + w) * 8) = f32x2{ms, l};
      lds_set(FL + F_MW + w, tg);
      v4u pf = {pk(p[0], p[1]), pk(p[2], p[3]), pk(p[4], p[5]), pk(p[6], p[7])};
      unsigned pa = L_PART + w * 2048 + gq * 512 + r16 * 32;
      PINV(pf); PINV(pa);
      STAMP(4);
      v4u vt[8];
#pragma unroll
      for (int mm = 0; mm < 4; ++mm) {
        if (mm == 0) VMWAIT2(6, vf[0], vf[1]);
        if (mm == 1) VMWAIT2(4, vf[2], vf[3]);
        if (mm == 2) { lds_set(FL + F_VL + w, tg); VMWAIT2(2, vf[4], vf[5]); }
        if (mm == 3) { VMWAIT2(0, vf[6], vf[7]); STAMP(5); }
#pragma unroll
        for (int d = 0; d < 4; ++d) { vt[2 * d][mm] = plo(vf[2 * mm][d], vf[2 * mm + 1][d]); vt[2 * d + 1][mm] = phi(vf[2 * mm][d], vf[2 * mm + 1][d]); }
        if (mm < 3) {
#pragma unroll
          for (int e = 0; e < 8; ++e) PINV(vt[e]);
        }
        SCHED();
      }
      float o[8];
#pragma unroll
      for (int e = 0; e < 8; ++e) o[e] = mfma(pf, vt[e], Z4)[0];
      *(LAS f32x4*)(L + pa) = f32x4{o[0], o[1], o[2], o[3]};
      *(LAS f32x4*)(L + pa + 16) = f32x4{o[4], o[5], o[6], o[7]};
      lds_set(FL + F_OPF + w, tg);
      STAMP(6);
      if (w < 4) {
        const int jl = r16 < ntile ? r16 : 0;
        lds_wait(FL + F_MW + jl, tg);
        const f32x2 ml = *(const LAS f32x2*)(L + L_MW + (w * 12 + jl) * 8);
        const float mg = row_max(ml.x);
        const float wt = r16 < ntile ? __builtin_amdgcn_exp2f(ml.x - mg) : 0.f;
        const float lt = row_sum(wt * ml.y);
        float wj[11];
#pragma unroll
        for (int j = 0; j < 11; ++j) { wj[j] = __builtin_bit_cast(float, __builtin_amdgcn_readlane(__builtin_bit_cast(int, wt), j)); PINS(wj[j]); }
        if (lane == 0) ST64((v2u{nosent(__float_as_uint(mg)), nosent(__float_as_uint(lt))}), orec + 1024 + w * 8);
        unsigned ra = L_PART + w * 512 + lane * 8, so = orec + w * 256 + lane * 4;
        PINV(ra); PINV(so);
        lds_wait(FL + F_OPF + jl, tg);
        f32x2 pv[11];
#pragma unroll
        for (int j = 0; j < 11; ++j) pv[j] = *(const LAS f32x2*)(L + ra + j * 2048);
        SCHED();
        f32x2 a = pv[0] * wj[0], a2 = pv[1] * wj[1];
#pragma unroll
        for (int j = 2; j < 10; j += 2) { a += pv[j] * wj[j]; a2 += pv[j + 1] * wj[j + 1]; }
        a += pv[10] * wj[10];
        a += a2;
        const unsigned v = nosent(pk(a.x, a.y));
        const v4u o4 = {dppu<0x00>(v), dppu<0x55>(v), dppu<0xAA>(v), dppu<0xFF>(v)};
        if ((lane & 3) == 0) ST128(o4, so);
        STAMP(7);
      }
    } else if (w == 11) {
      __builtin_amdgcn_s_setprio(3);
      ST128(SV, orec + lane * 16);
      if (lane < 8) ST128(SV, orec + 1024 + lane * 16);
      const u64 tag = load_tag(ws);
      const unsigned taghi = (unsigned)(tag >> 16), taglo = (unsigned)(tag << 16), tg = (unsigned)tag;
      const bool isR = i < 8;
      const int ln = g * 8 + i, rb = ln >> 4, pg = lane >> 3, q4 = lane & 7, col = 32 * (ln & 15) + 4 * q4;
      v2u xv = {0u, 0u};
      if (isR) xv = __builtin_amdgcn_raw_buffer_load_b64(rx, (rb * DM + col) * 2, 0, 0);
      DRAIN();
      lds_set(FL + F_CL, tg);
      STAMP(3);
      lds_wait(FL + F_VL + (lane < ntile ? lane : 0), tg);
      if (lane == 0) ST64(hint(taglo, taghi), WS_HA + (g * 32 + i) * 128);
      STAMPE(4);
      if (isR) {
        const rsrc_t ro = mk(out, NB * DM * 2u);
        unsigned pf = __builtin_amdgcn_raw_buffer_load_b32(ro, 0, 0, AUX_SC1);
        asm volatile("" :: "v"(pf));
        v2u hb;
        GSPIN {
          CBAR();
          hb = LD64(WS_HB + ((lane & 7) * 4 + ((ln >> 2) & 3)) * 128, 0);
          if (__all(hint_ok(hb, taglo, taghi))) break;
          __builtin_amdgcn_s_sleep(2);
        }
        STAMP(5);
        wait_age(hb.x, DLY3 + 20);
        v2u hv;
        GSPIN {
          CBAR();
          hv = LD64(WS_HP + (pg * NB + rb) * 1024 + col * 2, 0);
          if (__all(max(hv.x, hv.y) != SENT)) break;
        }
        STAMP(6);
        float h[4] = {blo(hv.x), bhi(hv.x), blo(hv.y), bhi(hv.y)};
#pragma unroll
        for (int d = 0; d < 4; ++d) { h[d] += dpp<ROR8>(h[d]); h[d] = x32_sum(x16_sum(h[d])); }
        {
          const unsigned a0 = pk(h[0], h[1]), a1 = pk(h[2], h[3]);
          const unsigned h0 = pk(blo(xv.x) + blo(a0), bhi(xv.x) + bhi(a0)), h1 = pk(blo(xv.y) + blo(a1), bhi(xv.y) + bhi(a1));
          h[0] = blo(h0); h[1] = bhi(h0); h[2] = blo(h1); h[3] = bhi(h1);
        }
        unsigned po = WS_OP + pg * 8192 + ln * 128 + q4 * 16, po3 = pg < 4 ? po + 24 * 8192 : po, oo = (rb * DM + col) * 2;
        PINV(po); PINV(po3); PINV(oo); PINV(h[0]); PINV(h[1]); PINV(h[2]); PINV(h[3]);
        v2u hc;
        GSPIN {
          CBAR();
          hc = LD64(WS_HC + (lane < NDCU ? lane : 0) * 128, 0);
          if (__all(hint_ok(hc, taglo, taghi))) break;
        }
        wait_age(hc.x, DLY5);
        STAMP(7);
        v4u pv[4];
        GSPIN {
          CBAR();
#pragma unroll
          for (int t = 0; t < 3; ++t) pv[t] = LD128(po, t * 8 * 8192);
          pv[3] = LD128(po3, 0);
          if (__all(okv(pv[0]) & okv(pv[1]) & okv(pv[2]) & okv(pv[3]))) break;
          RETRY();
        }
        STAMP(8);
        float y[4];
#pragma unroll
        for (int d = 0; d < 4; ++d) {
          float t = (__uint_as_float(pv[0][d]) + __uint_as_float(pv[1][d])) + (__uint_as_float(pv[2][d]) + (pg < 4 ? __uint_as_float(pv[3][d]) : 0.f));
          t += dpp<ROR8>(t);
          y[d] = x32_sum(x16_sum(t));
        }
        const unsigned y0 = pk(y[0], y[1]), y1 = pk(y[2], y[3]);
        if (lane < 8) __builtin_amdgcn_raw_buffer_store_b64(v2u{pk(h[0] + blo(y0), h[1] + bhi(y0)), pk(h[2] + blo(y1), h[3] + bhi(y1))}, ro, oo, 0, AUX_SC1);
        if (ln == 0 && lane == 0) ST64((v2u{(unsigned)tag, (unsigned)(tag >> 32)}), WS_EPOCH);
        STAMPE(9);
      }
    }
    PDUMP();
    return;
  }

  const int jq = i - 24;
  const bool isC = jq < 4;
  const int dI = g + 8 * (jq - 4);
  if (!isC && dI >= NDCU) { PDUMP(); return; }
  const u64 tag = load_tag(ws);
  const unsigned taghi = (unsigned)(tag >> 16), taglo = (unsigned)(tag << 16), tg = (unsigned)tag;
#define GATHER_H() do { \
    unsigned klo = 0x00003F80u, khi = 0x3F800000u, ho = WS_HP + w * 256 + lane * 4, oa = L_OA + w * 256 + lane * 4; \
    unsigned hp[8] = {0u, 0u, 0u, 0u, 0u, 0u, 0u, 0u}; \
    float x0 = blo(xp), x1 = bhi(xp); \
    PINS(klo); PINS(khi); PINV(oa); PINV(x0); PINV(x1); \
    ask8(hp, (unsigned)(size_t)(FL + F_GO + (lane & 7)), tg, ho, mkd(ws, WS_END)); \
    if (!__all(max(max(max(hp[0], hp[1]), max(hp[2], hp[3])), max(max(hp[4], hp[5]), max(hp[6], hp[7]))) != SENT)) { \
      GSPIN { \
        RETRY(); \
        CBAR(); \
        _Pragma("unroll") for (int j = 0; j < 8; ++j) hp[j] = LD32(ho, j * 4096); \
        if (__all(max(max(max(hp[0], hp[1]), max(hp[2], hp[3])), max(max(hp[4], hp[5]), max(hp[6], hp[7]))) != SENT)) break; \
      } \
      DRAIN(); \
    } \
    STAMP(7); \
    float a0 = 0.f, a1 = 0.f, a2 = 0.f, a3 = 0.f; \
    _Pragma("unroll") for (int j = 0; j < 4; ++j) { a0 = dot2(hp[j], klo, a0); a1 = dot2(hp[j], khi, a1); a2 = dot2(hp[4 + j], klo, a2); a3 = dot2(hp[4 + j], khi, a3); } \
    const unsigned t0 = pk(a0 + a2, a1 + a3); \
    *(LAS unsigned*)(L + oa) = pk(x0 + blo(t0), x1 + bhi(t0)); \
  } while (0)
  unsigned xp = 0;
  if (!isC) xp = __builtin_amdgcn_raw_buffer_load_b32(rx, w * 256 + lane * 4, 0, 0);
  if (w < 8) {
    const unsigned stride = isC ? DM * 2 : FF * 2;
    const GAS unsigned char* p = isC ? (const GAS unsigned char*)wo + (size_t)(512 * g) * (DM * 2) + (128 * jq + 8 * r16) * 2
                                     : (const GAS unsigned char*)(r16 < 8 ? wg : wu) + (64 * dI + 8 * (r16 & 7)) * 2;
    p += (size_t)(64 * w + 8 * gq) * stride;
    unsigned aa = L_OA + (r16 >> 2) * 1024 + w * 128 + gq * 16, pa = L_PART + w * 2048 + gq * 512 + r16 * 32;
    PINV(aa); PINV(pa);
    v4u src[16], bt[16];
    until(tstart, isC ? TC0 : TD0);
#pragma unroll
    for (int j = 0; j < WPRE; ++j) src[j] = __builtin_nontemporal_load((const GAS v4u*)(p + ((j >> 3) * 32 + (j & 7)) * stride));
    lds_wait(FL + F_QPUB + (lane & 3), tg);
#pragma unroll
    for (int j = WPRE; j < 16; ++j) src[j] = __builtin_nontemporal_load((const GAS v4u*)(p + ((j >> 3) * 32 + (j & 7)) * stride));
    SCHED();
    STAMP(4);
    tr8(src, bt);
    tr8(src + 8, bt + 8);
    if (!isC) {
      const v4u ones = {ONE2, ONE2, ONE2, ONE2};
      float cs[8];
#pragma unroll
      for (int e = 0; e < 8; ++e) cs[e] = mfma(ones, bt[8 + e], mfma(ones, bt[e], Z4))[0];
      if (lane < 16) {
        *(LAS f32x4*)(L + L_SS + w * 512 + lane * 32) = f32x4{cs[0], cs[1], cs[2], cs[3]};
        *(LAS f32x4*)(L + L_SS + w * 512 + lane * 32 + 16) = f32x4{cs[4], cs[5], cs[6], cs[7]};
      }
      lds_set(FL + F_SR + w, tg);
    }
    DRAIN();
    STAMP(5);
    if (w == 0) {
      if (isC) {
        const int hl = lane < 24 ? lane : 0;
        watch(rws, WS_HA + (g * 32 + hl) * 128, taglo, taghi, (LAS v2u*)(L + L_HINT + hl * 8), tg);
      } else {
        watch8(rws, WS_HB + (lane & 31) * 128, taglo, taghi, DLY3, FL + F_GO, tg, lane);
      }
      STAMP(10);
      DRAIN();
    } else if (w == 1 && isC) {
      lds_wait(FL + F_SR + (lane & 7), tg);
      if (lane == 0) ST64(hint(taglo, taghi), WS_HB + (g * 4 + jq) * 128);
    }
    if (!isC) GATHER_H();
    WGBAR();
    STAMP(8);
    {
      const v4u a0 = *(const LAS v4u*)(L + aa), a1 = *(const LAS v4u*)(L + aa + 64);
#pragma unroll
      for (int h = 0; h < 2; ++h) {
        f32x4 acc[4];
#pragma unroll
        for (int e = 0; e < 4; ++e) acc[e] = mfma(a0, bt[4 * h + e], Z4);
#pragma unroll
        for (int e = 0; e < 4; ++e) acc[e] = mfma(a1, bt[8 + 4 * h + e], acc[e]);
        *(LAS f32x4*)(L + pa + 16 * h) = f32x4{acc[0][0], acc[1][0], acc[2][0], acc[3][0]};
      }
    }
    STAMP(9);
    WGBAR();
    if (!isC) WGBAR();
  } else if (isC) {
    const int v = w & 7, bb = v >> 1, head = 2 * (v & 1) + (lane >> 5), sg = gq & 1;
    unsigned ra = L_PART + (w & 3) * 512 + lane * 8, so = WS_HP + (g * NB + (w & 3)) * 1024 + jq * 256 + lane * 4;
    unsigned rd = WS_OREC + (g * 24 + bb * NSPL + sg) * OREC_SZ + head * 256 + r16 * 16, rs = WS_OREC + (g * 24 + bb * NSPL + sg) * OREC_SZ + 1024 + head * 8;
    unsigned oa = L_OA + bb * 1024 + head * 256 + r16 * 16 + sg * 8;
    PINV(ra); PINV(so); PINV(rd); PINV(rs); PINV(oa);
    if (w == 15) ST128(SV, WS_HP + (g * NB + gq) * 1024 + jq * 256 + r16 * 16);
    DRAIN();
    STAMP(5);
    {
      v2u e;
#pragma nounroll
      for (unsigned sp = 0; sp < SPIN_L; ++sp) { e = *(volatile LAS v2u*)(L + L_HINT + (bb * NSPL + (lane < NSPL ? lane : 0)) * 8); if (__all(e.x == tg)) break; }
      wait_age(e.y, DLY2);
    }
    STAMP(6);
    GSPIN {
      v4u od[3];
      v2u st[3];
      CBAR();
#pragma unroll
      for (int u = 0; u < 3; ++u) st[u] = LD64(rs, 2 * u * OREC_SZ);
      SCHED();
#pragma unroll
      for (int u = 0; u < 3; ++u) od[u] = LD128(rd, 2 * u * OREC_SZ);
      SCHED();
      lds_set(FL + F_SR + v, tg);
      SCHED();
      unsigned bad = max(max(max(st[0].x, st[0].y), max(st[1].x, st[1].y)), max(st[2].x, st[2].y));
      const float m0 = __uint_as_float(st[0].x), m1 = __uint_as_float(st[1].x), m2 = __uint_as_float(st[2].x);
      const float mg = x16_max(fmaxf(fmaxf(m0, m1), m2));
      float wt[3] = {__builtin_amdgcn_exp2f(m0 - mg), __builtin_amdgcn_exp2f(m1 - mg), __builtin_amdgcn_exp2f(m2 - mg)};
      const float inv = __builtin_amdgcn_rcpf(x16_sum(fmaf(wt[0], __uint_as_float(st[0].y), fmaf(wt[1], __uint_as_float(st[1].y), wt[2] * __uint_as_float(st[2].y)))));
#pragma unroll
      for (int u = 0; u < 3; ++u) { wt[u] *= inv; PINV(wt[u]); }
      PINV(bad);
      SCHED();
      f32x2 a[4];
#pragma unroll
      for (int u = 0; u < 3; ++u) {
        f32x2 t[4];
#pragma unroll
        for (int d = 0; d < 4; ++d) { t[d] = f32x2{blo(od[u][d]), bhi(od[u][d])}; PINV(t[d]); }
        bad = max(bad, max(max(od[u].x, od[u].y), max(od[u].z, od[u].w)));
#pragma unroll
        for (int d = 0; d < 4; ++d) a[d] = u == 0 ? t[d] * wt[0] : t[d] * wt[u] + a[d];
      }
      float r[4];
#pragma unroll
      for (int d = 0; d < 4; ++d) {
        const auto sw = __builtin_amdgcn_permlane16_swap(__float_as_uint(a[d >> 1][d & 1]), __float_as_uint(a[2 + (d >> 1)][d & 1]), false, false);
        r[d] = __uint_as_float(sw[0]) + __uint_as_float(sw[1]);
      }
      *(LAS v2u*)(L + oa) = v2u{pk(r[0], r[1]), pk(r[2], r[3])};
      STAMP(7);
      if (__all(bad != SENT)) break;
      RETRY();
    }
    DRAIN();
    STAMP(8);
    WGBAR();
    WGBAR();
    if (w >= 12) {
      f32x2 pv[8];
#pragma unroll
      for (int j = 0; j < 8; ++j) pv[j] = *(const LAS f32x2*)(L + ra + j * 2048);
      SCHED();
      const f32x2 t = ((pv[0] + pv[1]) + (pv[2] + pv[3])) + ((pv[4] + pv[5]) + (pv[6] + pv[7]));
      const unsigned vv = nosent(pk(t.x, t.y));
      const v4u o4 = {dppu<0x00>(vv), dppu<0x55>(vv), dppu<0xAA>(vv), dppu<0xFF>(vv)};
      if ((lane & 3) == 0) ST128(o4, so);
      STAMPE(9);
    }
  } else {
    unsigned ra = L_PART + (w & 3) * 512 + lane * 4, hw = L_HH + (w & 3) * 128 + lane * 2, ha = L_HH + (r16 >> 2) * 128 + gq * 16;
    unsigned so = WS_OP + dI * 8192 + gq * 2048 + (w & 7) * 256 + r16 * 16, sa = L_OA + (w & 3) * 1024 + lane * 16;
    PINV(ra); PINV(hw); PINV(ha); PINV(so); PINV(sa);
    ST128(SV, WS_OP + dI * 8192 + (w - 8) * 1024 + lane * 16);
    until(tstart, TWD);
    const rsrc_t rw = mk(wd, FF * DM * 2u);
    v2u src[16];
    v4u bt[8];
#pragma unroll
    for (int j = 0; j < 16; ++j) src[j] = __builtin_amdgcn_raw_buffer_load_b64(rw, (unsigned)(8 * gq + (j & 3)) * 1024 + (w - 8) * 128 + r16 * 8, (64 * dI + (j >> 3) * 32 + (j & 4)) * 1024, AUX_NT);
    SCHED();
    tr4(src, bt);
    tr4(src + 8, bt + 4);
    float sgs = 0.f, sus = 0.f;
    if (w >= 12) {
      lds_wait(FL + F_SR + (lane & 7), tg);
#pragma unroll
      for (int j = 0; j < 8; ++j) { sgs += *(const LAS float*)(L + L_SS + j * 512 + lane * 4); sus += *(const LAS float*)(L + L_SS + j * 512 + 256 + lane * 4); }
    }
    PINV(sgs); PINV(sus);
    DRAIN();
    STAMP(5);
    GATHER_H();
    WGBAR();
    if (w >= 12) {
      const f32x2 st = ln_stats(*(const LAS v4u*)(L + sa));
      float kg = st.y * -1.4426950408889634f, cg = st.x * sgs, cu = st.x * sus, ku = st.y;
      PINV(kg); PINV(cg); PINV(cu); PINV(ku);
      WGBAR();
      float gp[8], up[8];
#pragma unroll
      for (int j = 0; j < 8; ++j) { gp[j] = *(const LAS float*)(L + ra + j * 2048); up[j] = *(const LAS float*)(L + ra + j * 2048 + 256); }
      SCHED();
      const float gg = ((gp[0] + gp[1]) + (gp[2] + gp[3])) + ((gp[4] + gp[5]) + (gp[6] + gp[7]));
      const float uu = ((up[0] + up[1]) + (up[2] + up[3])) + ((up[4] + up[5]) + (up[6] + up[7]));
      const float hh = ku * (uu - cu) * __builtin_amdgcn_rcpf(1.f + __builtin_amdgcn_exp2f(kg * (gg - cg)));
      *(LAS u16*)(L + hw) = (u16)pk(hh, hh);
    } else {
      if (w == 8 && lane == 0) ST64(hint(taglo, taghi), WS_HC + dI * 128);
      WGBAR();
    }
    WGBAR();
    STAMP(8);
    const v4u a0 = *(const LAS v4u*)(L + ha), a1 = *(const LAS v4u*)(L + ha + 64);
    f32x4 acc[4];
#pragma unroll
    for (int e = 0; e < 4; ++e) acc[e] = mfma(a0, bt[e], Z4);
#pragma unroll
    for (int e = 0; e < 4; ++e) acc[e] = mfma(a1, bt[4 + e], acc[e]);
    ST128((v4u{nosent(__float_as_uint(acc[0][0])), nosent(__float_as_uint(acc[1][0])), nosent(__float_as_uint(acc[2][0])), nosent(__float_as_uint(acc[3][0]))}), so);
    STAMPE(9);
  }
  PDUMP();
}
}

extern "C" void kernel_launch(void* const* d_in, const int* in_sizes, int n_in, void* d_out, int out_size, void* d_ws, size_t ws_size, hipStream_t stream) {
  (void)in_sizes; (void)n_in; (void)out_size; (void)ws_size;
  fused_decode<<<dim3(256), dim3(1024), 0, stream>>>((const u16*)d_in[0], (const u16*)d_in[1], (const u16*)d_in[2], (const u16*)d_in[3], (const u16*)d_in[4],
                                                     (const u16*)d_in[6], (const u16*)d_in[7], (const u16*)d_in[8], (u16*)d_out, (unsigned char*)d_ws);
}
```

```cpp
#include <hip/hip_runtime.h>
#include <stdint.h>

#define PROF 1
#define TK0 80
#define KPRE 4
#define QDELTA 70
#define TC0 245
#define TD0 100
#define KMID 1
#define KLEFT 2
#define VPRE 0
#define WPRE 3
#define TWD 450
#define DLY2 60
#define DLY3 85
#define DLY5 76

namespace {
typedef unsigned short u16;
typedef unsigned long long u64;
typedef unsigned v4u __attribute__((ext_vector_type(4)));
typedef unsigned v2u __attribute__((ext_vector_type(2)));
typedef int v4i __attribute__((ext_vector_type(4)));
typedef float f32x4 __attribute__((ext_vector_type(4)));
typedef float f32x2 __attribute__((ext_vector_type(2)));
typedef short bf16x8 __attribute__((ext_vector_type(8)));
typedef __bf16 bf16x2 __attribute__((ext_vector_type(2)));
#define GAS __attribute__((address_space(1)))
#define LAS __attribute__((address_space(3)))
typedef __amdgpu_buffer_rsrc_t rsrc_t;

constexpr int NB = 4, DM = 512, HKV = 8, SK = 8192, WIN = 2048, FF = 1792, NQ = 4096;
constexpr int NSPL = 6, NDCU = 28;
constexpr int AUX_NT = 2, AUX_SC1 = 16;
constexpr unsigned SENT = 0xFFFFFFFFu, ONE2 = 0x3F803F80u;
constexpr int WS_EPOCH = 0, WS_HB = 4096, WS_HC = WS_HB + 32 * 128, WS_HA = WS_HC + 32 * 128, WS_QG = WS_HA + 8 * 32 * 128;
constexpr int WS_OREC = WS_QG + 8 * 4 * 4 * 256 * 8, OREC_SZ = 1152;
constexpr int WS_HP = WS_OREC + 8 * 24 * OREC_SZ, WS_OP = WS_HP + 8 * 4 * 512 * 2, WS_END = WS_OP + NDCU * 8192;
constexpr int WS_PROF = 2 << 20;
constexpr int L_FLAG = 0, L_MW = 512, L_HINT = 512, L_QS = 1024, L_OA = L_QS + 1024, L_HH = L_OA + 4096, L_SS = L_HH + 512;
constexpr int L_QRED = L_SS + 4096, L_PART = L_QRED + 5120, L_USED = L_PART + 11 * 2048;
constexpr int L_TOTAL = 84 * 1024;
static_assert(L_USED <= L_TOTAL, "lds");
enum { F_QIS = 0, F_QS = 4, F_QPUB = 12, F_QR = 16, F_CL = 24, F_GO = 32, F_SR = 72, F_MW = 80, F_VL = 96, F_OPF = 112 };
constexpr int CODE_BYTES = 15360;
constexpr unsigned SPIN_G = 1u << 17, SPIN_L = 1u << 21;

__device__ __forceinline__ u64 rt() { return __builtin_amdgcn_s_memrealtime(); }
__device__ __forceinline__ rsrc_t mk(const void* p, unsigned bytes) { return __builtin_amdgcn_make_buffer_rsrc((void*)p, 0, bytes, 0x00020000); }
__device__ __forceinline__ f32x4 mfma(v4u a, v4u b, f32x4 c) { return __builtin_amdgcn_mfma_f32_16x16x32_bf16(__builtin_bit_cast(bf16x8, a), __builtin_bit_cast(bf16x8, b), c, 0, 0, 0); }
__device__ __forceinline__ unsigned pk(float lo, float hi) { return __builtin_bit_cast(unsigned, __builtin_convertvector(f32x2{lo, hi}, bf16x2)); }
__device__ __forceinline__ float blo(unsigned v) { return __builtin_bit_cast(float, v << 16); }
__device__ __forceinline__ float bhi(unsigned v) { return __builtin_bit_cast(float, v & 0xffff0000u); }
__device__ __forceinline__ unsigned plo(unsigned a, unsigned b) { return __builtin_amdgcn_perm(b, a, 0x05040100u); }
__device__ __forceinline__ unsigned phi(unsigned a, unsigned b) { return __builtin_amdgcn_perm(b, a, 0x07060302u); }
__device__ __forceinline__ unsigned nosent(unsigned v) { return v < 0xFFFFFFFEu ? v : 0xFFFFFFFEu; }
template <int C> __device__ __forceinline__ float dpp(float x) { return __builtin_bit_cast(float, __builtin_amdgcn_mov_dpp(__builtin_bit_cast(int, x), C, 0xf, 0xf, true)); }
template <int C> __device__ __forceinline__ unsigned dppu(unsigned x) { return (unsigned)__builtin_amdgcn_mov_dpp((int)x, C, 0xf, 0xf, true); }
constexpr int XOR1 = 0xB1, XOR2 = 0x4E, ROR4 = 0x124, ROR8 = 0x128;
__device__ __forceinline__ float x16_sum(float x) { auto s = __builtin_amdgcn_permlane16_swap(__float_as_uint(x), __float_as_uint(x), false, false); return __uint_as_float(s[0]) + __uint_as_float(s[1]); }
__device__ __forceinline__ float x32_sum(float x) { auto s = __builtin_amdgcn_permlane32_swap(__float_as_uint(x), __float_as_uint(x), false, false); return __uint_as_float(s[0]) + __uint_as_float(s[1]); }
__device__ __forceinline__ float x16_max(float x) { auto s = __builtin_amdgcn_permlane16_swap(__float_as_uint(x), __float_as_uint(x), false, false); return fmaxf(__uint_as_float(s[0]), __uint_as_float(s[1])); }
__device__ __forceinline__ float x32_max(float x) { auto s = __builtin_amdgcn_permlane32_swap(__float_as_uint(x), __float_as_uint(x), false, false); return fmaxf(__uint_as_float(s[0]), __uint_as_float(s[1])); }
__device__ __forceinline__ float row_sum(float x) { x += dpp<XOR1>(x); x += dpp<XOR2>(x); x += dpp<ROR4>(x); x += dpp<ROR8>(x); return x; }
__device__ __forceinline__ float row_max(float x) { x = fmaxf(x, dpp<XOR1>(x)); x = fmaxf(x, dpp<XOR2>(x)); x = fmaxf(x, dpp<ROR4>(x)); x = fmaxf(x, dpp<ROR8>(x)); return x; }
__device__ __forceinline__ float dot2(unsigned a, unsigned b, float c) { return __builtin_amdgcn_fdot2_f32_bf16(__builtin_bit_cast(bf16x2, a), __builtin_bit_cast(bf16x2, b), c, false); }
#define CBAR() asm volatile("" ::: "memory")
#define SCHED() __builtin_amdgcn_sched_barrier(0)
#define DRAIN() do { asm volatile("s_waitcnt vmcnt(0)" ::: "memory"); __builtin_amdgcn_s_waitcnt(0x0F70); } while (0)
#define PINV(x) asm volatile("" : "+v"(x))
#define PINS(x) asm volatile("" : "+s"(x))
#define WGBAR() do { SCHED(); asm volatile("s_waitcnt lgkmcnt(0)" ::: "memory"); __builtin_amdgcn_s_barrier(); CBAR(); SCHED(); } while (0)
#define GSPIN _Pragma("nounroll") for (unsigned sp = 0; sp < SPIN_G; ++sp)
#define LD32(o, so) __builtin_amdgcn_raw_buffer_load_b32(rws, (o), (so), AUX_SC1)
#define LD64(o, so) __builtin_amdgcn_raw_buffer_load_b64(rws, (o), (so), AUX_SC1)
#define LD128(o, so) __builtin_amdgcn_raw_buffer_load_b128(rws, (o), (so), AUX_SC1)
#define ST64(v, o) __builtin_amdgcn_raw_buffer_store_b64((v), rws, (o), 0, AUX_SC1)
#define ST128(v, o) __builtin_amdgcn_raw_buffer_store_b128((v), rws, (o), 0, AUX_SC1)
__device__ __forceinline__ bool okv(v4u r) { return max(max(r.x, r.y), max(r.z, r.w)) != SENT; }
__device__ __forceinline__ void lds_wait(volatile LAS unsigned* f, unsigned tg) {
  SCHED();
#pragma nounroll
  for (unsigned s = 0; s < SPIN_L; ++s) { if (__all(*f == tg)) break; }
  CBAR();
  SCHED();
}
__device__ __forceinline__ void lds_set(volatile LAS unsigned* f, unsigned tg) { CBAR(); *f = tg; CBAR(); }
__device__ __forceinline__ void until(u64 t0, unsigned ticks) { while (rt() - t0 < ticks) __builtin_amdgcn_s_sleep(1); }
__device__ __forceinline__ v2u hint(unsigned taglo, unsigned taghi) { return v2u{taglo | ((unsigned)rt() & 0xFFFFu), taghi}; }
__device__ __forceinline__ bool hint_ok(v2u h, unsigned taglo, unsigned taghi) { return (h.y == taghi) & ((h.x ^ taglo) < 65536u); }
__device__ __forceinline__ void wait_age(unsigned t16, unsigned dly) {
#pragma nounroll
  for (unsigned s = 0; s < 4096u; ++s) { if (__all((((unsigned)rt() - t16) & 0xFFFFu) >= dly)) break; }
}
__device__ __forceinline__ void watch(rsrc_t rws, unsigned ho, unsigned taglo, unsigned taghi, LAS v2u* slot, unsigned tg) {
  GSPIN {
    CBAR();
    const v2u h = LD64(ho, 0);
    const bool ok = hint_ok(h, taglo, taghi);
    if (ok) *slot = v2u{tg, h.x};
    CBAR();
    if (__all(ok)) break;
  }
}
__device__ __forceinline__ void watch8(rsrc_t rws, unsigned ho, unsigned taglo, unsigned taghi, unsigned dly, volatile LAS unsigned* go, unsigned tg, int lane) {
  v2u h;
  unsigned seen = 0, done = 0;
#pragma nounroll
  for (unsigned sp = 0; sp < SPIN_G && done != 0x11111111u; ++sp) {
    if (seen != ~0u) {
      CBAR();
      h = LD64(ho, 0);
      seen = (unsigned)__builtin_amdgcn_ballot_w64(hint_ok(h, taglo, taghi));
    }
    unsigned m = (unsigned)__builtin_amdgcn_ballot_w64((((unsigned)rt() - h.x) & 0xFFFFu) >= dly) & seen;
    m &= m >> 1; m &= m >> 2; m &= 0x11111111u;
    if (m != done) { if ((m >> (4 * (lane & 7))) & 1u) go[lane & 7] = tg; done = m; }
  }
  CBAR();
}
__device__ __forceinline__ u64 load_tag(const unsigned char* ws) {
  u64 e;
  asm volatile("s_load_dwordx2 %0, %1, 0x0\n\ts_waitcnt lgkmcnt(0)" : "=s"(e) : "s"(ws) : "memory");
  return (e + 1ull) & 0xFFFFFFFFFFFFull;
}
__device__ __forceinline__ void code_fetch(int part) {
  u64 pc0;
  asm volatile("s_getpc_b64 %0" : "=s"(pc0));
  const u64 a0 = (pc0 & ~127ull) + (u64)(part * 512 < CODE_BYTES - 512 ? part * 512 : CODE_BYTES - 512);
  unsigned t0, t1, t2, t3, t4, t5, t6, t7;
  asm volatile("s_load_dword %0, %8, 0x0\n\ts_load_dword %1, %8, 0x40\n\ts_load_dword %2, %8, 0x80\n\ts_load_dword %3, %8, 0xc0\n\t"
               "s_load_dword %4, %8, 0x100\n\ts_load_dword %5, %8, 0x140\n\ts_load_dword %6, %8, 0x180\n\ts_load_dword %7, %8, 0x1c0\n\ts_waitcnt lgkmcnt(0)"
               : "=&s"(t0), "=&s"(t1), "=&s"(t2), "=&s"(t3), "=&s"(t4), "=&s"(t5), "=&s"(t6), "=&s"(t7) : "s"(a0) : "memory");
}
__device__ __forceinline__ v4i mkd(const void* p, unsigned bytes) { return v4i{(int)(u64)p, (int)((u64)p >> 32) & 0xFFFF, (int)bytes, 0x00020000}; }
template <int OFF> __device__ __forceinline__ void ldnt(v4u& d, unsigned vo, v4i rs, unsigned so) {
  asm volatile("buffer_load_dwordx4 %0, %1, %2, %3 offen offset:%4 nt" : "=v"(d) : "v"(vo), "s"(rs), "s"(so), "i"(OFF) : "memory");
}
template <int LO, int HI> __device__ __forceinline__ void ldtile(v4u* d, unsigned vo, v4i rs, unsigned so, unsigned so2) {
  if constexpr (LO < HI) { ldnt<(LO & 3) * 1024>(d[LO], vo, rs, LO < 4 ? so : so2); ldtile<LO + 1, HI>(d, vo, rs, so, so2); }
}
__device__ __forceinline__ void ldnt2(v2u& d, unsigned vo, v4i rs, unsigned so) {
  asm volatile("buffer_load_dwordx2 %0, %1, %2, %3 offen nt" : "=v"(d) : "v"(vo), "s"(rs), "s"(so) : "memory");
}
__device__ __forceinline__ void ldp4(v4u& d, unsigned vo, v4i rs) {
  asm volatile("buffer_load_dwordx4 %0, %1, %2, 0 offen" : "=v"(d) : "v"(vo), "s"(rs) : "memory");
}
#define VMWAIT2(n, a, b) asm volatile("s_waitcnt vmcnt(%2)" : "+v"(a), "+v"(b) : "i"(n) : "memory")
__device__ __forceinline__ void ask8(unsigned (&hp)[8], unsigned go, unsigned tg, unsigned ho, v4i rs) {
  unsigned t, done, n, cnt;
#define ASK1(j, off) "s_bitcmp1_b32 %10, " #j "\n\ts_cbranch_scc0 2" #j "f\n\ts_movk_i32 %11, " #off "\n\tbuffer_load_dword %" #j ", %14, %15, %11 offen sc1\n2" #j ":\n\t"
  asm volatile("s_mov_b32 %9, 0\n\ts_mov_b32 %11, 0x200000\n"
               "1:\n\tds_read_b32 %8, %12\n\ts_sub_u32 %11, %11, 1\n\ts_cbranch_scc1 3f\n\ts_waitcnt lgkmcnt(0)\n\t"
               "v_cmp_eq_u32 vcc, %13, %8\n\ts_andn2_b32 %10, vcc_lo, %9\n\ts_and_b32 %10, %10, 0xff\n\ts_cbranch_scc0 1b\n\t"
               ASK1(0, 0) ASK1(1, 0x1000) ASK1(2, 0x2000) ASK1(3, 0x3000) ASK1(4, 0x4000) ASK1(5, 0x5000) ASK1(6, 0x6000) ASK1(7, 0x7000)
               "s_mov_b32 %11, 0x200000\n\ts_or_b32 %9, %9, %10\n\ts_cmp_lg_u32 %9, 0xff\n\ts_cbranch_scc1 1b\n"
               "3:\n\ts_waitcnt vmcnt(0) lgkmcnt(0)"
               : "+v"(hp[0]), "+v"(hp[1]), "+v"(hp[2]), "+v"(hp[3]), "+v"(hp[4]), "+v"(hp[5]), "+v"(hp[6]), "+v"(hp[7]), "=&v"(t), "=&s"(done), "=&s"(n), "=&s"(cnt)
               : "v"(go), "s"(tg), "v"(ho), "s"(rs) : "vcc", "scc", "memory");
#undef ASK1
}
__device__ __forceinline__ f32x2 ln_stats(v4u hv) {
  const float c0 = blo((unsigned)__builtin_amdgcn_readfirstlane(hv[0]));
  float t[8];
#pragma unroll
  for (int d = 0; d < 4; ++d) { t[2 * d] = blo(hv[d]) - c0; t[2 * d + 1] = bhi(hv[d]) - c0; }
  float s1 = ((t[0] + t[1]) + (t[2] + t[3])) + ((t[4] + t[5]) + (t[6] + t[7]));
  float s2 = (fmaf(t[1], t[1], t[0] * t[0]) + fmaf(t[3], t[3], t[2] * t[2])) + (fmaf(t[5], t[5], t[4] * t[4]) + fmaf(t[7], t[7], t[6] * t[6]));
  s1 += dpp<XOR1>(s1); s2 += dpp<XOR1>(s2);
  s1 += dpp<XOR2>(s1); s2 += dpp<XOR2>(s2);
  s1 += dpp<ROR4>(s1); s2 += dpp<ROR4>(s2);
  s1 += dpp<ROR8>(s1); s2 += dpp<ROR8>(s2);
  s1 = x16_sum(s1); s2 = x16_sum(s2);
  s1 = x32_sum(s1); s2 = x32_sum(s2);
  const float d = s1 * (1.f / DM), var = fmaxf(fmaf(-d, d, s2 * (1.f / DM)), 0.f);
  return f32x2{c0 + d, __builtin_amdgcn_rsqf(var + 1e-5f)};
}
#define STAMP(k) do { if (PROF == 1) { __builtin_amdgcn_sched_barrier(0); ts[k] = (unsigned)rt(); __builtin_amdgcn_sched_barrier(0); } } while (0)
#define STAMPE(k) do { __builtin_amdgcn_sched_barrier(0); ts[k] = (unsigned)rt(); __builtin_amdgcn_sched_barrier(0); } while (0)
#define PDUMP() do { until(tstart, 2500); if (lane == 0) { _Pragma("unroll") for (int k_ = 0; k_ < 12; ++k_) __hip_atomic_store((GAS unsigned*)(ws + WS_PROF + (((size_t)c * 16 + w) * 16 + k_) * 4), ts[k_], __ATOMIC_RELAXED, __HIP_MEMORY_SCOPE_AGENT); } \
  if (c == 0 && w == 11) { until(tstart, 4000); int mn = 0, mx = -100000; \
    for (int u = lane; u < 4096; u += 64) { const int cc = u >> 4, ww = u & 15, ii = cc >> 3; \
      const unsigned t0_ = __hip_atomic_load((GAS unsigned*)(ws + WS_PROF + (size_t)u * 64), __ATOMIC_RELAXED, __HIP_MEMORY_SCOPE_AGENT); \
      const unsigned tk_ = __hip_atomic_load((GAS unsigned*)(ws + WS_PROF + (size_t)u * 64 + 0 * 4), __ATOMIC_RELAXED, __HIP_MEMORY_SCOPE_AGENT); \
      const bool dumped = !(ii == 31 && (cc & 7) >= 4 && false); \
      if (dumped) mn = min(mn, (int)(t0_ - (unsigned)tstart)); \
      if (ii >= 0 && ii <= 31 && ww >= 0 && ww <= 15 && tk_ != 0u) mx = max(mx, (int)(tk_ - (unsigned)tstart)); } \
    for (int m_ = 32; m_ >= 1; m_ >>= 1) { mn = min(mn, __shfl_xor(mn, m_)); mx = max(mx, __shfl_xor(mx, m_)); } \
    mn = __builtin_amdgcn_readfirstlane(mn); mx = __builtin_amdgcn_readfirstlane(mx); \
    int mv = mx - mn; mv = mv < 0 ? 0 : mv > 3000 ? 3000 : mv; \
    until(tstart, (unsigned)(6000 + mn + 10 * mv)); } } while (0)
#define RETRY() ts[11]++

__device__ __forceinline__ void tr4(const v2u* s, v4u* o) {
#pragma unroll
  for (int m = 0; m < 4; ++m) {
    o[0][m] = plo(s[2 * m].x, s[2 * m + 1].x); o[1][m] = phi(s[2 * m].x, s[2 * m + 1].x);
    o[2][m] = plo(s[2 * m].y, s[2 * m + 1].y); o[3][m] = phi(s[2 * m].y, s[2 * m + 1].y);
  }
#pragma unroll
  for (int e = 0; e < 4; ++e) PINV(o[e]);
}
__device__ __forceinline__ void tr8(const v4u* s, v4u* o) {
#pragma unroll
  for (int m = 0; m < 4; ++m)
#pragma unroll
    for (int d = 0; d < 4; ++d) { o[2 * d][m] = plo(s[2 * m][d], s[2 * m + 1][d]); o[2 * d + 1][m] = phi(s[2 * m][d], s[2 * m + 1][d]); }
#pragma unroll
  for (int e = 0; e < 8; ++e) PINV(o[e]);
}

__global__ void __launch_bounds__(1024) fused_decode(const u16* __restrict__ x, const u16* __restrict__ kc, const u16* __restrict__ vc,
                                                     const u16* __restrict__ wq, const u16* __restrict__ wo, const u16* __restrict__ wg,
                                                     const u16* __restrict__ wu, const u16* __restrict__ wd, u16* __restrict__ out, unsigned char* ws) {
  const u64 tstart = rt();
  unsigned ts[12];
#pragma unroll
  for (int k_ = 0; k_ < 12; ++k_) ts[k_] = 0;
  ts[0] = (unsigned)tstart;
  SCHED();
  __shared__ __attribute__((aligned(16))) unsigned char smem[L_TOTAL];
  LAS unsigned char* L = (LAS unsigned char*)smem;
  volatile LAS unsigned* FL = (volatile LAS unsigned*)(L + L_FLAG);
  const int lane = threadIdx.x & 63;
  int w = (__builtin_amdgcn_readfirstlane(threadIdx.x >> 6) + 11) & 15;
  const int c = blockIdx.x, g = c & 7, i = c >> 3;
  if (w == 11) code_fetch(i);
  const int gq = lane >> 4, r16 = lane & 15;
  const int b = (i * 43) >> 8, s = i - b * NSPL;
  constexpr int ntile = 11;
  const int row0 = 344 * s - (s > 4 ? 8 : 0);
  const int cw = w - 12, rq = i & 3, cq = i >> 2, R0 = 128 * rq + 32 * cw;
  asm volatile("" :: "s"(x), "s"(kc), "s"(vc), "s"(wq), "s"(wo), "s"(wg), "s"(wu), "s"(wd), "s"(out), "s"(ws));
  v4u kf[8], vf[8], xa, xr;
  v2u wsrc[8];
  const unsigned kvbase = ((unsigned)(b * HKV + g) * SK + (SK - WIN) + row0 + 16u * w) * 256u, kvbase2 = kvbase + ntile * 4096u;
  const v4i rk = mkd(kc, NB * HKV * SK * 256u), rv = mkd(vc, NB * HKV * SK * 256u);
  if (w >= 12) {
    __builtin_amdgcn_s_setprio(3);
    const v4i rw = mkd(wq, DM * NQ * 2u), rxd = mkd(x, NB * DM * 2u);
    const unsigned o = (unsigned)(8 * gq) * (NQ * 2) + r16 * 8, so = R0 * (NQ * 2) + (512 * g + 64 * cq) * 2;
#pragma unroll
    for (int j = 0; j < 8; ++j) ldnt2(wsrc[j], o, rw, so + j * (NQ * 2));
    ldp4(xa, (r16 >> 2) * 1024 + (R0 + 8 * gq) * 2, rxd);
    ldp4(xr, cw * 1024 + lane * 16, rxd);
  } else if (i < 24 && w < ntile) {
    __builtin_amdgcn_s_setprio(2);
    until(tstart, TK0);
    ldtile<0, KPRE>(kf, r16 * 64 + gq * 16, rk, kvbase, kvbase2);
  }
  STAMP(1);
  PINS(w);
  const rsrc_t rws = mk(ws, WS_END), rx = mk(x, NB * DM * 2u);
  const f32x4 Z4 = {0.f, 0.f, 0.f, 0.f};
  const v4u SV = {SENT, SENT, SENT, SENT};
  const unsigned orec = WS_OREC + (g * 24 + i) * OREC_SZ;

  if (__builtin_expect(w >= 12, 1)) {
    const unsigned tg = (unsigned)load_tag(ws);
    asm volatile("s_waitcnt vmcnt(0)" : "+v"(wsrc[0]), "+v"(wsrc[1]), "+v"(wsrc[2]), "+v"(wsrc[3]), "+v"(wsrc[4]), "+v"(wsrc[5]), "+v"(wsrc[6]), "+v"(wsrc[7]), "+v"(xa), "+v"(xr) :: "memory");
    {
      v4u bt[4];
      tr4(wsrc, bt);
      if ((lane & 3) == 1) xa = v4u{ONE2, ONE2, ONE2, ONE2};
      f32x4 qp, cs;
#pragma unroll
      for (int e = 0; e < 4; ++e) { const f32x4 d = mfma(xa, bt[e], Z4); qp[e] = d[0]; cs[e] = d[1]; }
      *(LAS f32x4*)(L + L_QRED + cw * 1024 + lane * 16) = qp;
      if (lane < 16) *(LAS f32x4*)(L + L_QRED + 4096 + cw * 256 + lane * 16) = cs;
    }
    lds_set(FL + F_QR + cw, tg);
    f32x2 xst = ln_stats(xr);
    PINV(xst);
    STAMP(2);
    lds_wait(FL + F_QR + (lane & 3), tg);
    {
      float a[4], cs[4];
#pragma unroll
      for (int j = 0; j < 4; ++j) { a[j] = *(const LAS float*)(L + L_QRED + j * 1024 + cw * 256 + lane * 4); cs[j] = *(const LAS float*)(L + L_QRED + 4096 + j * 256 + lane * 4); }
      const float q = xst.y * (((a[0] + a[1]) + (a[2] + a[3])) - xst.x * ((cs[0] + cs[1]) + (cs[2] + cs[3])));
      const unsigned q2 = pk(q, dpp<XOR1>(q));
      if ((lane & 1) == 0) ST64((v2u{q2, tg}), WS_QG + (((g * NB + cw) * 4 + rq) * 256 + 32 * cq) * 8 + lane * 4);
    }
    lds_set(FL + F_QPUB + cw, tg);
    STAMP(3);
    if (i < 24) {
      until(rt(), QDELTA);
      const unsigned qo = WS_QG + ((g * NB + b) * 4 * 256 + cw * 64 + lane) * 8;
      v2u qg[4];
      GSPIN {
        CBAR();
#pragma unroll
        for (int j = 0; j < 4; ++j) qg[j] = LD64(qo, j * 2048);
        SCHED();
        lds_set(FL + F_QIS + cw, tg);
        SCHED();
        if (__all((qg[0].y == tg) & (qg[1].y == tg) & (qg[2].y == tg) & (qg[3].y == tg))) break;
        RETRY();
      }
      const float q0 = (blo(qg[0].x) + blo(qg[1].x)) + (blo(qg[2].x) + blo(qg[3].x)), q1 = (bhi(qg[0].x) + bhi(qg[1].x)) + (bhi(qg[2].x) + bhi(qg[3].x));
      *(LAS unsigned*)(L + L_QS + (cw * 64 + lane) * 4) = pk(q0, q1);
      lds_set(FL + F_QS + cw, tg);
      STAMP(4);
      PDUMP();
      return;
    }
    __builtin_amdgcn_s_setprio(0);
  } else if (i < 24) {
    if (w < ntile) {
      const unsigned tg = (unsigned)load_tag(ws);
      const int nval = w < 10 ? 8 : s < 4 ? 6 : 4;
      const unsigned oob6 = nval < 6 ? 0x80000000u : 0u, oob8 = nval < 8 ? 0x80000000u : 0u;
      static_assert(KPRE == 4, "");
      if (KMID >= 1) { asm volatile("s_waitcnt vmcnt(%0)" :: "i"(KLEFT) : "memory"); ldtile<4, 6>(kf, (r16 * 64 + gq * 16) | oob6, rk, kvbase, kvbase2); }
      if (KMID >= 2) { asm volatile("s_waitcnt vmcnt(%0)" :: "i"(KLEFT) : "memory"); ldtile<6, 8>(kf, (r16 * 64 + gq * 16) | oob8, rk, kvbase, kvbase2); }
      lds_wait(FL + F_QPUB + (lane & 3), tg);
      if (KMID < 1) ldtile<4, 6>(kf, (r16 * 64 + gq * 16) | oob6, rk, kvbase, kvbase2);
      if (KMID < 2) ldtile<6, 8>(kf, (r16 * 64 + gq * 16) | oob8, rk, kvbase, kvbase2);
      if (VPRE) ldtile<0, VPRE>(vf, lane * 16, rv, kvbase, kvbase2);
      __builtin_amdgcn_s_setprio(0);
      lds_wait(FL + F_QIS + (lane & 3), tg);
      ldtile<VPRE, 4>(vf, lane * 16, rv, kvbase, kvbase2);
      ldtile<4, 6>(vf, (lane * 16) | oob6, rv, kvbase, kvbase2);
      ldtile<6, 8>(vf, (lane * 16) | oob8, rv, kvbase, kvbase2);
      STAMP(2);
      lds_wait(FL + (lane < 4 ? F_QS + lane : F_CL), tg);
      asm volatile("s_waitcnt vmcnt(8)" : "+v"(kf[0]), "+v"(kf[1]), "+v"(kf[2]), "+v"(kf[3]), "+v"(kf[4]), "+v"(kf[5]), "+v"(kf[6]), "+v"(kf[7]) :: "memory");
      STAMP(3);
      const v4u qf = *(const LAS v4u*)(L + L_QS + r16 * 64 + gq * 16);
      float sc[8];
#pragma unroll
      for (int t = 0; t < 8; ++t) {
        const f32x4 d = mfma(kf[t], qf, Z4);
        sc[t] = (dpp<0x00>(d[0]) + dpp<0x55>(d[1])) + (dpp<0xAA>(d[2]) + dpp<0xFF>(d[3]));
        if (t >= 4 && t >= nval) sc[t] = -__builtin_inff();
      }
      float m = fmaxf(fmaxf(fmaxf(sc[0], sc[1]), fmaxf(sc[2], sc[3])), fmaxf(fmaxf(sc[4], sc[5]), fmaxf(sc[6], sc[7])));
      m = x32_max(x16_max(m));
      constexpr float C2 = 0.08838834764831845f * 1.4426950408889634f;
      const float ms = m * C2;
      float p[8], l = 0.f;
#pragma unroll
      for (int t = 0; t < 8; ++t) { p[t] = __builtin_amdgcn_exp2f(fmaf(sc[t], C2, -ms)); l += p[t]; }
      l = x32_sum(x16_sum(l));
      if ((lane & 0x33) == 0) *(LAS f32x2*)(L + L_MW + ((lane >> 2) * 12 + w) * 8) = f32x2{ms, l};
      lds_set(FL + F_MW + w, tg);
      v4u pf = {pk(p[0], p[1]), pk(p[2], p[3]), pk(p[4], p[5]), pk(p[6], p[7])};
      unsigned pa = L_PART + w * 2048 + gq * 512 + r16 * 32;
      PINV(pf); PINV(pa);
      STAMP(4);
      v4u vt[8];
#pragma unroll
      for (int mm = 0; mm < 4; ++mm) {
        if (mm == 0) VMWAIT2(6, vf[0], vf[1]);
        if (mm == 1) VMWAIT2(4, vf[2], vf[3]);
        if (mm == 2) { lds_set(FL + F_VL + w, tg); VMWAIT2(2, vf[4], vf[5]); }
        if (mm == 3) { VMWAIT2(0, vf[6], vf[7]); STAMP(5); }
#pragma unroll
        for (int d = 0; d < 4; ++d) { vt[2 * d][mm] = plo(vf[2 * mm][d], vf[2 * mm + 1][d]); vt[2 * d + 1][mm] = phi(vf[2 * mm][d], vf[2 * mm + 1][d]); }
        if (mm < 3) {
#pragma unroll
          for (int e = 0; e < 8; ++e) PINV(vt[e]);
        }
        SCHED();
      }
      float o[8];
#pragma unroll
      for (int e = 0; e < 8; ++e) o[e] = mfma(pf, vt[e], Z4)[0];
      *(LAS f32x4*)(L + pa) = f32x4{o[0], o[1], o[2], o[3]};
      *(LAS f32x4*)(L + pa + 16) = f32x4{o[4], o[5], o[6], o[7]};
      lds_set(FL + F_OPF + w, tg);
      STAMP(6);
      if (w < 4) {
        const int jl = r16 < ntile ? r16 : 0;
        lds_wait(FL + F_MW + jl, tg);
        const f32x2 ml = *(const LAS f32x2*)(L + L_MW + (w * 12 + jl) * 8);
        const float mg = row_max(ml.x);
        const float wt = r16 < ntile ? __builtin_amdgcn_exp2f(ml.x - mg) : 0.f;
        const float lt = row_sum(wt * ml.y);
        float wj[11];
#pragma unroll
        for (int j = 0; j < 11; ++j) { wj[j] = __builtin_bit_cast(float, __builtin_amdgcn_readlane(__builtin_bit_cast(int, wt), j)); PINS(wj[j]); }
        if (lane == 0) ST64((v2u{nosent(__float_as_uint(mg)), nosent(__float_as_uint(lt))}), orec + 1024 + w * 8);
        unsigned ra = L_PART + w * 512 + lane * 8, so = orec + w * 256 + lane * 4;
        PINV(ra); PINV(so);
        lds_wait(FL + F_OPF + jl, tg);
        f32x2 pv[11];
#pragma unroll
        for (int j = 0; j < 11; ++j) pv[j] = *(const LAS f32x2*)(L + ra + j * 2048);
        SCHED();
        f32x2 a = pv[0] * wj[0], a2 = pv[1] * wj[1];
#pragma unroll
        for (int j = 2; j < 10; j += 2) { a += pv[j] * wj[j]; a2 += pv[j + 1] * wj[j + 1]; }
        a += pv[10] * wj[10];
        a += a2;
        const unsigned v = nosent(pk(a.x, a.y));
        const v4u o4 = {dppu<0x00>(v), dppu<0x55>(v), dppu<0xAA>(v), dppu<0xFF>(v)};
        if ((lane & 3) == 0) ST128(o4, so);
        STAMP(7);
      }
    } else if (w == 11) {
      __builtin_amdgcn_s_setprio(3);
      ST128(SV, orec + lane * 16);
      if (lane < 8) ST128(SV, orec + 1024 + lane * 16);
      const u64 tag = load_tag(ws);
      const unsigned taghi = (unsigned)(tag >> 16), taglo = (unsigned)(tag << 16), tg = (unsigned)tag;
      const bool isR = i < 8;
      const int ln = g * 8 + i, rb = ln >> 4, pg = lane >> 3, q4 = lane & 7, col = 32 * (ln & 15) + 4 * q4;
      v2u xv = {0u, 0u};
      if (isR) xv = __builtin_amdgcn_raw_buffer_load_b64(rx, (rb * DM + col) * 2, 0, 0);
      DRAIN();
      lds_set(FL + F_CL, tg);
      STAMP(3);
      lds_wait(FL + F_VL + (lane < ntile ? lane : 0), tg);
      if (lane == 0) ST64(hint(taglo, taghi), WS_HA + (g * 32 + i) * 128);
      STAMPE(4);
      if (isR) {
        const rsrc_t ro = mk(out, NB * DM * 2u);
        unsigned pf = __builtin_amdgcn_raw_buffer_load_b32(ro, 0, 0, AUX_SC1);
        asm volatile("" :: "v"(pf));
        v2u hb;
        GSPIN {
          CBAR();
          hb = LD64(WS_HB + ((lane & 7) * 4 + ((ln >> 2) & 3)) * 128, 0);
          if (__all(hint_ok(hb, taglo, taghi))) break;
          __builtin_amdgcn_s_sleep(2);
        }
        STAMP(5);
        wait_age(hb.x, DLY3 + 20);
        v2u hv;
        GSPIN {
          CBAR();
          hv = LD64(WS_HP + (pg * NB + rb) * 1024 + col * 2, 0);
          if (__all(max(hv.x, hv.y) != SENT)) break;
        }
        STAMP(6);
        float h[4] = {blo(hv.x), bhi(hv.x), blo(hv.y), bhi(hv.y)};
#pragma unroll
        for (int d = 0; d < 4; ++d) { h[d] += dpp<ROR8>(h[d]); h[d] = x32_sum(x16_sum(h[d])); }
        {
          const unsigned a0 = pk(h[0], h[1]), a1 = pk(h[2], h[3]);
          const unsigned h0 = pk(blo(xv.x) + blo(a0), bhi(xv.x) + bhi(a0)), h1 = pk(blo(xv.y) + blo(a1), bhi(xv.y) + bhi(a1));
          h[0] = blo(h0); h[1] = bhi(h0); h[2] = blo(h1); h[3] = bhi(h1);
        }
        unsigned po = WS_OP + pg * 8192 + ln * 128 + q4 * 16, po3 = pg < 4 ? po + 24 * 8192 : po, oo = (rb * DM + col) * 2;
        PINV(po); PINV(po3); PINV(oo); PINV(h[0]); PINV(h[1]); PINV(h[2]); PINV(h[3]);
        v2u hc;
        GSPIN {
          CBAR();
          hc = LD64(WS_HC + (lane < NDCU ? lane : 0) * 128, 0);
          if (__all(hint_ok(hc, taglo, taghi))) break;
        }
        wait_age(hc.x, DLY5);
        STAMP(7);
        v4u pv[4];
        GSPIN {
          CBAR();
#pragma unroll
          for (int t = 0; t < 3; ++t) pv[t] = LD128(po, t * 8 * 8192);
          pv[3] = LD128(po3, 0);
          if (__all(okv(pv[0]) & okv(pv[1]) & okv(pv[2]) & okv(pv[3]))) break;
          RETRY();
        }
        STAMP(8);
        float y[4];
#pragma unroll
        for (int d = 0; d < 4; ++d) {
          float t = (__uint_as_float(pv[0][d]) + __uint_as_float(pv[1][d])) + (__uint_as_float(pv[2][d]) + (pg < 4 ? __uint_as_float(pv[3][d]) : 0.f));
          t += dpp<ROR8>(t);
          y[d] = x32_sum(x16_sum(t));
        }
        const unsigned y0 = pk(y[0], y[1]), y1 = pk(y[2], y[3]);
        if (lane < 8) __builtin_amdgcn_raw_buffer_store_b64(v2u{pk(h[0] + blo(y0), h[1] + bhi(y0)), pk(h[2] + blo(y1), h[3] + bhi(y1))}, ro, oo, 0, AUX_SC1);
        if (ln == 0 && lane == 0) ST64((v2u{(unsigned)tag, (unsigned)(tag >> 32)}), WS_EPOCH);
        STAMPE(9);
      }
    }
    PDUMP();
    return;
  }

  const int jq = i - 24;
  const bool isC = jq < 4;
  const int dI = g + 8 * (jq - 4);
  if (!isC && dI >= NDCU) { PDUMP(); return; }
  const u64 tag = load_tag(ws);
  const unsigned taghi = (unsigned)(tag >> 16), taglo = (unsigned)(tag << 16), tg = (unsigned)tag;
#define GATHER_H() do { \
    unsigned klo = 0x00003F80u, khi = 0x3F800000u, ho = WS_HP + w * 256 + lane * 4, oa = L_OA + w * 256 + lane * 4; \
    unsigned hp[8] = {0u, 0u, 0u, 0u, 0u, 0u, 0u, 0u}; \
    float x0 = blo(xp), x1 = bhi(xp); \
    PINS(klo); PINS(khi); PINV(oa); PINV(x0); PINV(x1); \
    ask8(hp, (unsigned)(size_t)(FL + F_GO + (lane & 7)), tg, ho, mkd(ws, WS_END)); \
    if (!__all(max(max(max(hp[0], hp[1]), max(hp[2], hp[3])), max(max(hp[4], hp[5]), max(hp[6], hp[7]))) != SENT)) { \
      GSPIN { \
        RETRY(); \
        CBAR(); \
        _Pragma("unroll") for (int j = 0; j < 8; ++j) hp[j] = LD32(ho, j * 4096); \
        if (__all(max(max(max(hp[0], hp[1]), max(hp[2], hp[3])), max(max(hp[4], hp[5]), max(hp[6], hp[7]))) != SENT)) break; \
      } \
      DRAIN(); \
    } \
    STAMP(7); \
    float a0 = 0.f, a1 = 0.f, a2 = 0.f, a3 = 0.f; \
    _Pragma("unroll") for (int j = 0; j < 4; ++j) { a0 = dot2(hp[j], klo, a0); a1 = dot2(hp[j], khi, a1); a2 = dot2(hp[4 + j], klo, a2); a3 = dot2(hp[4 + j], khi, a3); } \
    const unsigned t0 = pk(a0 + a2, a1 + a3); \
    *(LAS unsigned*)(L + oa) = pk(x0 + blo(t0), x1 + bhi(t0)); \
  } while (0)
  unsigned xp = 0;
  if (!isC) xp = __builtin_amdgcn_raw_buffer_load_b32(rx, w * 256 + lane * 4, 0, 0);
  if (w < 8) {
    const unsigned stride = isC ? DM * 2 : FF * 2;
    const GAS unsigned char* p = isC ? (const GAS unsigned char*)wo + (size_t)(512 * g) * (DM * 2) + (128 * jq + 8 * r16) * 2
                                     : (const GAS unsigned char*)(r16 < 8 ? wg : wu) + (64 * dI + 8 * (r16 & 7)) * 2;
    p += (size_t)(64 * w + 8 * gq) * stride;
    unsigned aa = L_OA + (r16 >> 2) * 1024 + w * 128 + gq * 16, pa = L_PART + w * 2048 + gq * 512 + r16 * 32;
    PINV(aa); PINV(pa);
    v4u src[16], bt[16];
    until(tstart, isC ? TC0 : TD0);
#pragma unroll
    for (int j = 0; j < WPRE; ++j) src[j] = __builtin_nontemporal_load((const GAS v4u*)(p + ((j >> 3) * 32 + (j & 7)) * stride));
    lds_wait(FL + F_QPUB + (lane & 3), tg);
#pragma unroll
    for (int j = WPRE; j < 16; ++j) src[j] = __builtin_nontemporal_load((const GAS v4u*)(p + ((j >> 3) * 32 + (j & 7)) * stride));
    SCHED();
    STAMP(4);
    tr8(src, bt);
    tr8(src + 8, bt + 8);
    if (!isC) {
      const v4u ones = {ONE2, ONE2, ONE2, ONE2};
      float cs[8];
#pragma unroll
      for (int e = 0; e < 8; ++e) cs[e] = mfma(ones, bt[8 + e], mfma(ones, bt[e], Z4))[0];
      if (lane < 16) {
        *(LAS f32x4*)(L + L_SS + w * 512 + lane * 32) = f32x4{cs[0], cs[1], cs[2], cs[3]};
        *(LAS f32x4*)(L + L_SS + w * 512 + lane * 32 + 16) = f32x4{cs[4], cs[5], cs[6], cs[7]};
      }
      lds_set(FL + F_SR + w, tg);
    }
    DRAIN();
    STAMP(5);
    if (w == 0) {
      if (isC) {
        const int hl = lane < 24 ? lane : 0;
        watch(rws, WS_HA + (g * 32 + hl) * 128, taglo, taghi, (LAS v2u*)(L + L_HINT + hl * 8), tg);
      } else {
        watch8(rws, WS_HB + (lane & 31) * 128, taglo, taghi, DLY3, FL + F_GO, tg, lane);
      }
      STAMP(10);
      DRAIN();
    } else if (w == 1 && isC) {
      lds_wait(FL + F_SR + (lane & 7), tg);
      if (lane == 0) ST64(hint(taglo, taghi), WS_HB + (g * 4 + jq) * 128);
    }
    if (!isC) GATHER_H();
    WGBAR();
    STAMP(8);
    {
      const v4u a0 = *(const LAS v4u*)(L + aa), a1 = *(const LAS v4u*)(L + aa + 64);
#pragma unroll
      for (int h = 0; h < 2; ++h) {
        f32x4 acc[4];
#pragma unroll
        for (int e = 0; e < 4; ++e) acc[e] = mfma(a0, bt[4 * h + e], Z4);
#pragma unroll
        for (int e = 0; e < 4; ++e) acc[e] = mfma(a1, bt[8 + 4 * h + e], acc[e]);
        *(LAS f32x4*)(L + pa + 16 * h) = f32x4{acc[0][0], acc[1][0], acc[2][0], acc[3][0]};
      }
    }
    STAMP(9);
    WGBAR();
    if (!isC) WGBAR();
  } else if (isC) {
    const int v = w & 7, bb = v >> 1, head = 2 * (v & 1) + (lane >> 5), sg = gq & 1;
    unsigned ra = L_PART + (w & 3) * 512 + lane * 8, so = WS_HP + (g * NB + (w & 3)) * 1024 + jq * 256 + lane * 4;
    unsigned rd = WS_OREC + (g * 24 + bb * NSPL + sg) * OREC_SZ + head * 256 + r16 * 16, rs = WS_OREC + (g * 24 + bb * NSPL + sg) * OREC_SZ + 1024 + head * 8;
    unsigned oa = L_OA + bb * 1024 + head * 256 + r16 * 16 + sg * 8;
    PINV(ra); PINV(so); PINV(rd); PINV(rs); PINV(oa);
    if (w == 15) ST128(SV, WS_HP + (g * NB + gq) * 1024 + jq * 256 + r16 * 16);
    DRAIN();
    STAMP(5);
    {
      v2u e;
#pragma nounroll
      for (unsigned sp = 0; sp < SPIN_L; ++sp) { e = *(volatile LAS v2u*)(L + L_HINT + (bb * NSPL + (lane < NSPL ? lane : 0)) * 8); if (__all(e.x == tg)) break; }
      wait_age(e.y, DLY2);
    }
    STAMP(6);
    GSPIN {
      v4u od[3];
      v2u st[3];
      CBAR();
#pragma unroll
      for (int u = 0; u < 3; ++u) st[u] = LD64(rs, 2 * u * OREC_SZ);
      SCHED();
#pragma unroll
      for (int u = 0; u < 3; ++u) od[u] = LD128(rd, 2 * u * OREC_SZ);
      SCHED();
      lds_set(FL + F_SR + v, tg);
      SCHED();
      unsigned bad = max(max(max(st[0].x, st[0].y), max(st[1].x, st[1].y)), max(st[2].x, st[2].y));
      const float m0 = __uint_as_float(st[0].x), m1 = __uint_as_float(st[1].x), m2 = __uint_as_float(st[2].x);
      const float mg = x16_max(fmaxf(fmaxf(m0, m1), m2));
      float wt[3] = {__builtin_amdgcn_exp2f(m0 - mg), __builtin_amdgcn_exp2f(m1 - mg), __builtin_amdgcn_exp2f(m2 - mg)};
      const float inv = __builtin_amdgcn_rcpf(x16_sum(fmaf(wt[0], __uint_as_float(st[0].y), fmaf(wt[1], __uint_as_float(st[1].y), wt[2] * __uint_as_float(st[2].y)))));
#pragma unroll
      for (int u = 0; u < 3; ++u) { wt[u] *= inv; PINV(wt[u]); }
      PINV(bad);
      SCHED();
      f32x2 a[4];
#pragma unroll
      for (int u = 0; u < 3; ++u) {
        f32x2 t[4];
#pragma unroll
        for (int d = 0; d < 4; ++d) { t[d] = f32x2{blo(od[u][d]), bhi(od[u][d])}; PINV(t[d]); }
        bad = max(bad, max(max(od[u].x, od[u].y), max(od[u].z, od[u].w)));
#pragma unroll
        for (int d = 0; d < 4; ++d) a[d] = u == 0 ? t[d] * wt[0] : t[d] * wt[u] + a[d];
      }
      float r[4];
#pragma unroll
      for (int d = 0; d < 4; ++d) {
        const auto sw = __builtin_amdgcn_permlane16_swap(__float_as_uint(a[d >> 1][d & 1]), __float_as_uint(a[2 + (d >> 1)][d & 1]), false, false);
        r[d] = __uint_as_float(sw[0]) + __uint_as_float(sw[1]);
      }
      *(LAS v2u*)(L + oa) = v2u{pk(r[0], r[1]), pk(r[2], r[3])};
      STAMP(7);
      if (__all(bad != SENT)) break;
      RETRY();
    }
    DRAIN();
    STAMP(8);
    WGBAR();
    WGBAR();
    if (w >= 12) {
      f32x2 pv[8];
#pragma unroll
      for (int j = 0; j < 8; ++j) pv[j] = *(const LAS f32x2*)(L + ra + j * 2048);
      SCHED();
      const f32x2 t = ((pv[0] + pv[1]) + (pv[2] + pv[3])) + ((pv[4] + pv[5]) + (pv[6] + pv[7]));
      const unsigned vv = nosent(pk(t.x, t.y));
      const v4u o4 = {dppu<0x00>(vv), dppu<0x55>(vv), dppu<0xAA>(vv), dppu<0xFF>(vv)};
      if ((lane & 3) == 0) ST128(o4, so);
      STAMPE(9);
    }
  } else {
    unsigned ra = L_PART + (w & 3) * 512 + lane * 4, hw = L_HH + (w & 3) * 128 + lane * 2, ha = L_HH + (r16 >> 2) * 128 + gq * 16;
    unsigned so = WS_OP + dI * 8192 + gq * 2048 + (w & 7) * 256 + r16 * 16, sa = L_OA + (w & 3) * 1024 + lane * 16;
    PINV(ra); PINV(hw); PINV(ha); PINV(so); PINV(sa);
    ST128(SV, WS_OP + dI * 8192 + (w - 8) * 1024 + lane * 16);
    until(tstart, TWD);
    const rsrc_t rw = mk(wd, FF * DM * 2u);
    v2u src[16];
    v4u bt[8];
#pragma unroll
    for (int j = 0; j < 16; ++j) src[j] = __builtin_amdgcn_raw_buffer_load_b64(rw, (unsigned)(8 * gq + (j & 3)) * 1024 + (w - 8) * 128 + r16 * 8, (64 * dI + (j >> 3) * 32 + (j & 4)) * 1024, AUX_NT);
    SCHED();
    tr4(src, bt);
    tr4(src + 8, bt + 4);
    float sgs = 0.f, sus = 0.f;
    if (w >= 12) {
      lds_wait(FL + F_SR + (lane & 7), tg);
#pragma unroll
      for (int j = 0; j < 8; ++j) { sgs += *(const LAS float*)(L + L_SS + j * 512 + lane * 4); sus += *(const LAS float*)(L + L_SS + j * 512 + 256 + lane * 4); }
    }
    PINV(sgs); PINV(sus);
    DRAIN();
    STAMP(5);
    GATHER_H();
    WGBAR();
    if (w >= 12) {
      const f32x2 st = ln_stats(*(const LAS v4u*)(L + sa));
      float kg = st.y * -1.4426950408889634f, cg = st.x * sgs, cu = st.x * sus, ku = st.y;
      PINV(kg); PINV(cg); PINV(cu); PINV(ku);
      WGBAR();
      float gp[8], up[8];
#pragma unroll
      for (int j = 0; j < 8; ++j) { gp[j] = *(const LAS float*)(L + ra + j * 2048); up[j] = *(const LAS float*)(L + ra + j * 2048 + 256); }
      SCHED();
      const float gg = ((gp[0] + gp[1]) + (gp[2] + gp[3])) + ((gp[4] + gp[5]) + (gp[6] + gp[7]));
      const float uu = ((up[0] + up[1]) + (up[2] + up[3])) + ((up[4] + up[5]) + (up[6] + up[7]));
      const float hh = ku * (uu - cu) * __builtin_amdgcn_rcpf(1.f + __builtin_amdgcn_exp2f(kg * (gg - cg)));
      *(LAS u16*)(L + hw) = (u16)pk(hh, hh);
    } else {
      if (w == 8 && lane == 0) ST64(hint(taglo, taghi), WS_HC + dI * 128);
      WGBAR();
    }
    WGBAR();
    STAMP(8);
    const v4u a0 = *(const LAS v4u*)(L + ha), a1 = *(const LAS v4u*)(L + ha + 64);
    f32x4 acc[4];
#pragma unroll
    for (int e = 0; e < 4; ++e) acc[e] = mfma(a0, bt[e], Z4);
#pragma unroll
    for (int e = 0; e < 4; ++e) acc[e] = mfma(a1, bt[4 + e], acc[e]);
    ST128((v4u{nosent(__float_as_uint(acc[0][0])), nosent(__float_as_uint(acc[1][0])), nosent(__float_as_uint(acc[2][0])), nosent(__float_as_uint(acc[3][0]))}), so);
    STAMPE(9);
  }
  PDUMP();
}
}

extern "C" void kernel_launch(void* const* d_in, const int* in_sizes, int n_in, void* d_out, int out_size, void* d_ws, size_t ws_size, hipStream_t stream) {
  (void)in_sizes; (void)n_in; (void)out_size; (void)ws_size;
  fused_decode<<<dim3(256), dim3(1024), 0, stream>>>((const u16*)d_in[0], (const u16*)d_in[1], (const u16*)d_in[2], (const u16*)d_in[3], (const u16*)d_in[4],
                                                     (const u16*)d_in[6], (const u16*)d_in[7], (const u16*)d_in[8], (u16*)d_out, (unsigned char*)d_ws);
}
```

```cpp
#include <hip/hip_runtime.h>
#include <stdint.h>

#define PROF 0
#define TK0 35
#define KPRE 4
#define QDELTA 70
#define TC0 245
#define TD0 100
#define KMID 0
#define KLEFT 2
#define VPRE 0
#define WPRE 3
#define TWD 450
#define DLY2 60
#define DLY3 85
#define DLY5 76

namespace {
typedef unsigned short u16;
typedef unsigned long long u64;
typedef unsigned v4u __attribute__((ext_vector_type(4)));
typedef unsigned v2u __attribute__((ext_vector_type(2)));
typedef int v4i __attribute__((ext_vector_type(4)));
typedef float f32x4 __attribute__((ext_vector_type(4)));
typedef float f32x2 __attribute__((ext_vector_type(2)));
typedef short bf16x8 __attribute__((ext_vector_type(8)));
typedef __bf16 bf16x2 __attribute__((ext_vector_type(2)));
#define GAS __attribute__((address_space(1)))
#define LAS __attribute__((address_space(3)))
typedef __amdgpu_buffer_rsrc_t rsrc_t;

constexpr int NB = 4, DM = 512, HKV = 8, SK = 8192, WIN = 2048, FF = 1792, NQ = 4096;
constexpr int NSPL = 6, NDCU = 28;
constexpr int AUX_NT = 2, AUX_SC1 = 16;
constexpr unsigned SENT = 0xFFFFFFFFu, ONE2 = 0x3F803F80u;
constexpr int WS_EPOCH = 0, WS_HB = 4096, WS_HC = WS_HB + 32 * 128, WS_HA = WS_HC + 32 * 128, WS_QG = WS_HA + 8 * 32 * 128;
constexpr int WS_OREC = WS_QG + 8 * 4 * 4 * 256 * 8, OREC_SZ = 1152;
constexpr int WS_HP = WS_OREC + 8 * 24 * OREC_SZ, WS_OP = WS_HP + 8 * 4 * 512 * 2, WS_END = WS_OP + NDCU * 8192;
constexpr int WS_PROF = 2 << 20;
constexpr int L_FLAG = 0, L_MW = 512, L_HINT = 512, L_QS = 1024, L_OA = L_QS + 1024, L_HH = L_OA + 4096, L_SS = L_HH + 512;
constexpr int L_QRED = L_SS + 4096, L_PART = L_QRED + 5120, L_USED = L_PART + 11 * 2048;
constexpr int L_TOTAL = 84 * 1024;
static_assert(L_USED <= L_TOTAL, "lds");
enum { F_QIS = 0, F_QS = 4, F_QPUB = 12, F_QR = 16, F_CL = 24, F_GO = 32, F_SR = 72, F_MW = 80, F_VL = 96, F_OPF = 112 };
constexpr int CODE_BYTES = 15360;
constexpr unsigned SPIN_G = 1u << 17, SPIN_L = 1u << 21;

__device__ __forceinline__ u64 rt() { return __builtin_amdgcn_s_memrealtime(); }
__device__ __forceinline__ rsrc_t mk(const void* p, unsigned bytes) { return __builtin_amdgcn_make_buffer_rsrc((void*)p, 0, bytes, 0x00020000); }
__device__ __forceinline__ f32x4 mfma(v4u a, v4u b, f32x4 c) { return __builtin_amdgcn_mfma_f32_16x16x32_bf16(__builtin_bit_cast(bf16x8, a), __builtin_bit_cast(bf16x8, b), c, 0, 0, 0); }
__device__ __forceinline__ unsigned pk(float lo, float hi) { return __builtin_bit_cast(unsigned, __builtin_convertvector(f32x2{lo, hi}, bf16x2)); }
__device__ __forceinline__ float blo(unsigned v) { return __builtin_bit_cast(float, v << 16); }
__device__ __forceinline__ float bhi(unsigned v) { return __builtin_bit_cast(float, v & 0xffff0000u); }
__device__ __forceinline__ unsigned plo(unsigned a, unsigned b) { return __builtin_amdgcn_perm(b, a, 0x05040100u); }
__device__ __forceinline__ unsigned phi(unsigned a, unsigned b) { return __builtin_amdgcn_perm(b, a, 0x07060302u); }
__device__ __forceinline__ unsigned nosent(unsigned v) { return v < 0xFFFFFFFEu ? v : 0xFFFFFFFEu; }
template <int C> __device__ __forceinline__ float dpp(float x) { return __builtin_bit_cast(float, __builtin_amdgcn_mov_dpp(__builtin_bit_cast(int, x), C, 0xf, 0xf, true)); }
template <int C> __device__ __forceinline__ unsigned dppu(unsigned x) { return (unsigned)__builtin_amdgcn_mov_dpp((int)x, C, 0xf, 0xf, true); }
constexpr int XOR1 = 0xB1, XOR2 = 0x4E, ROR4 = 0x124, ROR8 = 0x128;
__device__ __forceinline__ float x16_sum(float x) { auto s = __builtin_amdgcn_permlane16_swap(__float_as_uint(x), __float_as_uint(x), false, false); return __uint_as_float(s[0]) + __uint_as_float(s[1]); }
__device__ __forceinline__ float x32_sum(float x) { auto s = __builtin_amdgcn_permlane32_swap(__float_as_uint(x), __float_as_uint(x), false, false); return __uint_as_float(s[0]) + __uint_as_float(s[1]); }
__device__ __forceinline__ float x16_max(float x) { auto s = __builtin_amdgcn_permlane16_swap(__float_as_uint(x), __float_as_uint(x), false, false); return fmaxf(__uint_as_float(s[0]), __uint_as_float(s[1])); }
__device__ __forceinline__ float x32_max(float x) { auto s = __builtin_amdgcn_permlane32_swap(__float_as_uint(x), __float_as_uint(x), false, false); return fmaxf(__uint_as_float(s[0]), __uint_as_float(s[1])); }
__device__ __forceinline__ float row_sum(float x) { x += dpp<XOR1>(x); x += dpp<XOR2>(x); x += dpp<ROR4>(x); x += dpp<ROR8>(x); return x; }
__device__ __forceinline__ float row_max(float x) { x = fmaxf(x, dpp<XOR1>(x)); x = fmaxf(x, dpp<XOR2>(x)); x = fmaxf(x, dpp<ROR4>(x)); x = fmaxf(x, dpp<ROR8>(x)); return x; }
__device__ __forceinline__ float dot2(unsigned a, unsigned b, float c) { return __builtin_amdgcn_fdot2_f32_bf16(__builtin_bit_cast(bf16x2, a), __builtin_bit_cast(bf16x2, b), c, false); }
#define CBAR() asm volatile("" ::: "memory")
#define SCHED() __builtin_amdgcn_sched_barrier(0)
#define DRAIN() do { asm volatile("s_waitcnt vmcnt(0)" ::: "memory"); __builtin_amdgcn_s_waitcnt(0x0F70); } while (0)
#define PINV(x) asm volatile("" : "+v"(x))
#define PINS(x) asm volatile("" : "+s"(x))
#define WGBAR() do { SCHED(); asm volatile("s_waitcnt lgkmcnt(0)" ::: "memory"); __builtin_amdgcn_s_barrier(); CBAR(); SCHED(); } while (0)
#define GSPIN _Pragma("nounroll") for (unsigned sp = 0; sp < SPIN_G; ++sp)
#define LD32(o, so) __builtin_amdgcn_raw_buffer_load_b32(rws, (o), (so), AUX_SC1)
#define LD64(o, so) __builtin_amdgcn_raw_buffer_load_b64(rws, (o), (so), AUX_SC1)
#define LD128(o, so) __builtin_amdgcn_raw_buffer_load_b128(rws, (o), (so), AUX_SC1)
#define ST64(v, o) __builtin_amdgcn_raw_buffer_store_b64((v), rws, (o), 0, AUX_SC1)
#define ST128(v, o) __builtin_amdgcn_raw_buffer_store_b128((v), rws, (o), 0, AUX_SC1)
__device__ __forceinline__ bool okv(v4u r) { return max(max(r.x, r.y), max(r.z, r.w)) != SENT; }
__device__ __forceinline__ void lds_wait(volatile LAS unsigned* f, unsigned tg) {
  SCHED();
#pragma nounroll
  for (unsigned s = 0; s < SPIN_L; ++s) { if (__all(*f == tg)) break; }
  CBAR();
  SCHED();
}
__device__ __forceinline__ void lds_set(volatile LAS unsigned* f, unsigned tg) { CBAR(); *f = tg; CBAR(); }
__device__ __forceinline__ void until(u64 t0, unsigned ticks) { while (rt() - t0 < ticks) __builtin_amdgcn_s_sleep(1); }
__device__ __forceinline__ v2u hint(unsigned taglo, unsigned taghi) { return v2u{taglo | ((unsigned)rt() & 0xFFFFu), taghi}; }
__device__ __forceinline__ bool hint_ok(v2u h, unsigned taglo, unsigned taghi) { return (h.y == taghi) & ((h.x ^ taglo) < 65536u); }
__device__ __forceinline__ void wait_age(unsigned t16, unsigned dly) {
#pragma nounroll
  for (unsigned s = 0; s < 4096u; ++s) { if (__all((((unsigned)rt() - t16) & 0xFFFFu) >= dly)) break; }
}
__device__ __forceinline__ void watch(rsrc_t rws, unsigned ho, unsigned taglo, unsigned taghi, LAS v2u* slot, unsigned tg) {
  GSPIN {
    CBAR();
    const v2u h = LD64(ho, 0);
    const bool ok = hint_ok(h, taglo, taghi);
    if (ok) *slot = v2u{tg, h.x};
    CBAR();
    if (__all(ok)) break;
  }
}
__device__ __forceinline__ void watch8(rsrc_t rws, unsigned ho, unsigned taglo, unsigned taghi, unsigned dly, volatile LAS unsigned* go, unsigned tg, int lane) {
  v2u h;
  unsigned seen = 0, done = 0;
#pragma nounroll
  for (unsigned sp = 0; sp < SPIN_G && done != 0x11111111u; ++sp) {
    if (seen != ~0u) {
      CBAR();
      h = LD64(ho, 0);
      seen = (unsigned)__builtin_amdgcn_ballot_w64(hint_ok(h, taglo, taghi));
    }
    unsigned m = (unsigned)__builtin_amdgcn_ballot_w64((((unsigned)rt() - h.x) & 0xFFFFu) >= dly) & seen;
    m &= m >> 1; m &= m >> 2; m &= 0x11111111u;
    if (m != done) { if ((m >> (4 * (lane & 7))) & 1u) go[lane & 7] = tg; done = m; }
  }
  CBAR();
}
__device__ __forceinline__ u64 load_tag(const unsigned char* ws) {
  u64 e;
  asm volatile("s_load_dwordx2 %0, %1, 0x0\n\ts_waitcnt lgkmcnt(0)" : "=s"(e) : "s"(ws) : "memory");
  return (e + 1ull) & 0xFFFFFFFFFFFFull;
}
__device__ __forceinline__ void code_fetch(int part) {
  u64 pc0;
  asm volatile("s_getpc_b64 %0" : "=s"(pc0));
  const u64 a0 = (pc0 & ~127ull) + (u64)(part * 512 < CODE_BYTES - 512 ? part * 512 : CODE_BYTES - 512);
  unsigned t0, t1, t2, t3, t4, t5, t6, t7;
  asm volatile("s_load_dword %0, %8, 0x0\n\ts_load_dword %1, %8, 0x40\n\ts_load_dword %2, %8, 0x80\n\ts_load_dword %3, %8, 0xc0\n\t"
               "s_load_dword %4, %8, 0x100\n\ts_load_dword %5, %8, 0x140\n\ts_load_dword %6, %8, 0x180\n\ts_load_dword %7, %8, 0x1c0\n\ts_waitcnt lgkmcnt(0)"
               : "=&s"(t0), "=&s"(t1), "=&s"(t2), "=&s"(t3), "=&s"(t4), "=&s"(t5), "=&s"(t6), "=&s"(t7) : "s"(a0) : "memory");
}
__device__ __forceinline__ v4i mkd(const void* p, unsigned bytes) { return v4i{(int)(u64)p, (int)((u64)p >> 32) & 0xFFFF, (int)bytes, 0x00020000}; }
template <int OFF> __device__ __forceinline__ void ldnt(v4u& d, unsigned vo, v4i rs, unsigned so) {
  asm volatile("buffer_load_dwordx4 %0, %1, %2, %3 offen offset:%4 nt" : "=v"(d) : "v"(vo), "s"(rs), "s"(so), "i"(OFF) : "memory");
}
template <int LO, int HI> __device__ __forceinline__ void ldtile(v4u* d, unsigned vo, v4i rs, unsigned so, unsigned so2) {
  if constexpr (LO < HI) { ldnt<(LO & 3) * 1024>(d[LO], vo, rs, LO < 4 ? so : so2); ldtile<LO + 1, HI>(d, vo, rs, so, so2); }
}
__device__ __forceinline__ void ldnt2(v2u& d, unsigned vo, v4i rs, unsigned so) {
  asm volatile("buffer_load_dwordx2 %0, %1, %2, %3 offen nt" : "=v"(d) : "v"(vo), "s"(rs), "s"(so) : "memory");
}
__device__ __forceinline__ void ldp4(v4u& d, unsigned vo, v4i rs) {
  asm volatile("buffer_load_dwordx4 %0, %1, %2, 0 offen" : "=v"(d) : "v"(vo), "s"(rs) : "memory");
}
#define VMWAIT2(n, a, b) asm volatile("s_waitcnt vmcnt(%2)" : "+v"(a), "+v"(b) : "i"(n) : "memory")
__device__ __forceinline__ void ask8(unsigned (&hp)[8], unsigned go, unsigned tg, unsigned ho, v4i rs) {
  unsigned t, done, n, cnt;
#define ASK1(j, off) "s_bitcmp1_b32 %10, " #j "\n\ts_cbranch_scc0 2" #j "f\n\ts_movk_i32 %11, " #off "\n\tbuffer_load_dword %" #j ", %14, %15, %11 offen sc1\n2" #j ":\n\t"
  asm volatile("s_mov_b32 %9, 0\n\ts_mov_b32 %11, 0x200000\n"
               "1:\n\tds_read_b32 %8, %12\n\ts_sub_u32 %11, %11, 1\n\ts_cbranch_scc1 3f\n\ts_waitcnt lgkmcnt(0)\n\t"
               "v_cmp_eq_u32 vcc, %13, %8\n\ts_andn2_b32 %10, vcc_lo, %9\n\ts_and_b32 %10, %10, 0xff\n\ts_cbranch_scc0 1b\n\t"
               ASK1(0, 0) ASK1(1, 0x1000) ASK1(2, 0x2000) ASK1(3, 0x3000) ASK1(4, 0x4000) ASK1(5, 0x5000) ASK1(6, 0x6000) ASK1(7, 0x7000)
               "s_mov_b32 %11, 0x200000\n\ts_or_b32 %9, %9, %10\n\ts_cmp_lg_u32 %9, 0xff\n\ts_cbranch_scc1 1b\n"
               "3:\n\ts_waitcnt vmcnt(0) lgkmcnt(0)"
               : "+v"(hp[0]), "+v"(hp[1]), "+v"(hp[2]), "+v"(hp[3]), "+v"(hp[4]), "+v"(hp[5]), "+v"(hp[6]), "+v"(hp[7]), "=&v"(t), "=&s"(done), "=&s"(n), "=&s"(cnt)
               : "v"(go), "s"(tg), "v"(ho), "s"(rs) : "vcc", "scc", "memory");
#undef ASK1
}
__device__ __forceinline__ f32x2 ln_stats(v4u hv) {
  const float c0 = blo((unsigned)__builtin_amdgcn_readfirstlane(hv[0]));
  float t[8];
#pragma unroll
  for (int d = 0; d < 4; ++d) { t[2 * d] = blo(hv[d]) - c0; t[2 * d + 1] = bhi(hv[d]) - c0; }
  float s1 = ((t[0] + t[1]) + (t[2] + t[3])) + ((t[4] + t[5]) + (t[6] + t[7]));
  float s2 = (fmaf(t[1], t[1], t[0] * t[0]) + fmaf(t[3], t[3], t[2] * t[2])) + (fmaf(t[5], t[5], t[4] * t[4]) + fmaf(t[7], t[7], t[6] * t[6]));
  s1 += dpp<XOR1>(s1); s2 += dpp<XOR1>(s2);
  s1 += dpp<XOR2>(s1); s2 += dpp<XOR2>(s2);
  s1 += dpp<ROR4>(s1); s2 += dpp<ROR4>(s2);
  s1 += dpp<ROR8>(s1); s2 += dpp<ROR8>(s2);
  s1 = x16_sum(s1); s2 = x16_sum(s2);
  s1 = x32_sum(s1); s2 = x32_sum(s2);
  const float d = s1 * (1.f / DM), var = fmaxf(fmaf(-d, d, s2 * (1.f / DM)), 0.f);
  return f32x2{c0 + d, __builtin_amdgcn_rsqf(var + 1e-5f)};
}
#define STAMP(k) do {} while (0)
#define STAMPE(k) do {} while (0)
#define PDUMP() do {} while (0)
#define RETRY() do {} while (0)

__device__ __forceinline__ void tr4(const v2u* s, v4u* o) {
#pragma unroll
  for (int m = 0; m < 4; ++m) {
    o[0][m] = plo(s[2 * m].x, s[2 * m + 1].x); o[1][m] = phi(s[2 * m].x, s[2 * m + 1].x);
    o[2][m] = plo(s[2 * m].y, s[2 * m + 1].y); o[3][m] = phi(s[2 * m].y, s[2 * m + 1].y);
  }
#pragma unroll
  for (int e = 0; e < 4; ++e) PINV(o[e]);
}
__device__ __forceinline__ void tr8(const v4u* s, v4u* o) {
#pragma unroll
  for (int m = 0; m < 4; ++m)
#pragma unroll
    for (int d = 0; d < 4; ++d) { o[2 * d][m] = plo(s[2 * m][d], s[2 * m + 1][d]); o[2 * d + 1][m] = phi(s[2 * m][d], s[2 * m + 1][d]); }
#pragma unroll
  for (int e = 0; e < 8; ++e) PINV(o[e]);
}

__global__ void __launch_bounds__(1024) fused_decode(const u16* __restrict__ x, const u16* __restrict__ kc, const u16* __restrict__ vc,
                                                     const u16* __restrict__ wq, const u16* __restrict__ wo, const u16* __restrict__ wg,
                                                     const u16* __restrict__ wu, const u16* __restrict__ wd, u16* __restrict__ out, unsigned char* ws) {
  const u64 tstart = rt();
  __shared__ __attribute__((aligned(16))) unsigned char smem[L_TOTAL];
  LAS unsigned char* L = (LAS unsigned char*)smem;
  volatile LAS unsigned* FL = (volatile LAS unsigned*)(L + L_FLAG);
  const int lane = threadIdx.x & 63;
  int w = (__builtin_amdgcn_readfirstlane(threadIdx.x >> 6) + 11) & 15;
  const int c = blockIdx.x, g = c & 7, i = c >> 3;
  if (w == 11) code_fetch(i);
  const int gq = lane >> 4, r16 = lane & 15;
  const int b = (i * 43) >> 8, s = i - b * NSPL;
  constexpr int ntile = 11;
  const int row0 = 344 * s - (s > 4 ? 8 : 0);
  const int cw = w - 12, rq = i & 3, cq = i >> 2, R0 = 128 * rq + 32 * cw;
  asm volatile("" :: "s"(x), "s"(kc), "s"(vc), "s"(wq), "s"(wo), "s"(wg), "s"(wu), "s"(wd), "s"(out), "s"(ws) : "memory");
  v4u kf[8], vf[8], xa, xr;
  v2u wsrc[8];
  const unsigned kvbase = ((unsigned)(b * HKV + g) * SK + (SK - WIN) + row0 + 16u * w) * 256u, kvbase2 = kvbase + ntile * 4096u;
  const v4i rk = mkd(kc, NB * HKV * SK * 256u), rv = mkd(vc, NB * HKV * SK * 256u);
  if (w >= 12) {
    __builtin_amdgcn_s_setprio(3);
    const v4i rw = mkd(wq, DM * NQ * 2u), rxd = mkd(x, NB * DM * 2u);
    const unsigned o = (unsigned)(8 * gq) * (NQ * 2) + r16 * 8, so = R0 * (NQ * 2) + (512 * g + 64 * cq) * 2;
#pragma unroll
    for (int j = 0; j < 8; ++j) ldnt2(wsrc[j], o, rw, so + j * (NQ * 2));
    ldp4(xa, (r16 >> 2) * 1024 + (R0 + 8 * gq) * 2, rxd);
    ldp4(xr, cw * 1024 + lane * 16, rxd);
  } else if (i < 24 && w < ntile) {
    __builtin_amdgcn_s_setprio(2);
    until(rt(), TK0);
    ldtile<0, KPRE>(kf, r16 * 64 + gq * 16, rk, kvbase, kvbase2);
  }
  STAMP(1);
  PINS(w);
  const rsrc_t rws = mk(ws, WS_END), rx = mk(x, NB * DM * 2u);
  const f32x4 Z4 = {0.f, 0.f, 0.f, 0.f};
  const v4u SV = {SENT, SENT, SENT, SENT};
  const unsigned orec = WS_OREC + (g * 24 + i) * OREC_SZ;

  if (__builtin_expect(w >= 12, 1)) {
    const unsigned tg = (unsigned)load_tag(ws);
    asm volatile("s_waitcnt vmcnt(0)" : "+v"(wsrc[0]), "+v"(wsrc[1]), "+v"(wsrc[2]), "+v"(wsrc[3]), "+v"(wsrc[4]), "+v"(wsrc[5]), "+v"(wsrc[6]), "+v"(wsrc[7]), "+v"(xa), "+v"(xr) :: "memory");
    {
      v4u bt[4];
      tr4(wsrc, bt);
      if ((lane & 3) == 1) xa = v4u{ONE2, ONE2, ONE2, ONE2};
      f32x4 qp, cs;
#pragma unroll
      for (int e = 0; e < 4; ++e) { const f32x4 d = mfma(xa, bt[e], Z4); qp[e] = d[0]; cs[e] = d[1]; }
      *(LAS f32x4*)(L + L_QRED + cw * 1024 + lane * 16) = qp;
      if (lane < 16) *(LAS f32x4*)(L + L_QRED + 4096 + cw * 256 + lane * 16) = cs;
    }
    lds_set(FL + F_QR + cw, tg);
    f32x2 xst = ln_stats(xr);
    PINV(xst);
    STAMP(2);
    lds_wait(FL + F_QR + (lane & 3), tg);
    {
      float a[4], cs[4];
#pragma unroll
      for (int j = 0; j < 4; ++j) { a[j] = *(const LAS float*)(L + L_QRED + j * 1024 + cw * 256 + lane * 4); cs[j] = *(const LAS float*)(L + L_QRED + 4096 + j * 256 + lane * 4); }
      const float q = xst.y * (((a[0] + a[1]) + (a[2] + a[3])) - xst.x * ((cs[0] + cs[1]) + (cs[2] + cs[3])));
      const unsigned q2 = pk(q, dpp<XOR1>(q));
      if ((lane & 1) == 0) ST64((v2u{q2, tg}), WS_QG + (((g * NB + cw) * 4 + rq) * 256 + 32 * cq) * 8 + lane * 4);
    }
    lds_set(FL + F_QPUB + cw, tg);
    STAMP(3);
    if (i < 24) {
      until(rt(), QDELTA);
      const unsigned qo = WS_QG + ((g * NB + b) * 4 * 256 + cw * 64 + lane) * 8;
      v2u qg[4];
      GSPIN {
        CBAR();
#pragma unroll
        for (int j = 0; j < 4; ++j) qg[j] = LD64(qo, j * 2048);
        SCHED();
        lds_set(FL + F_QIS + cw, tg);
        SCHED();
        if (__all((qg[0].y == tg) & (qg[1].y == tg) & (qg[2].y == tg) & (qg[3].y == tg))) break;
        RETRY();
      }
      const float q0 = (blo(qg[0].x) + blo(qg[1].x)) + (blo(qg[2].x) + blo(qg[3].x)), q1 = (bhi(qg[0].x) + bhi(qg[1].x)) + (bhi(qg[2].x) + bhi(qg[3].x));
      *(LAS unsigned*)(L + L_QS + (cw * 64 + lane) * 4) = pk(q0, q1);
      lds_set(FL + F_QS + cw, tg);
      STAMP(4);
      PDUMP();
      return;
    }
    __builtin_amdgcn_s_setprio(0);
  } else if (i < 24) {
    if (w < ntile) {
      const unsigned tg = (unsigned)load_tag(ws);
      const int nval = w < 10 ? 8 : s < 4 ? 6 : 4;
      const unsigned oob6 = nval < 6 ? 0x80000000u : 0u, oob8 = nval < 8 ? 0x80000000u : 0u;
      static_assert(KPRE == 4, "");
      if (KMID >= 1) { asm volatile("s_waitcnt vmcnt(%0)" :: "i"(KLEFT) : "memory"); ldtile<4, 6>(kf, (r16 * 64 + gq * 16) | oob6, rk, kvbase, kvbase2); }
      if (KMID >= 2) { asm volatile("s_waitcnt vmcnt(%0)" :: "i"(KLEFT) : "memory"); ldtile<6, 8>(kf, (r16 * 64 + gq * 16) | oob8, rk, kvbase, kvbase2); }
      lds_wait(FL + F_QPUB + (lane & 3), tg);
      if (KMID < 1) ldtile<4, 6>(kf, (r16 * 64 + gq * 16) | oob6, rk, kvbase, kvbase2);
      if (KMID < 2) ldtile<6, 8>(kf, (r16 * 64 + gq * 16) | oob8, rk, kvbase, kvbase2);
      if (VPRE) ldtile<0, VPRE>(vf, lane * 16, rv, kvbase, kvbase2);
      __builtin_amdgcn_s_setprio(0);
      lds_wait(FL + F_QIS + (lane & 3), tg);
      ldtile<VPRE, 4>(vf, lane * 16, rv, kvbase, kvbase2);
      ldtile<4, 6>(vf, (lane * 16) | oob6, rv, kvbase, kvbase2);
      ldtile<6, 8>(vf, (lane * 16) | oob8, rv, kvbase, kvbase2);
      STAMP(2);
      lds_wait(FL + (lane < 4 ? F_QS + lane : F_CL), tg);
      asm volatile("s_waitcnt vmcnt(8)" : "+v"(kf[0]), "+v"(kf[1]), "+v"(kf[2]), "+v"(kf[3]), "+v"(kf[4]), "+v"(kf[5]), "+v"(kf[6]), "+v"(kf[7]) :: "memory");
      STAMP(3);
      const v4u qf = *(const LAS v4u*)(L + L_QS + r16 * 64 + gq * 16);
      float sc[8];
#pragma unroll
      for (int t = 0; t < 8; ++t) {
        const f32x4 d = mfma(kf[t], qf, Z4);
        sc[t] = (dpp<0x00>(d[0]) + dpp<0x55>(d[1])) + (dpp<0xAA>(d[2]) + dpp<0xFF>(d[3]));
        if (t >= 4 && t >= nval) sc[t] = -__builtin_inff();
      }
      float m = fmaxf(fmaxf(fmaxf(sc[0], sc[1]), fmaxf(sc[2], sc[3])), fmaxf(fmaxf(sc[4], sc[5]), fmaxf(sc[6], sc[7])));
      m = x32_max(x16_max(m));
      constexpr float C2 = 0.08838834764831845f * 1.4426950408889634f;
      const float ms = m * C2;
      float p[8], l = 0.f;
#pragma unroll
      for (int t = 0; t < 8; ++t) { p[t] = __builtin_amdgcn_exp2f(fmaf(sc[t], C2, -ms)); l += p[t]; }
      l = x32_sum(x16_sum(l));
      if ((lane & 0x33) == 0) *(LAS f32x2*)(L + L_MW + ((lane >> 2) * 12 + w) * 8) = f32x2{ms, l};
      lds_set(FL + F_MW + w, tg);
      v4u pf = {pk(p[0], p[1]), pk(p[2], p[3]), pk(p[4], p[5]), pk(p[6], p[7])};
      unsigned pa = L_PART + w * 2048 + gq * 512 + r16 * 32;
      PINV(pf); PINV(pa);
      STAMP(4);
      v4u vt[8];
#pragma unroll
      for (int mm = 0; mm < 4; ++mm) {
        if (mm == 0) VMWAIT2(6, vf[0], vf[1]);
        if (mm == 1) VMWAIT2(4, vf[2], vf[3]);
        if (mm == 2) { lds_set(FL + F_VL + w, tg); VMWAIT2(2, vf[4], vf[5]); }
        if (mm == 3) { VMWAIT2(0, vf[6], vf[7]); STAMP(5); }
#pragma unroll
        for (int d = 0; d < 4; ++d) { vt[2 * d][mm] = plo(vf[2 * mm][d], vf[2 * mm + 1][d]); vt[2 * d + 1][mm] = phi(vf[2 * mm][d], vf[2 * mm + 1][d]); }
        if (mm < 3) {
#pragma unroll
          for (int e = 0; e < 8; ++e) PINV(vt[e]);
        }
        SCHED();
      }
      float o[8];
#pragma unroll
      for (int e = 0; e < 8; ++e) o[e] = mfma(pf, vt[e], Z4)[0];
      *(LAS f32x4*)(L + pa) = f32x4{o[0], o[1], o[2], o[3]};
      *(LAS f32x4*)(L + pa + 16) = f32x4{o[4], o[5], o[6], o[7]};
      lds_set(FL + F_OPF + w, tg);
      STAMP(6);
      if (w < 4) {
        const int jl = r16 < ntile ? r16 : 0;
        lds_wait(FL + F_MW + jl, tg);
        const f32x2 ml = *(const LAS f32x2*)(L + L_MW + (w * 12 + jl) * 8);
        const float mg = row_max(ml.x);
        const float wt = r16 < ntile ? __builtin_amdgcn_exp2f(ml.x - mg) : 0.f;
        const float lt = row_sum(wt * ml.y);
        float wj[11];
#pragma unroll
        for (int j = 0; j < 11; ++j) { wj[j] = __builtin_bit_cast(float, __builtin_amdgcn_readlane(__builtin_bit_cast(int, wt), j)); PINS(wj[j]); }
        if (lane == 0) ST64((v2u{nosent(__float_as_uint(mg)), nosent(__float_as_uint(lt))}), orec + 1024 + w * 8);
        unsigned ra = L_PART + w * 512 + lane * 8, so = orec + w * 256 + lane * 4;
        PINV(ra); PINV(so);
        lds_wait(FL + F_OPF + jl, tg);
        f32x2 pv[11];
#pragma unroll
        for (int j = 0; j < 11; ++j) pv[j] = *(const LAS f32x2*)(L + ra + j * 2048);
        SCHED();
        f32x2 a = pv[0] * wj[0], a2 = pv[1] * wj[1];
#pragma unroll
        for (int j = 2; j < 10; j += 2) { a += pv[j] * wj[j]; a2 += pv[j + 1] * wj[j + 1]; }
        a += pv[10] * wj[10];
        a += a2;
        const unsigned v = nosent(pk(a.x, a.y));
        const v4u o4 = {dppu<0x00>(v), dppu<0x55>(v), dppu<0xAA>(v), dppu<0xFF>(v)};
        if ((lane & 3) == 0) ST128(o4, so);
        STAMP(7);
      }
    } else if (w == 11) {
      __builtin_amdgcn_s_setprio(3);
      ST128(SV, orec + lane * 16);
      if (lane < 8) ST128(SV, orec + 1024 + lane * 16);
      const u64 tag = load_tag(ws);
      const unsigned taghi = (unsigned)(tag >> 16), taglo = (unsigned)(tag << 16), tg = (unsigned)tag;
      const bool isR = i < 8;
      const int ln = g * 8 + i, rb = ln >> 4, pg = lane >> 3, q4 = lane & 7, col = 32 * (ln & 15) + 4 * q4;
      v2u xv = {0u, 0u};
      if (isR) xv = __builtin_amdgcn_raw_buffer_load_b64(rx, (rb * DM + col) * 2, 0, 0);
      DRAIN();
      lds_set(FL + F_CL, tg);
      STAMP(3);
      lds_wait(FL + F_VL + (lane < ntile ? lane : 0), tg);
      if (lane == 0) ST64(hint(taglo, taghi), WS_HA + (g * 32 + i) * 128);
      STAMPE(4);
      if (isR) {
        const rsrc_t ro = mk(out, NB * DM * 2u);
        unsigned pf = __builtin_amdgcn_raw_buffer_load_b32(ro, 0, 0, AUX_SC1);
        asm volatile("" :: "v"(pf));
        v2u hb;
        GSPIN {
          CBAR();
          hb = LD64(WS_HB + ((lane & 7) * 4 + ((ln >> 2) & 3)) * 128, 0);
          if (__all(hint_ok(hb, taglo, taghi))) break;
          __builtin_amdgcn_s_sleep(2);
        }
        STAMP(5);
        wait_age(hb.x, DLY3 + 20);
        v2u hv;
        GSPIN {
          CBAR();
          hv = LD64(WS_HP + (pg * NB + rb) * 1024 + col * 2, 0);
          if (__all(max(hv.x, hv.y) != SENT)) break;
        }
        STAMP(6);
        float h[4] = {blo(hv.x), bhi(hv.x), blo(hv.y), bhi(hv.y)};
#pragma unroll
        for (int d = 0; d < 4; ++d) { h[d] += dpp<ROR8>(h[d]); h[d] = x32_sum(x16_sum(h[d])); }
        {
          const unsigned a0 = pk(h[0], h[1]), a1 = pk(h[2], h[3]);
          const unsigned h0 = pk(blo(xv.x) + blo(a0), bhi(xv.x) + bhi(a0)), h1 = pk(blo(xv.y) + blo(a1), bhi(xv.y) + bhi(a1));
          h[0] = blo(h0); h[1] = bhi(h0); h[2] = blo(h1); h[3] = bhi(h1);
        }
        unsigned po = WS_OP + pg * 8192 + ln * 128 + q4 * 16, po3 = pg < 4 ? po + 24 * 8192 : po, oo = (rb * DM + col) * 2;
        PINV(po); PINV(po3); PINV(oo); PINV(h[0]); PINV(h[1]); PINV(h[2]); PINV(h[3]);
        v2u hc;
        GSPIN {
          CBAR();
          hc = LD64(WS_HC + (lane < NDCU ? lane : 0) * 128, 0);
          if (__all(hint_ok(hc, taglo, taghi))) break;
        }
        wait_age(hc.x, DLY5);
        STAMP(7);
        v4u pv[4];
        GSPIN {
          CBAR();
#pragma unroll
          for (int t = 0; t < 3; ++t) pv[t] = LD128(po, t * 8 * 8192);
          pv[3] = LD128(po3, 0);
          if (__all(okv(pv[0]) & okv(pv[1]) & okv(pv[2]) & okv(pv[3]))) break;
          RETRY();
        }
        STAMP(8);
        float y[4];
#pragma unroll
        for (int d = 0; d < 4; ++d) {
          float t = (__uint_as_float(pv[0][d]) + __uint_as_float(pv[1][d])) + (__uint_as_float(pv[2][d]) + (pg < 4 ? __uint_as_float(pv[3][d]) : 0.f));
          t += dpp<ROR8>(t);
          y[d] = x32_sum(x16_sum(t));
        }
        const unsigned y0 = pk(y[0], y[1]), y1 = pk(y[2], y[3]);
        if (lane < 8) __builtin_amdgcn_raw_buffer_store_b64(v2u{pk(h[0] + blo(y0), h[1] + bhi(y0)), pk(h[2] + blo(y1), h[3] + bhi(y1))}, ro, oo, 0, AUX_SC1);
        if (ln == 0 && lane == 0) ST64((v2u{(unsigned)tag, (unsigned)(tag >> 32)}), WS_EPOCH);
        STAMPE(9);
      }
    }
    PDUMP();
    return;
  }

  const int jq = i - 24;
  const bool isC = jq < 4;
  const int dI = g + 8 * (jq - 4);
  if (!isC && dI >= NDCU) { PDUMP(); return; }
  const u64 tag = load_tag(ws);
  const unsigned taghi = (unsigned)(tag >> 16), taglo = (unsigned)(tag << 16), tg = (unsigned)tag;
#define GATHER_H() do { \
    unsigned klo = 0x00003F80u, khi = 0x3F800000u, ho = WS_HP + w * 256 + lane * 4, oa = L_OA + w * 256 + lane * 4; \
    unsigned hp[8] = {0u, 0u, 0u, 0u, 0u, 0u, 0u, 0u}; \
    float x0 = blo(xp), x1 = bhi(xp); \
    PINS(klo); PINS(khi); PINV(oa); PINV(x0); PINV(x1); \
    ask8(hp, (unsigned)(size_t)(FL + F_GO + (lane & 7)), tg, ho, mkd(ws, WS_END)); \
    if (!__all(max(max(max(hp[0], hp[1]), max(hp[2], hp[3])), max(max(hp[4], hp[5]), max(hp[6], hp[7]))) != SENT)) { \
      GSPIN { \
        RETRY(); \
        CBAR(); \
        _Pragma("unroll") for (int j = 0; j < 8; ++j) hp[j] = LD32(ho, j * 4096); \
        if (__all(max(max(max(hp[0], hp[1]), max(hp[2], hp[3])), max(max(hp[4], hp[5]), max(hp[6], hp[7]))) != SENT)) break; \
      } \
      DRAIN(); \
    } \
    STAMP(7); \
    float a0 = 0.f, a1 = 0.f, a2 = 0.f, a3 = 0.f; \
    _Pragma("unroll") for (int j = 0; j < 4; ++j) { a0 = dot2(hp[j], klo, a0); a1 = dot2(hp[j], khi, a1); a2 = dot2(hp[4 + j], klo, a2); a3 = dot2(hp[4 + j], khi, a3); } \
    const unsigned t0 = pk(a0 + a2, a1 + a3); \
    *(LAS unsigned*)(L + oa) = pk(x0 + blo(t0), x1 + bhi(t0)); \
  } while (0)
  unsigned xp = 0;
  if (!isC) xp = __builtin_amdgcn_raw_buffer_load_b32(rx, w * 256 + lane * 4, 0, 0);
  if (w < 8) {
    const unsigned stride = isC ? DM * 2 : FF * 2;
    const GAS unsigned char* p = isC ? (const GAS unsigned char*)wo + (size_t)(512 * g) * (DM * 2) + (128 * jq + 8 * r16) * 2
                                     : (const GAS unsigned char*)(r16 < 8 ? wg : wu) + (64 * dI + 8 * (r16 & 7)) * 2;
    p += (size_t)(64 * w + 8 * gq) * stride;
    unsigned aa = L_OA + (r16 >> 2) * 1024 + w * 128 + gq * 16, pa = L_PART + w * 2048 + gq * 512 + r16 * 32;
    PINV(aa); PINV(pa);
    v4u src[16], bt[16];
    until(tstart, isC ? TC0 : TD0);
#pragma unroll
    for (int j = 0; j < WPRE; ++j) src[j] = __builtin_nontemporal_load((const GAS v4u*)(p + ((j >> 3) * 32 + (j & 7)) * stride));
    lds_wait(FL + F_QPUB + (lane & 3), tg);
#pragma unroll
    for (int j = WPRE; j < 16; ++j) src[j] = __builtin_nontemporal_load((const GAS v4u*)(p + ((j >> 3) * 32 + (j & 7)) * stride));
    SCHED();
    STAMP(4);
    tr8(src, bt);
    tr8(src + 8, bt + 8);
    if (!isC) {
      const v4u ones = {ONE2, ONE2, ONE2, ONE2};
      float cs[8];
#pragma unroll
      for (int e = 0; e < 8; ++e) cs[e] = mfma(ones, bt[8 + e], mfma(ones, bt[e], Z4))[0];
      if (lane < 16) {
        *(LAS f32x4*)(L + L_SS + w * 512 + lane * 32) = f32x4{cs[0], cs[1], cs[2], cs[3]};
        *(LAS f32x4*)(L + L_SS + w * 512 + lane * 32 + 16) = f32x4{cs[4], cs[5], cs[6], cs[7]};
      }
      lds_set(FL + F_SR + w, tg);
    }
    DRAIN();
    STAMP(5);
    if (w == 0) {
      if (isC) {
        const int hl = lane < 24 ? lane : 0;
        watch(rws, WS_HA + (g * 32 + hl) * 128, taglo, taghi, (LAS v2u*)(L + L_HINT + hl * 8), tg);
      } else {
        watch8(rws, WS_HB + (lane & 31) * 128, taglo, taghi, DLY3, FL + F_GO, tg, lane);
      }
      STAMP(10);
      DRAIN();
    } else if (w == 1 && isC) {
      lds_wait(FL + F_SR + (lane & 7), tg);
      if (lane == 0) ST64(hint(taglo, taghi), WS_HB + (g * 4 + jq) * 128);
    }
    if (!isC) GATHER_H();
    WGBAR();
    STAMP(8);
    {
      const v4u a0 = *(const LAS v4u*)(L + aa), a1 = *(const LAS v4u*)(L + aa + 64);
#pragma unroll
      for (int h = 0; h < 2; ++h) {
        f32x4 acc[4];
#pragma unroll
        for (int e = 0; e < 4; ++e) acc[e] = mfma(a0, bt[4 * h + e], Z4);
#pragma unroll
        for (int e = 0; e < 4; ++e) acc[e] = mfma(a1, bt[8 + 4 * h + e], acc[e]);
        *(LAS f32x4*)(L + pa + 16 * h) = f32x4{acc[0][0], acc[1][0], acc[2][0], acc[3][0]};
      }
    }
    STAMP(9);
    WGBAR();
    if (!isC) WGBAR();
  } else if (isC) {
    const int v = w & 7, bb = v >> 1, head = 2 * (v & 1) + (lane >> 5), sg = gq & 1;
    unsigned ra = L_PART + (w & 3) * 512 + lane * 8, so = WS_HP + (g * NB + (w & 3)) * 1024 + jq * 256 + lane * 4;
    unsigned rd = WS_OREC + (g * 24 + bb * NSPL + sg) * OREC_SZ + head * 256 + r16 * 16, rs = WS_OREC + (g * 24 + bb * NSPL + sg) * OREC_SZ + 1024 + head * 8;
    unsigned oa = L_OA + bb * 1024 + head * 256 + r16 * 16 + sg * 8;
    PINV(ra); PINV(so); PINV(rd); PINV(rs); PINV(oa);
    if (w == 15) ST128(SV, WS_HP + (g * NB + gq) * 1024 + jq * 256 + r16 * 16);
    DRAIN();
    STAMP(5);
    {
      v2u e;
#pragma nounroll
      for (unsigned sp = 0; sp < SPIN_L; ++sp) { e = *(volatile LAS v2u*)(L + L_HINT + (bb * NSPL + (lane < NSPL ? lane : 0)) * 8); if (__all(e.x == tg)) break; }
      wait_age(e.y, DLY2);
    }
    STAMP(6);
    GSPIN {
      v4u od[3];
      v2u st[3];
      CBAR();
#pragma unroll
      for (int u = 0; u < 3; ++u) st[u] = LD64(rs, 2 * u * OREC_SZ);
      SCHED();
#pragma unroll
      for (int u = 0; u < 3; ++u) od[u] = LD128(rd, 2 * u * OREC_SZ);
      SCHED();
      lds_set(FL + F_SR + v, tg);
      SCHED();
      unsigned bad = max(max(max(st[0].x, st[0].y), max(st[1].x, st[1].y)), max(st[2].x, st[2].y));
      const float m0 = __uint_as_float(st[0].x), m1 = __uint_as_float(st[1].x), m2 = __uint_as_float(st[2].x);
      const float mg = x16_max(fmaxf(fmaxf(m0, m1), m2));
      float wt[3] = {__builtin_amdgcn_exp2f(m0 - mg), __builtin_amdgcn_exp2f(m1 - mg), __builtin_amdgcn_exp2f(m2 - mg)};
      const float inv = __builtin_amdgcn_rcpf(x16_sum(fmaf(wt[0], __uint_as_float(st[0].y), fmaf(wt[1], __uint_as_float(st[1].y), wt[2] * __uint_as_float(st[2].y)))));
#pragma unroll
      for (int u = 0; u < 3; ++u) { wt[u] *= inv; PINV(wt[u]); }
      PINV(bad);
      SCHED();
      f32x2 a[4];
#pragma unroll
      for (int u = 0; u < 3; ++u) {
        f32x2 t[4];
#pragma unroll
        for (int d = 0; d < 4; ++d) { t[d] = f32x2{blo(od[u][d]), bhi(od[u][d])}; PINV(t[d]); }
        bad = max(bad, max(max(od[u].x, od[u].y), max(od[u].z, od[u].w)));
#pragma unroll
        for (int d = 0; d < 4; ++d) a[d] = u == 0 ? t[d] * wt[0] : t[d] * wt[u] + a[d];
      }
      float r[4];
#pragma unroll
      for (int d = 0; d < 4; ++d) {
        const auto sw = __builtin_amdgcn_permlane16_swap(__float_as_uint(a[d >> 1][d & 1]), __float_as_uint(a[2 + (d >> 1)][d & 1]), false, false);
        r[d] = __uint_as_float(sw[0]) + __uint_as_float(sw[1]);
      }
      *(LAS v2u*)(L + oa) = v2u{pk(r[0], r[1]), pk(r[2], r[3])};
      STAMP(7);
      if (__all(bad != SENT)) break;
      RETRY();
    }
    DRAIN();
    STAMP(8);
    WGBAR();
    WGBAR();
    if (w >= 12) {
      f32x2 pv[8];
#pragma unroll
      for (int j = 0; j < 8; ++j) pv[j] = *(const LAS f32x2*)(L + ra + j * 2048);
      SCHED();
      const f32x2 t = ((pv[0] + pv[1]) + (pv[2] + pv[3])) + ((pv[4] + pv[5]) + (pv[6] + pv[7]));
      const unsigned vv = nosent(pk(t.x, t.y));
      const v4u o4 = {dppu<0x00>(vv), dppu<0x55>(vv), dppu<0xAA>(vv), dppu<0xFF>(vv)};
      if ((lane & 3) == 0) ST128(o4, so);
      STAMPE(9);
    }
  } else {
    unsigned ra = L_PART + (w & 3) * 512 + lane * 4, hw = L_HH + (w & 3) * 128 + lane * 2, ha = L_HH + (r16 >> 2) * 128 + gq * 16;
    unsigned so = WS_OP + dI * 8192 + gq * 2048 + (w & 7) * 256 + r16 * 16, sa = L_OA + (w & 3) * 1024 + lane * 16;
    PINV(ra); PINV(hw); PINV(ha); PINV(so); PINV(sa);
    ST128(SV, WS_OP + dI * 8192 + (w - 8) * 1024 + lane * 16);
    until(tstart, TWD);
    const rsrc_t rw = mk(wd, FF * DM * 2u);
    v2u src[16];
    v4u bt[8];
#pragma unroll
    for (int j = 0; j < 16; ++j) src[j] = __builtin_amdgcn_raw_buffer_load_b64(rw, (unsigned)(8 * gq + (j & 3)) * 1024 + (w - 8) * 128 + r16 * 8, (64 * dI + (j >> 3) * 32 + (j & 4)) * 1024, AUX_NT);
    SCHED();
    tr4(src, bt);
    tr4(src + 8, bt + 4);
    float sgs = 0.f, sus = 0.f;
    if (w >= 12) {
      lds_wait(FL + F_SR + (lane & 7), tg);
#pragma unroll
      for (int j = 0; j < 8; ++j) { sgs += *(const LAS float*)(L + L_SS + j * 512 + lane * 4); sus += *(const LAS float*)(L + L_SS + j * 512 + 256 + lane * 4); }
    }
    PINV(sgs); PINV(sus);
    DRAIN();
    STAMP(5);
    GATHER_H();
    WGBAR();
    if (w >= 12) {
      const f32x2 st = ln_stats(*(const LAS v4u*)(L + sa));
      float kg = st.y * -1.4426950408889634f, cg = st.x * sgs, cu = st.x * sus, ku = st.y;
      PINV(kg); PINV(cg); PINV(cu); PINV(ku);
      WGBAR();
      float gp[8], up[8];
#pragma unroll
      for (int j = 0; j < 8; ++j) { gp[j] = *(const LAS float*)(L + ra + j * 2048); up[j] = *(const LAS float*)(L + ra + j * 2048 + 256); }
      SCHED();
      const float gg = ((gp[0] + gp[1]) + (gp[2] + gp[3])) + ((gp[4] + gp[5]) + (gp[6] + gp[7]));
      const float uu = ((up[0] + up[1]) + (up[2] + up[3])) + ((up[4] + up[5]) + (up[6] + up[7]));
      const float hh = ku * (uu - cu) * __builtin_amdgcn_rcpf(1.f + __builtin_amdgcn_exp2f(kg * (gg - cg)));
      *(LAS u16*)(L + hw) = (u16)pk(hh, hh);
    } else {
      if (w == 8 && lane == 0) ST64(hint(taglo, taghi), WS_HC + dI * 128);
      WGBAR();
    }
    WGBAR();
    STAMP(8);
    const v4u a0 = *(const LAS v4u*)(L + ha), a1 = *(const LAS v4u*)(L + ha + 64);
    f32x4 acc[4];
#pragma unroll
    for (int e = 0; e < 4; ++e) acc[e] = mfma(a0, bt[e], Z4);
#pragma unroll
    for (int e = 0; e < 4; ++e) acc[e] = mfma(a1, bt[4 + e], acc[e]);
    ST128((v4u{nosent(__float_as_uint(acc[0][0])), nosent(__float_as_uint(acc[1][0])), nosent(__float_as_uint(acc[2][0])), nosent(__float_as_uint(acc[3][0]))}), so);
    STAMPE(9);
  }
  PDUMP();
}
}

extern "C" void kernel_launch(void* const* d_in, const int* in_sizes, int n_in, void* d_out, int out_size, void* d_ws, size_t ws_size, hipStream_t stream) {
  (void)in_sizes; (void)n_in; (void)out_size; (void)ws_size;
  fused_decode<<<dim3(256), dim3(1024), 0, stream>>>((const u16*)d_in[0], (const u16*)d_in[1], (const u16*)d_in[2], (const u16*)d_in[3], (const u16*)d_in[4],
                                                     (const u16*)d_in[6], (const u16*)d_in[7], (const u16*)d_in[8], (u16*)d_out, (unsigned char*)d_ws);
}
```

```cpp
#include <hip/hip_runtime.h>
#include <stdint.h>

#define PROF 1
#define TK0 35
#define KPRE 4
#define QDELTA 70
#define TC0 245
#define TD0 100
#define KMID 0
#define KLEFT 2
#define VPRE 0
#define WPRE 3
#define TWD 450
#define DLY2 60
#define DLY3 85
#define DLY5 76

namespace {
typedef unsigned short u16;
typedef unsigned long long u64;
typedef unsigned v4u __attribute__((ext_vector_type(4)));
typedef unsigned v2u __attribute__((ext_vector_type(2)));
typedef int v4i __attribute__((ext_vector_type(4)));
typedef float f32x4 __attribute__((ext_vector_type(4)));
typedef float f32x2 __attribute__((ext_vector_type(2)));
typedef short bf16x8 __attribute__((ext_vector_type(8)));
typedef __bf16 bf16x2 __attribute__((ext_vector_type(2)));
#define GAS __attribute__((address_space(1)))
#define LAS __attribute__((address_space(3)))
typedef __amdgpu_buffer_rsrc_t rsrc_t;

constexpr int NB = 4, DM = 512, HKV = 8, SK = 8192, WIN = 2048, FF = 1792, NQ = 4096;
constexpr int NSPL = 6, NDCU = 28;
constexpr int AUX_NT = 2, AUX_SC1 = 16;
constexpr unsigned SENT = 0xFFFFFFFFu, ONE2 = 0x3F803F80u;
constexpr int WS_EPOCH = 0, WS_HB = 4096, WS_HC = WS_HB + 32 * 128, WS_HA = WS_HC + 32 * 128, WS_QG = WS_HA + 8 * 32 * 128;
constexpr int WS_OREC = WS_QG + 8 * 4 * 4 * 256 * 8, OREC_SZ = 1152;
constexpr int WS_HP = WS_OREC + 8 * 24 * OREC_SZ, WS_OP = WS_HP + 8 * 4 * 512 * 2, WS_END = WS_OP + NDCU * 8192;
constexpr int WS_PROF = 2 << 20;
constexpr int L_FLAG = 0, L_MW = 512, L_HINT = 512, L_QS = 1024, L_OA = L_QS + 1024, L_HH = L_OA + 4096, L_SS = L_HH + 512;
constexpr int L_QRED = L_SS + 4096, L_PART = L_QRED + 5120, L_USED = L_PART + 11 * 2048;
constexpr int L_TOTAL = 84 * 1024;
static_assert(L_USED <= L_TOTAL, "lds");
enum { F_QIS = 0, F_QS = 4, F_QPUB = 12, F_QR = 16, F_CL = 24, F_GO = 32, F_SR = 72, F_MW = 80, F_VL = 96, F_OPF = 112 };
constexpr int CODE_BYTES = 15360;
constexpr unsigned SPIN_G = 1u << 17, SPIN_L = 1u << 21;

__device__ __forceinline__ u64 rt() { return __builtin_amdgcn_s_memrealtime(); }
__device__ __forceinline__ rsrc_t mk(const void* p, unsigned bytes) { return __builtin_amdgcn_make_buffer_rsrc((void*)p, 0, bytes, 0x00020000); }
__device__ __forceinline__ f32x4 mfma(v4u a, v4u b, f32x4 c) { return __builtin_amdgcn_mfma_f32_16x16x32_bf16(__builtin_bit_cast(bf16x8, a), __builtin_bit_cast(bf16x8, b), c, 0, 0, 0); }
__device__ __forceinline__ unsigned pk(float lo, float hi) { return __builtin_bit_cast(unsigned, __builtin_convertvector(f32x2{lo, hi}, bf16x2)); }
__device__ __forceinline__ float blo(unsigned v) { return __builtin_bit_cast(float, v << 16); }
__device__ __forceinline__ float bhi(unsigned v) { return __builtin_bit_cast(float, v & 0xffff0000u); }
__device__ __forceinline__ unsigned plo(unsigned a, unsigned b) { return __builtin_amdgcn_perm(b, a, 0x05040100u); }
__device__ __forceinline__ unsigned phi(unsigned a, unsigned b) { return __builtin_amdgcn_perm(b, a, 0x07060302u); }
__device__ __forceinline__ unsigned nosent(unsigned v) { return v < 0xFFFFFFFEu ? v : 0xFFFFFFFEu; }
template <int C> __device__ __forceinline__ float dpp(float x) { return __builtin_bit_cast(float, __builtin_amdgcn_mov_dpp(__builtin_bit_cast(int, x), C, 0xf, 0xf, true)); }
template <int C> __device__ __forceinline__ unsigned dppu(unsigned x) { return (unsigned)__builtin_amdgcn_mov_dpp((int)x, C, 0xf, 0xf, true); }
constexpr int XOR1 = 0xB1, XOR2 = 0x4E, ROR4 = 0x124, ROR8 = 0x128;
__device__ __forceinline__ float x16_sum(float x) { auto s = __builtin_amdgcn_permlane16_swap(__float_as_uint(x), __float_as_uint(x), false, false); return __uint_as_float(s[0]) + __uint_as_float(s[1]); }
__device__ __forceinline__ float x32_sum(float x) { auto s = __builtin_amdgcn_permlane32_swap(__float_as_uint(x), __float_as_uint(x), false, false); return __uint_as_float(s[0]) + __uint_as_float(s[1]); }
__device__ __forceinline__ float x16_max(float x) { auto s = __builtin_amdgcn_permlane16_swap(__float_as_uint(x), __float_as_uint(x), false, false); return fmaxf(__uint_as_float(s[0]), __uint_as_float(s[1])); }
__device__ __forceinline__ float x32_max(float x) { auto s = __builtin_amdgcn_permlane32_swap(__float_as_uint(x), __float_as_uint(x), false, false); return fmaxf(__uint_as_float(s[0]), __uint_as_float(s[1])); }
__device__ __forceinline__ float row_sum(float x) { x += dpp<XOR1>(x); x += dpp<XOR2>(x); x += dpp<ROR4>(x); x += dpp<ROR8>(x); return x; }
__device__ __forceinline__ float row_max(float x) { x = fmaxf(x, dpp<XOR1>(x)); x = fmaxf(x, dpp<XOR2>(x)); x = fmaxf(x, dpp<ROR4>(x)); x = fmaxf(x, dpp<ROR8>(x)); return x; }
__device__ __forceinline__ float dot2(unsigned a, unsigned b, float c) { return __builtin_amdgcn_fdot2_f32_bf16(__builtin_bit_cast(bf16x2, a), __builtin_bit_cast(bf16x2, b), c, false); }
#define CBAR() asm volatile("" ::: "memory")
#define SCHED() __builtin_amdgcn_sched_barrier(0)
#define DRAIN() do { asm volatile("s_waitcnt vmcnt(0)" ::: "memory"); __builtin_amdgcn_s_waitcnt(0x0F70); } while (0)
#define PINV(x) asm volatile("" : "+v"(x))
#define PINS(x) asm volatile("" : "+s"(x))
#define WGBAR() do { SCHED(); asm volatile("s_waitcnt lgkmcnt(0)" ::: "memory"); __builtin_amdgcn_s_barrier(); CBAR(); SCHED(); } while (0)
#define GSPIN _Pragma("nounroll") for (unsigned sp = 0; sp < SPIN_G; ++sp)
#define LD32(o, so) __builtin_amdgcn_raw_buffer_load_b32(rws, (o), (so), AUX_SC1)
#define LD64(o, so) __builtin_amdgcn_raw_buffer_load_b64(rws, (o), (so), AUX_SC1)
#define LD128(o, so) __builtin_amdgcn_raw_buffer_load_b128(rws, (o), (so), AUX_SC1)
#define ST64(v, o) __builtin_amdgcn_raw_buffer_store_b64((v), rws, (o), 0, AUX_SC1)
#define ST128(v, o) __builtin_amdgcn_raw_buffer_store_b128((v), rws, (o), 0, AUX_SC1)
__device__ __forceinline__ bool okv(v4u r) { return max(max(r.x, r.y), max(r.z, r.w)) != SENT; }
__device__ __forceinline__ void lds_wait(volatile LAS unsigned* f, unsigned tg) {
  SCHED();
#pragma nounroll
  for (unsigned s = 0; s < SPIN_L; ++s) { if (__all(*f == tg)) break; }
  CBAR();
  SCHED();
}
__device__ __forceinline__ void lds_set(volatile LAS unsigned* f, unsigned tg) { CBAR(); *f = tg; CBAR(); }
__device__ __forceinline__ void until(u64 t0, unsigned ticks) { while (rt() - t0 < ticks) __builtin_amdgcn_s_sleep(1); }
__device__ __forceinline__ v2u hint(unsigned taglo, unsigned taghi) { return v2u{taglo | ((unsigned)rt() & 0xFFFFu), taghi}; }
__device__ __forceinline__ bool hint_ok(v2u h, unsigned taglo, unsigned taghi) { return (h.y == taghi) & ((h.x ^ taglo) < 65536u); }
__device__ __forceinline__ void wait_age(unsigned t16, unsigned dly) {
#pragma nounroll
  for (unsigned s = 0; s < 4096u; ++s) { if (__all((((unsigned)rt() - t16) & 0xFFFFu) >= dly)) break; }
}
__device__ __forceinline__ void watch(rsrc_t rws, unsigned ho, unsigned taglo, unsigned taghi, LAS v2u* slot, unsigned tg) {
  GSPIN {
    CBAR();
    const v2u h = LD64(ho, 0);
    const bool ok = hint_ok(h, taglo, taghi);
    if (ok) *slot = v2u{tg, h.x};
    CBAR();
    if (__all(ok)) break;
  }
}
__device__ __forceinline__ void watch8(rsrc_t rws, unsigned ho, unsigned taglo, unsigned taghi, unsigned dly, volatile LAS unsigned* go, unsigned tg, int lane) {
  v2u h;
  unsigned seen = 0, done = 0;
#pragma nounroll
  for (unsigned sp = 0; sp < SPIN_G && done != 0x11111111u; ++sp) {
    if (seen != ~0u) {
      CBAR();
      h = LD64(ho, 0);
      seen = (unsigned)__builtin_amdgcn_ballot_w64(hint_ok(h, taglo, taghi));
    }
    unsigned m = (unsigned)__builtin_amdgcn_ballot_w64((((unsigned)rt() - h.x) & 0xFFFFu) >= dly) & seen;
    m &= m >> 1; m &= m >> 2; m &= 0x11111111u;
    if (m != done) { if ((m >> (4 * (lane & 7))) & 1u) go[lane & 7] = tg; done = m; }
  }
  CBAR();
}
__device__ __forceinline__ u64 load_tag(const unsigned char* ws) {
  u64 e;
  asm volatile("s_load_dwordx2 %0, %1, 0x0\n\ts_waitcnt lgkmcnt(0)" : "=s"(e) : "s"(ws) : "memory");
  return (e + 1ull) & 0xFFFFFFFFFFFFull;
}
__device__ __forceinline__ void code_fetch(int part) {
  u64 pc0;
  asm volatile("s_getpc_b64 %0" : "=s"(pc0));
  const u64 a0 = (pc0 & ~127ull) + (u64)(part * 512 < CODE_BYTES - 512 ? part * 512 : CODE_BYTES - 512);
  unsigned t0, t1, t2, t3, t4, t5, t6, t7;
  asm volatile("s_load_dword %0, %8, 0x0\n\ts_load_dword %1, %8, 0x40\n\ts_load_dword %2, %8, 0x80\n\ts_load_dword %3, %8, 0xc0\n\t"
               "s_load_dword %4, %8, 0x100\n\ts_load_dword %5, %8, 0x140\n\ts_load_dword %6, %8, 0x180\n\ts_load_dword %7, %8, 0x1c0\n\ts_waitcnt lgkmcnt(0)"
               : "=&s"(t0), "=&s"(t1), "=&s"(t2), "=&s"(t3), "=&s"(t4), "=&s"(t5), "=&s"(t6), "=&s"(t7) : "s"(a0) : "memory");
}
__device__ __forceinline__ v4i mkd(const void* p, unsigned bytes) { return v4i{(int)(u64)p, (int)((u64)p >> 32) & 0xFFFF, (int)bytes, 0x00020000}; }
template <int OFF> __device__ __forceinline__ void ldnt(v4u& d, unsigned vo, v4i rs, unsigned so) {
  asm volatile("buffer_load_dwordx4 %0, %1, %2, %3 offen offset:%4 nt" : "=v"(d) : "v"(vo), "s"(rs), "s"(so), "i"(OFF) : "memory");
}
template <int LO, int HI> __device__ __forceinline__ void ldtile(v4u* d, unsigned vo, v4i rs, unsigned so, unsigned so2) {
  if constexpr (LO < HI) { ldnt<(LO & 3) * 1024>(d[LO], vo, rs, LO < 4 ? so : so2); ldtile<LO + 1, HI>(d, vo, rs, so, so2); }
}
__device__ __forceinline__ void ldnt2(v2u& d, unsigned vo, v4i rs, unsigned so) {
  asm volatile("buffer_load_dwordx2 %0, %1, %2, %3 offen nt" : "=v"(d) : "v"(vo), "s"(rs), "s"(so) : "memory");
}
__device__ __forceinline__ void ldp4(v4u& d, unsigned vo, v4i rs) {
  asm volatile("buffer_load_dwordx4 %0, %1, %2, 0 offen" : "=v"(d) : "v"(vo), "s"(rs) : "memory");
}
#define VMWAIT2(n, a, b) asm volatile("s_waitcnt vmcnt(%2)" : "+v"(a), "+v"(b) : "i"(n) : "memory")
__device__ __forceinline__ void ask8(unsigned (&hp)[8], unsigned go, unsigned tg, unsigned ho, v4i rs) {
  unsigned t, done, n, cnt;
#define ASK1(j, off) "s_bitcmp1_b32 %10, " #j "\n\ts_cbranch_scc0 2" #j "f\n\ts_movk_i32 %11, " #off "\n\tbuffer_load_dword %" #j ", %14, %15, %11 offen sc1\n2" #j ":\n\t"
  asm volatile("s_mov_b32 %9, 0\n\ts_mov_b32 %11, 0x200000\n"
               "1:\n\tds_read_b32 %8, %12\n\ts_sub_u32 %11, %11, 1\n\ts_cbranch_scc1 3f\n\ts_waitcnt lgkmcnt(0)\n\t"
               "v_cmp_eq_u32 vcc, %13, %8\n\ts_andn2_b32 %10, vcc_lo, %9\n\ts_and_b32 %10, %10, 0xff\n\ts_cbranch_scc0 1b\n\t"
               ASK1(0, 0) ASK1(1, 0x1000) ASK1(2, 0x2000) ASK1(3, 0x3000) ASK1(4, 0x4000) ASK1(5, 0x5000) ASK1(6, 0x6000) ASK1(7, 0x7000)
               "s_mov_b32 %11, 0x200000\n\ts_or_b32 %9, %9, %10\n\ts_cmp_lg_u32 %9, 0xff\n\ts_cbranch_scc1 1b\n"
               "3:\n\ts_waitcnt vmcnt(0) lgkmcnt(0)"
               : "+v"(hp[0]), "+v"(hp[1]), "+v"(hp[2]), "+v"(hp[3]), "+v"(hp[4]), "+v"(hp[5]), "+v"(hp[6]), "+v"(hp[7]), "=&v"(t), "=&s"(done), "=&s"(n), "=&s"(cnt)
               : "v"(go), "s"(tg), "v"(ho), "s"(rs) : "vcc", "scc", "memory");
#undef ASK1
}
__device__ __forceinline__ f32x2 ln_stats(v4u hv) {
  const float c0 = blo((unsigned)__builtin_amdgcn_readfirstlane(hv[0]));
  float t[8];
#pragma unroll
  for (int d = 0; d < 4; ++d) { t[2 * d] = blo(hv[d]) - c0; t[2 * d + 1] = bhi(hv[d]) - c0; }
  float s1 = ((t[0] + t[1]) + (t[2] + t[3])) + ((t[4] + t[5]) + (t[6] + t[7]));
  float s2 = (fmaf(t[1], t[1], t[0] * t[0]) + fmaf(t[3], t[3], t[2] * t[2])) + (fmaf(t[5], t[5], t[4] * t[4]) + fmaf(t[7], t[7], t[6] * t[6]));
  s1 += dpp<XOR1>(s1); s2 += dpp<XOR1>(s2);
  s1 += dpp<XOR2>(s1); s2 += dpp<XOR2>(s2);
  s1 += dpp<ROR4>(s1); s2 += dpp<ROR4>(s2);
  s1 += dpp<ROR8>(s1); s2 += dpp<ROR8>(s2);
  s1 = x16_sum(s1); s2 = x16_sum(s2);
  s1 = x32_sum(s1); s2 = x32_sum(s2);
  const float d = s1 * (1.f / DM), var = fmaxf(fmaf(-d, d, s2 * (1.f / DM)), 0.f);
  return f32x2{c0 + d, __builtin_amdgcn_rsqf(var + 1e-5f)};
}
#define STAMP(k) do { if (PROF == 1) { __builtin_amdgcn_sched_barrier(0); ts[k] = (unsigned)rt(); __builtin_amdgcn_sched_barrier(0); } } while (0)
#define STAMPE(k) do { __builtin_amdgcn_sched_barrier(0); ts[k] = (unsigned)rt(); __builtin_amdgcn_sched_barrier(0); } while (0)
#define LDP(u, k) __hip_atomic_load((GAS unsigned*)(ws + WS_PROF + (size_t)(u) * 64 + (k) * 4), __ATOMIC_RELAXED, __HIP_MEMORY_SCOPE_AGENT)
#define PDUMP() do { until(tstart, 2500); if (lane == 0) { _Pragma("unroll") for (int k_ = 0; k_ < 12; ++k_) __hip_atomic_store((GAS unsigned*)(ws + WS_PROF + (((size_t)c * 16 + w) * 16 + k_) * 4), ts[k_], __ATOMIC_RELAXED, __HIP_MEMORY_SCOPE_AGENT); } \
  if (c == 0 && w == 11) { until(tstart, 4000); int mn = 0, mx = 0; \
    for (int u = lane; u < 4096; u += 64) mn = min(mn, (int)(LDP(u, 0) - (unsigned)tstart)); \
    for (int m_ = 32; m_ >= 1; m_ >>= 1) mn = min(mn, __shfl_xor(mn, m_)); \
    for (int u = lane; u < 4096; u += 64) { const int ww = u & 15, ii = u >> 7; const unsigned tk_ = LDP(u, 2), ta_ = LDP(u, 0); \
      if (ii >= 0 && ii <= 31 && ww >= 12 && ww <= 12 && tk_ != 0u) { const int v_ = ((int)(tk_ - (unsigned)tstart) - mn > 300) ? 1 : 0; mx += v_; } } \
    for (int m_ = 32; m_ >= 1; m_ >>= 1) mx += __shfl_xor(mx, m_); \
    mn = __builtin_amdgcn_readfirstlane(mn); mx = __builtin_amdgcn_readfirstlane(mx); \
    mx = mx < 0 ? 0 : mx > 3000 ? 3000 : mx; \
    until(tstart, (unsigned)(7000 + mn + 10 * mx)); } } while (0)
#define RETRY() ts[11]++

__device__ __forceinline__ void tr4(const v2u* s, v4u* o) {
#pragma unroll
  for (int m = 0; m < 4; ++m) {
    o[0][m] = plo(s[2 * m].x, s[2 * m + 1].x); o[1][m] = phi(s[2 * m].x, s[2 * m + 1].x);
    o[2][m] = plo(s[2 * m].y, s[2 * m + 1].y); o[3][m] = phi(s[2 * m].y, s[2 * m + 1].y);
  }
#pragma unroll
  for (int e = 0; e < 4; ++e) PINV(o[e]);
}
__device__ __forceinline__ void tr8(const v4u* s, v4u* o) {
#pragma unroll
  for (int m = 0; m < 4; ++m)
#pragma unroll
    for (int d = 0; d < 4; ++d) { o[2 * d][m] = plo(s[2 * m][d], s[2 * m + 1][d]); o[2 * d + 1][m] = phi(s[2 * m][d], s[2 * m + 1][d]); }
#pragma unroll
  for (int e = 0; e < 8; ++e) PINV(o[e]);
}

__global__ void __launch_bounds__(1024) fused_decode(const u16* __restrict__ x, const u16* __restrict__ kc, const u16* __restrict__ vc,
                                                     const u16* __restrict__ wq, const u16* __restrict__ wo, const u16* __restrict__ wg,
                                                     const u16* __restrict__ wu, const u16* __restrict__ wd, u16* __restrict__ out, unsigned char* ws) {
  const u64 tstart = rt();
  unsigned ts[12];
#pragma unroll
  for (int k_ = 0; k_ < 12; ++k_) ts[k_] = 0;
  ts[0] = (unsigned)tstart;
  SCHED();
  __shared__ __attribute__((aligned(16))) unsigned char smem[L_TOTAL];
  LAS unsigned char* L = (LAS unsigned char*)smem;
  volatile LAS unsigned* FL = (volatile LAS unsigned*)(L + L_FLAG);
  const int lane = threadIdx.x & 63;
  int w = (__builtin_amdgcn_readfirstlane(threadIdx.x >> 6) + 11) & 15;
  const int c = blockIdx.x, g = c & 7, i = c >> 3;
  if (w == 11) code_fetch(i);
  const int gq = lane >> 4, r16 = lane & 15;
  const int b = (i * 43) >> 8, s = i - b * NSPL;
  constexpr int ntile = 11;
  const int row0 = 344 * s - (s > 4 ? 8 : 0);
  const int cw = w - 12, rq = i & 3, cq = i >> 2, R0 = 128 * rq + 32 * cw;
  asm volatile("" :: "s"(x), "s"(kc), "s"(vc), "s"(wq), "s"(wo), "s"(wg), "s"(wu), "s"(wd), "s"(out), "s"(ws) : "memory");
  v4u kf[8], vf[8], xa, xr;
  v2u wsrc[8];
  const unsigned kvbase = ((unsigned)(b * HKV + g) * SK + (SK - WIN) + row0 + 16u * w) * 256u, kvbase2 = kvbase + ntile * 4096u;
  const v4i rk = mkd(kc, NB * HKV * SK * 256u), rv = mkd(vc, NB * HKV * SK * 256u);
  if (w >= 12) {
    __builtin_amdgcn_s_setprio(3);
    const v4i rw = mkd(wq, DM * NQ * 2u), rxd = mkd(x, NB * DM * 2u);
    const unsigned o = (unsigned)(8 * gq) * (NQ * 2) + r16 * 8, so = R0 * (NQ * 2) + (512 * g + 64 * cq) * 2;
#pragma unroll
    for (int j = 0; j < 8; ++j) ldnt2(wsrc[j], o, rw, so + j * (NQ * 2));
    ldp4(xa, (r16 >> 2) * 1024 + (R0 + 8 * gq) * 2, rxd);
    ldp4(xr, cw * 1024 + lane * 16, rxd);
  } else if (i < 24 && w < ntile) {
    __builtin_amdgcn_s_setprio(2);
    until(rt(), TK0);
    ldtile<0, KPRE>(kf, r16 * 64 + gq * 16, rk, kvbase, kvbase2);
  }
  STAMP(1);
  PINS(w);
  const rsrc_t rws = mk(ws, WS_END), rx = mk(x, NB * DM * 2u);
  const f32x4 Z4 = {0.f, 0.f, 0.f, 0.f};
  const v4u SV = {SENT, SENT, SENT, SENT};
  const unsigned orec = WS_OREC + (g * 24 + i) * OREC_SZ;

  if (__builtin_expect(w >= 12, 1)) {
    const unsigned tg = (unsigned)load_tag(ws);
    asm volatile("s_waitcnt vmcnt(0)" : "+v"(wsrc[0]), "+v"(wsrc[1]), "+v"(wsrc[2]), "+v"(wsrc[3]), "+v"(wsrc[4]), "+v"(wsrc[5]), "+v"(wsrc[6]), "+v"(wsrc[7]), "+v"(xa), "+v"(xr) :: "memory");
    {
      v4u bt[4];
      tr4(wsrc, bt);
      if ((lane & 3) == 1) xa = v4u{ONE2, ONE2, ONE2, ONE2};
      f32x4 qp, cs;
#pragma unroll
      for (int e = 0; e < 4; ++e) { const f32x4 d = mfma(xa, bt[e], Z4); qp[e] = d[0]; cs[e] = d[1]; }
      *(LAS f32x4*)(L + L_QRED + cw * 1024 + lane * 16) = qp;
      if (lane < 16) *(LAS f32x4*)(L + L_QRED + 4096 + cw * 256 + lane * 16) = cs;
    }
    lds_set(FL + F_QR + cw, tg);
    f32x2 xst = ln_stats(xr);
    PINV(xst);
    STAMP(2);
    lds_wait(FL + F_QR + (lane & 3), tg);
    {
      float a[4], cs[4];
#pragma unroll
      for (int j = 0; j < 4; ++j) { a[j] = *(const LAS float*)(L + L_QRED + j * 1024 + cw * 256 + lane * 4); cs[j] = *(const LAS float*)(L + L_QRED + 4096 + j * 256 + lane * 4); }
      const float q = xst.y * (((a[0] + a[1]) + (a[2] + a[3])) - xst.x * ((cs[0] + cs[1]) + (cs[2] + cs[3])));
      const unsigned q2 = pk(q, dpp<XOR1>(q));
      if ((lane & 1) == 0) ST64((v2u{q2, tg}), WS_QG + (((g * NB + cw) * 4 + rq) * 256 + 32 * cq) * 8 + lane * 4);
    }
    lds_set(FL + F_QPUB + cw, tg);
    STAMP(3);
    if (i < 24) {
      until(rt(), QDELTA);
      const unsigned qo = WS_QG + ((g * NB + b) * 4 * 256 + cw * 64 + lane) * 8;
      v2u qg[4];
      GSPIN {
        CBAR();
#pragma unroll
        for (int j = 0; j < 4; ++j) qg[j] = LD64(qo, j * 2048);
        SCHED();
        lds_set(FL + F_QIS + cw, tg);
        SCHED();
        if (__all((qg[0].y == tg) & (qg[1].y == tg) & (qg[2].y == tg) & (qg[3].y == tg))) break;
        RETRY();
      }
      const float q0 = (blo(qg[0].x) + blo(qg[1].x)) + (blo(qg[2].x) + blo(qg[3].x)), q1 = (bhi(qg[0].x) + bhi(qg[1].x)) + (bhi(qg[2].x) + bhi(qg[3].x));
      *(LAS unsigned*)(L + L_QS + (cw * 64 + lane) * 4) = pk(q0, q1);
      lds_set(FL + F_QS + cw, tg);
      STAMP(4);
      PDUMP();
      return;
    }
    __builtin_amdgcn_s_setprio(0);
  } else if (i < 24) {
    if (w < ntile) {
      const unsigned tg = (unsigned)load_tag(ws);
      const int nval = w < 10 ? 8 : s < 4 ? 6 : 4;
      const unsigned oob6 = nval < 6 ? 0x80000000u : 0u, oob8 = nval < 8 ? 0x80000000u : 0u;
      static_assert(KPRE == 4, "");
      if (KMID >= 1) { asm volatile("s_waitcnt vmcnt(%0)" :: "i"(KLEFT) : "memory"); ldtile<4, 6>(kf, (r16 * 64 + gq * 16) | oob6, rk, kvbase, kvbase2); }
      if (KMID >= 2) { asm volatile("s_waitcnt vmcnt(%0)" :: "i"(KLEFT) : "memory"); ldtile<6, 8>(kf, (r16 * 64 + gq * 16) | oob8, rk, kvbase, kvbase2); }
      lds_wait(FL + F_QPUB + (lane & 3), tg);
      if (KMID < 1) ldtile<4, 6>(kf, (r16 * 64 + gq * 16) | oob6, rk, kvbase, kvbase2);
      if (KMID < 2) ldtile<6, 8>(kf, (r16 * 64 + gq * 16) | oob8, rk, kvbase, kvbase2);
      if (VPRE) ldtile<0, VPRE>(vf, lane * 16, rv, kvbase, kvbase2);
      __builtin_amdgcn_s_setprio(0);
      lds_wait(FL + F_QIS + (lane & 3), tg);
      ldtile<VPRE, 4>(vf, lane * 16, rv, kvbase, kvbase2);
      ldtile<4, 6>(vf, (lane * 16) | oob6, rv, kvbase, kvbase2);
      ldtile<6, 8>(vf, (lane * 16) | oob8, rv, kvbase, kvbase2);
      STAMP(2);
      lds_wait(FL + (lane < 4 ? F_QS + lane : F_CL), tg);
      asm volatile("s_waitcnt vmcnt(8)" : "+v"(kf[0]), "+v"(kf[1]), "+v"(kf[2]), "+v"(kf[3]), "+v"(kf[4]), "+v"(kf[5]), "+v"(kf[6]), "+v"(kf[7]) :: "memory");
      STAMP(3);
      const v4u qf = *(const LAS v4u*)(L + L_QS + r16 * 64 + gq * 16);
      float sc[8];
#pragma unroll
      for (int t = 0; t < 8; ++t) {
        const f32x4 d = mfma(kf[t], qf, Z4);
        sc[t] = (dpp<0x00>(d[0]) + dpp<0x55>(d[1])) + (dpp<0xAA>(d[2]) + dpp<0xFF>(d[3]));
        if (t >= 4 && t >= nval) sc[t] = -__builtin_inff();
      }
      float m = fmaxf(fmaxf(fmaxf(sc[0], sc[1]), fmaxf(sc[2], sc[3])), fmaxf(fmaxf(sc[4], sc[5]), fmaxf(sc[6], sc[7])));
      m = x32_max(x16_max(m));
      constexpr float C2 = 0.08838834764831845f * 1.4426950408889634f;
      const float ms = m * C2;
      float p[8], l = 0.f;
#pragma unroll
      for (int t = 0; t < 8; ++t) { p[t] = __builtin_amdgcn_exp2f(fmaf(sc[t], C2, -ms)); l += p[t]; }
      l = x32_sum(x16_sum(l));
      if ((lane & 0x33) == 0) *(LAS f32x2*)(L + L_MW + ((lane >> 2) * 12 + w) * 8) = f32x2{ms, l};
      lds_set(FL + F_MW + w, tg);
      v4u pf = {pk(p[0], p[1]), pk(p[2], p[3]), pk(p[4], p[5]), pk(p[6], p[7])};
      unsigned pa = L_PART + w * 2048 + gq * 512 + r16 * 32;
      PINV(pf); PINV(pa);
      STAMP(4);
      v4u vt[8];
#pragma unroll
      for (int mm = 0; mm < 4; ++mm) {
        if (mm == 0) VMWAIT2(6, vf[0], vf[1]);
        if (mm == 1) VMWAIT2(4, vf[2], vf[3]);
        if (mm == 2) { lds_set(FL + F_VL + w, tg); VMWAIT2(2, vf[4], vf[5]); }
        if (mm == 3) { VMWAIT2(0, vf[6], vf[7]); STAMP(5); }
#pragma unroll
        for (int d = 0; d < 4; ++d) { vt[2 * d][mm] = plo(vf[2 * mm][d], vf[2 * mm + 1][d]); vt[2 * d + 1][mm] = phi(vf[2 * mm][d], vf[2 * mm + 1][d]); }
        if (mm < 3) {
#pragma unroll
          for (int e = 0; e < 8; ++e) PINV(vt[e]);
        }
        SCHED();
      }
      float o[8];
#pragma unroll
      for (int e = 0; e < 8; ++e) o[e] = mfma(pf, vt[e], Z4)[0];
      *(LAS f32x4*)(L + pa) = f32x4{o[0], o[1], o[2], o[3]};
      *(LAS f32x4*)(L + pa + 16) = f32x4{o[4], o[5], o[6], o[7]};
      lds_set(FL + F_OPF + w, tg);
      STAMP(6);
      if (w < 4) {
        const int jl = r16 < ntile ? r16 : 0;
        lds_wait(FL + F_MW + jl, tg);
        const f32x2 ml = *(const LAS f32x2*)(L + L_MW + (w * 12 + jl) * 8);
        const float mg = row_max(ml.x);
        const float wt = r16 < ntile ? __builtin_amdgcn_exp2f(ml.x - mg) : 0.f;
        const float lt = row_sum(wt * ml.y);
        float wj[11];
#pragma unroll
        for (int j = 0; j < 11; ++j) { wj[j] = __builtin_bit_cast(float, __builtin_amdgcn_readlane(__builtin_bit_cast(int, wt), j)); PINS(wj[j]); }
        if (lane == 0) ST64((v2u{nosent(__float_as_uint(mg)), nosent(__float_as_uint(lt))}), orec + 1024 + w * 8);
        unsigned ra = L_PART + w * 512 + lane * 8, so = orec + w * 256 + lane * 4;
        PINV(ra); PINV(so);
        lds_wait(FL + F_OPF + jl, tg);
        f32x2 pv[11];
#pragma unroll
        for (int j = 0; j < 11; ++j) pv[j] = *(const LAS f32x2*)(L + ra + j * 2048);
        SCHED();
        f32x2 a = pv[0] * wj[0], a2 = pv[1] * wj[1];
#pragma unroll
        for (int j = 2; j < 10; j += 2) { a += pv[j] * wj[j]; a2 += pv[j + 1] * wj[j + 1]; }
        a += pv[10] * wj[10];
        a += a2;
        const unsigned v = nosent(pk(a.x, a.y));
        const v4u o4 = {dppu<0x00>(v), dppu<0x55>(v), dppu<0xAA>(v), dppu<0xFF>(v)};
        if ((lane & 3) == 0) ST128(o4, so);
        STAMP(7);
      }
    } else if (w == 11) {
      __builtin_amdgcn_s_setprio(3);
      ST128(SV, orec + lane * 16);
      if (lane < 8) ST128(SV, orec + 1024 + lane * 16);
      const u64 tag = load_tag(ws);
      const unsigned taghi = (unsigned)(tag >> 16), taglo = (unsigned)(tag << 16), tg = (unsigned)tag;
      const bool isR = i < 8;
      const int ln = g * 8 + i, rb = ln >> 4, pg = lane >> 3, q4 = lane & 7, col = 32 * (ln & 15) + 4 * q4;
      v2u xv = {0u, 0u};
      if (isR) xv = __builtin_amdgcn_raw_buffer_load_b64(rx, (rb * DM + col) * 2, 0, 0);
      DRAIN();
      lds_set(FL + F_CL, tg);
      STAMP(3);
      lds_wait(FL + F_VL + (lane < ntile ? lane : 0), tg);
      if (lane == 0) ST64(hint(taglo, taghi), WS_HA + (g * 32 + i) * 128);
      STAMPE(4);
      if (isR) {
        const rsrc_t ro = mk(out, NB * DM * 2u);
        unsigned pf = __builtin_amdgcn_raw_buffer_load_b32(ro, 0, 0, AUX_SC1);
        asm volatile("" :: "v"(pf));
        v2u hb;
        GSPIN {
          CBAR();
          hb = LD64(WS_HB + ((lane & 7) * 4 + ((ln >> 2) & 3)) * 128, 0);
          if (__all(hint_ok(hb, taglo, taghi))) break;
          __builtin_amdgcn_s_sleep(2);
        }
        STAMP(5);
        wait_age(hb.x, DLY3 + 20);
        v2u hv;
        GSPIN {
          CBAR();
          hv = LD64(WS_HP + (pg * NB + rb) * 1024 + col * 2, 0);
          if (__all(max(hv.x, hv.y) != SENT)) break;
        }
        STAMP(6);
        float h[4] = {blo(hv.x), bhi(hv.x), blo(hv.y), bhi(hv.y)};
#pragma unroll
        for (int d = 0; d < 4; ++d) { h[d] += dpp<ROR8>(h[d]); h[d] = x32_sum(x16_sum(h[d])); }
        {
          const unsigned a0 = pk(h[0], h[1]), a1 = pk(h[2], h[3]);
          const unsigned h0 = pk(blo(xv.x) + blo(a0), bhi(xv.x) + bhi(a0)), h1 = pk(blo(xv.y) + blo(a1), bhi(xv.y) + bhi(a1));
          h[0] = blo(h0); h[1] = bhi(h0); h[2] = blo(h1); h[3] = bhi(h1);
        }
        unsigned po = WS_OP + pg * 8192 + ln * 128 + q4 * 16, po3 = pg < 4 ? po + 24 * 8192 : po, oo = (rb * DM + col) * 2;
        PINV(po); PINV(po3); PINV(oo); PINV(h[0]); PINV(h[1]); PINV(h[2]); PINV(h[3]);
        v2u hc;
        GSPIN {
          CBAR();
          hc = LD64(WS_HC + (lane < NDCU ? lane : 0) * 128, 0);
          if (__all(hint_ok(hc, taglo, taghi))) break;
        }
        wait_age(hc.x, DLY5);
        STAMP(7);
        v4u pv[4];
        GSPIN {
          CBAR();
#pragma unroll
          for (int t = 0; t < 3; ++t) pv[t] = LD128(po, t * 8 * 8192);
          pv[3] = LD128(po3, 0);
          if (__all(okv(pv[0]) & okv(pv[1]) & okv(pv[2]) & okv(pv[3]))) break;
          RETRY();
        }
        STAMP(8);
        float y[4];
#pragma unroll
        for (int d = 0; d < 4; ++d) {
          float t = (__uint_as_float(pv[0][d]) + __uint_as_float(pv[1][d])) + (__uint_as_float(pv[2][d]) + (pg < 4 ? __uint_as_float(pv[3][d]) : 0.f));
          t += dpp<ROR8>(t);
          y[d] = x32_sum(x16_sum(t));
        }
        const unsigned y0 = pk(y[0], y[1]), y1 = pk(y[2], y[3]);
        if (lane < 8) __builtin_amdgcn_raw_buffer_store_b64(v2u{pk(h[0] + blo(y0), h[1] + bhi(y0)), pk(h[2] + blo(y1), h[3] + bhi(y1))}, ro, oo, 0, AUX_SC1);
        if (ln == 0 && lane == 0) ST64((v2u{(unsigned)tag, (unsigned)(tag >> 32)}), WS_EPOCH);
        STAMPE(9);
      }
    }
    PDUMP();
    return;
  }

  const int jq = i - 24;
  const bool isC = jq < 4;
  const int dI = g + 8 * (jq - 4);
  if (!isC && dI >= NDCU) { PDUMP(); return; }
  const u64 tag = load_tag(ws);
  const unsigned taghi = (unsigned)(tag >> 16), taglo = (unsigned)(tag << 16), tg = (unsigned)tag;
#define GATHER_H() do { \
    unsigned klo = 0x00003F80u, khi = 0x3F800000u, ho = WS_HP + w * 256 + lane * 4, oa = L_OA + w * 256 + lane * 4; \
    unsigned hp[8] = {0u, 0u, 0u, 0u, 0u, 0u, 0u, 0u}; \
    float x0 = blo(xp), x1 = bhi(xp); \
    PINS(klo); PINS(khi); PINV(oa); PINV(x0); PINV(x1); \
    ask8(hp, (unsigned)(size_t)(FL + F_GO + (lane & 7)), tg, ho, mkd(ws, WS_END)); \
    if (!__all(max(max(max(hp[0], hp[1]), max(hp[2], hp[3])), max(max(hp[4], hp[5]), max(hp[6], hp[7]))) != SENT)) { \
      GSPIN { \
        RETRY(); \
        CBAR(); \
        _Pragma("unroll") for (int j = 0; j < 8; ++j) hp[j] = LD32(ho, j * 4096); \
        if (__all(max(max(max(hp[0], hp[1]), max(hp[2], hp[3])), max(max(hp[4], hp[5]), max(hp[6], hp[7]))) != SENT)) break; \
      } \
      DRAIN(); \
    } \
    STAMP(7); \
    float a0 = 0.f, a1 = 0.f, a2 = 0.f, a3 = 0.f; \
    _Pragma("unroll") for (int j = 0; j < 4; ++j) { a0 = dot2(hp[j], klo, a0); a1 = dot2(hp[j], khi, a1); a2 = dot2(hp[4 + j], klo, a2); a3 = dot2(hp[4 + j], khi, a3); } \
    const unsigned t0 = pk(a0 + a2, a1 + a3); \
    *(LAS unsigned*)(L + oa) = pk(x0 + blo(t0), x1 + bhi(t0)); \
  } while (0)
  unsigned xp = 0;
  if (!isC) xp = __builtin_amdgcn_raw_buffer_load_b32(rx, w * 256 + lane * 4, 0, 0);
  if (w < 8) {
    const unsigned stride = isC ? DM * 2 : FF * 2;
    const GAS unsigned char* p = isC ? (const GAS unsigned char*)wo + (size_t)(512 * g) * (DM * 2) + (128 * jq + 8 * r16) * 2
                                     : (const GAS unsigned char*)(r16 < 8 ? wg : wu) + (64 * dI + 8 * (r16 & 7)) * 2;
    p += (size_t)(64 * w + 8 * gq) * stride;
    unsigned aa = L_OA + (r16 >> 2) * 1024 + w * 128 + gq * 16, pa = L_PART + w * 2048 + gq * 512 + r16 * 32;
    PINV(aa); PINV(pa);
    v4u src[16], bt[16];
    until(tstart, isC ? TC0 : TD0);
#pragma unroll
    for (int j = 0; j < WPRE; ++j) src[j] = __builtin_nontemporal_load((const GAS v4u*)(p + ((j >> 3) * 32 + (j & 7)) * stride));
    lds_wait(FL + F_QPUB + (lane & 3), tg);
#pragma unroll
    for (int j = WPRE; j < 16; ++j) src[j] = __builtin_nontemporal_load((const GAS v4u*)(p + ((j >> 3) * 32 + (j & 7)) * stride));
    SCHED();
    STAMP(4);
    tr8(src, bt);
    tr8(src + 8, bt + 8);
    if (!isC) {
      const v4u ones = {ONE2, ONE2, ONE2, ONE2};
      float cs[8];
#pragma unroll
      for (int e = 0; e < 8; ++e) cs[e] = mfma(ones, bt[8 + e], mfma(ones, bt[e], Z4))[0];
      if (lane < 16) {
        *(LAS f32x4*)(L + L_SS + w * 512 + lane * 32) = f32x4{cs[0], cs[1], cs[2], cs[3]};
        *(LAS f32x4*)(L + L_SS + w * 512 + lane * 32 + 16) = f32x4{cs[4], cs[5], cs[6], cs[7]};
      }
      lds_set(FL + F_SR + w, tg);
    }
    DRAIN();
    STAMP(5);
    if (w == 0) {
      if (isC) {
        const int hl = lane < 24 ? lane : 0;
        watch(rws, WS_HA + (g * 32 + hl) * 128, taglo, taghi, (LAS v2u*)(L + L_HINT + hl * 8), tg);
      } else {
        watch8(rws, WS_HB + (lane & 31) * 128, taglo, taghi, DLY3, FL + F_GO, tg, lane);
      }
      STAMP(10);
      DRAIN();
    } else if (w == 1 && isC) {
      lds_wait(FL + F_SR + (lane & 7), tg);
      if (lane == 0) ST64(hint(taglo, taghi), WS_HB + (g * 4 + jq) * 128);
    }
    if (!isC) GATHER_H();
    WGBAR();
    STAMP(8);
    {
      const v4u a0 = *(const LAS v4u*)(L + aa), a1 = *(const LAS v4u*)(L + aa + 64);
#pragma unroll
      for (int h = 0; h < 2; ++h) {
        f32x4 acc[4];
#pragma unroll
        for (int e = 0; e < 4; ++e) acc[e] = mfma(a0, bt[4 * h + e], Z4);
#pragma unroll
        for (int e = 0; e < 4; ++e) acc[e] = mfma(a1, bt[8 + 4 * h + e], acc[e]);
        *(LAS f32x4*)(L + pa + 16 * h) = f32x4{acc[0][0], acc[1][0], acc[2][0], acc[3][0]};
      }
    }
    STAMP(9);
    WGBAR();
    if (!isC) WGBAR();
  } else if (isC) {
    const int v = w & 7, bb = v >> 1, head = 2 * (v & 1) + (lane >> 5), sg = gq & 1;
    unsigned ra = L_PART + (w & 3) * 512 + lane * 8, so = WS_HP + (g * NB + (w & 3)) * 1024 + jq * 256 + lane * 4;
    unsigned rd = WS_OREC + (g * 24 + bb * NSPL + sg) * OREC_SZ + head * 256 + r16 * 16, rs = WS_OREC + (g * 24 + bb * NSPL + sg) * OREC_SZ + 1024 + head * 8;
    unsigned oa = L_OA + bb * 1024 + head * 256 + r16 * 16 + sg * 8;
    PINV(ra); PINV(so); PINV(rd); PINV(rs); PINV(oa);
    if (w == 15) ST128(SV, WS_HP + (g * NB + gq) * 1024 + jq * 256 + r16 * 16);
    DRAIN();
    STAMP(5);
    {
      v2u e;
#pragma nounroll
      for (unsigned sp = 0; sp < SPIN_L; ++sp) { e = *(volatile LAS v2u*)(L + L_HINT + (bb * NSPL + (lane < NSPL ? lane : 0)) * 8); if (__all(e.x == tg)) break; }
      wait_age(e.y, DLY2);
    }
    STAMP(6);
    GSPIN {
      v4u od[3];
      v2u st[3];
      CBAR();
#pragma unroll
      for (int u = 0; u < 3; ++u) st[u] = LD64(rs, 2 * u * OREC_SZ);
      SCHED();
#pragma unroll
      for (int u = 0; u < 3; ++u) od[u] = LD128(rd, 2 * u * OREC_SZ);
      SCHED();
      lds_set(FL + F_SR + v, tg);
      SCHED();
      unsigned bad = max(max(max(st[0].x, st[0].y), max(st[1].x, st[1].y)), max(st[2].x, st[2].y));
      const float m0 = __uint_as_float(st[0].x), m1 = __uint_as_float(st[1].x), m2 = __uint_as_float(st[2].x);
      const float mg = x16_max(fmaxf(fmaxf(m0, m1), m2));
      float wt[3] = {__builtin_amdgcn_exp2f(m0 - mg), __builtin_amdgcn_exp2f(m1 - mg), __builtin_amdgcn_exp2f(m2 - mg)};
      const float inv = __builtin_amdgcn_rcpf(x16_sum(fmaf(wt[0], __uint_as_float(st[0].y), fmaf(wt[1], __uint_as_float(st[1].y), wt[2] * __uint_as_float(st[2].y)))));
#pragma unroll
      for (int u = 0; u < 3; ++u) { wt[u] *= inv; PINV(wt[u]); }
      PINV(bad);
      SCHED();
      f32x2 a[4];
#pragma unroll
      for (int u = 0; u < 3; ++u) {
        f32x2 t[4];
#pragma unroll
        for (int d = 0; d < 4; ++d) { t[d] = f32x2{blo(od[u][d]), bhi(od[u][d])}; PINV(t[d]); }
        bad = max(bad, max(max(od[u].x, od[u].y), max(od[u].z, od[u].w)));
#pragma unroll
        for (int d = 0; d < 4; ++d) a[d] = u == 0 ? t[d] * wt[0] : t[d] * wt[u] + a[d];
      }
      float r[4];
#pragma unroll
      for (int d = 0; d < 4; ++d) {
        const auto sw = __builtin_amdgcn_permlane16_swap(__float_as_uint(a[d >> 1][d & 1]), __float_as_uint(a[2 + (d >> 1)][d & 1]), false, false);
        r[d] = __uint_as_float(sw[0]) + __uint_as_float(sw[1]);
      }
      *(LAS v2u*)(L + oa) = v2u{pk(r[0], r[1]), pk(r[2], r[3])};
      STAMP(7);
      if (__all(bad != SENT)) break;
      RETRY();
    }
    DRAIN();
    STAMP(8);
    WGBAR();
    WGBAR();
    if (w >= 12) {
      f32x2 pv[8];
#pragma unroll
      for (int j = 0; j < 8; ++j) pv[j] = *(const LAS f32x2*)(L + ra + j * 2048);
      SCHED();
      const f32x2 t = ((pv[0] + pv[1]) + (pv[2] + pv[3])) + ((pv[4] + pv[5]) + (pv[6] + pv[7]));
      const unsigned vv = nosent(pk(t.x, t.y));
      const v4u o4 = {dppu<0x00>(vv), dppu<0x55>(vv), dppu<0xAA>(vv), dppu<0xFF>(vv)};
      if ((lane & 3) == 0) ST128(o4, so);
      STAMPE(9);
    }
  } else {
    unsigned ra = L_PART + (w & 3) * 512 + lane * 4, hw = L_HH + (w & 3) * 128 + lane * 2, ha = L_HH + (r16 >> 2) * 128 + gq * 16;
    unsigned so = WS_OP + dI * 8192 + gq * 2048 + (w & 7) * 256 + r16 * 16, sa = L_OA + (w & 3) * 1024 + lane * 16;
    PINV(ra); PINV(hw); PINV(ha); PINV(so); PINV(sa);
    ST128(SV, WS_OP + dI * 8192 + (w - 8) * 1024 + lane * 16);
    until(tstart, TWD);
    const rsrc_t rw = mk(wd, FF * DM * 2u);
    v2u src[16];
    v4u bt[8];
#pragma unroll
    for (int j = 0; j < 16; ++j) src[j] = __builtin_amdgcn_raw_buffer_load_b64(rw, (unsigned)(8 * gq + (j & 3)) * 1024 + (w - 8) * 128 + r16 * 8, (64 * dI + (j >> 3) * 32 + (j & 4)) * 1024, AUX_NT);
    SCHED();
    tr4(src, bt);
    tr4(src + 8, bt + 4);
    float sgs = 0.f, sus = 0.f;
    if (w >= 12) {
      lds_wait(FL + F_SR + (lane & 7), tg);
#pragma unroll
      for (int j = 0; j < 8; ++j) { sgs += *(const LAS float*)(L + L_SS + j * 512 + lane * 4); sus += *(const LAS float*)(L + L_SS + j * 512 + 256 + lane * 4); }
    }
    PINV(sgs); PINV(sus);
    DRAIN();
    STAMP(5);
    GATHER_H();
    WGBAR();
    if (w >= 12) {
      const f32x2 st = ln_stats(*(const LAS v4u*)(L + sa));
      float kg = st.y * -1.4426950408889634f, cg = st.x * sgs, cu = st.x * sus, ku = st.y;
      PINV(kg); PINV(cg); PINV(cu); PINV(ku);
      WGBAR();
      float gp[8], up[8];
#pragma unroll
      for (int j = 0; j < 8; ++j) { gp[j] = *(const LAS float*)(L + ra + j * 2048); up[j] = *(const LAS float*)(L + ra + j * 2048 + 256); }
      SCHED();
      const float gg = ((gp[0] + gp[1]) + (gp[2] + gp[3])) + ((gp[4] + gp[5]) + (gp[6] + gp[7]));
      const float uu = ((up[0] + up[1]) + (up[2] + up[3])) + ((up[4] + up[5]) + (up[6] + up[7]));
      const float hh = ku * (uu - cu) * __builtin_amdgcn_rcpf(1.f + __builtin_amdgcn_exp2f(kg * (gg - cg)));
      *(LAS u16*)(L + hw) = (u16)pk(hh, hh);
    } else {
      if (w == 8 && lane == 0) ST64(hint(taglo, taghi), WS_HC + dI * 128);
      WGBAR();
    }
    WGBAR();
    STAMP(8);
    const v4u a0 = *(const LAS v4u*)(L + ha), a1 = *(const LAS v4u*)(L + ha + 64);
    f32x4 acc[4];
#pragma unroll
    for (int e = 0; e < 4; ++e) acc[e] = mfma(a0, bt[e], Z4);
#pragma unroll
    for (int e = 0; e < 4; ++e) acc[e] = mfma(a1, bt[4 + e], acc[e]);
    ST128((v4u{nosent(__float_as_uint(acc[0][0])), nosent(__float_as_uint(acc[1][0])), nosent(__float_as_uint(acc[2][0])), nosent(__float_as_uint(acc[3][0]))}), so);
    STAMPE(9);
  }
  PDUMP();
}
}

extern "C" void kernel_launch(void* const* d_in, const int* in_sizes, int n_in, void* d_out, int out_size, void* d_ws, size_t ws_size, hipStream_t stream) {
  (void)in_sizes; (void)n_in; (void)out_size; (void)ws_size;
  fused_decode<<<dim3(256), dim3(1024), 0, stream>>>((const u16*)d_in[0], (const u16*)d_in[1], (const u16*)d_in[2], (const u16*)d_in[3], (const u16*)d_in[4],
                                                     (const u16*)d_in[6], (const u16*)d_in[7], (const u16*)d_in[8], (u16*)d_out, (unsigned char*)d_ws);
}
```

```cpp
#include <hip/hip_runtime.h>
#include <stdint.h>

#define PROF 1
#define TK0 35
#define KPRE 4
#define QDELTA 70
#define TC0 245
#define TD0 100
#define KMID 0
#define KLEFT 2
#define VPRE 0
#define WPRE 3
#define TWD 450
#define DLY2 60
#define DLY3 85
#define DLY5 76

namespace {
typedef unsigned short u16;
typedef unsigned long long u64;
typedef unsigned v4u __attribute__((ext_vector_type(4)));
typedef unsigned v2u __attribute__((ext_vector_type(2)));
typedef int v4i __attribute__((ext_vector_type(4)));
typedef float f32x4 __attribute__((ext_vector_type(4)));
typedef float f32x2 __attribute__((ext_vector_type(2)));
typedef short bf16x8 __attribute__((ext_vector_type(8)));
typedef __bf16 bf16x2 __attribute__((ext_vector_type(2)));
#define GAS __attribute__((address_space(1)))
#define LAS __attribute__((address_space(3)))
typedef __amdgpu_buffer_rsrc_t rsrc_t;

constexpr int NB = 4, DM = 512, HKV = 8, SK = 8192, WIN = 2048, FF = 1792, NQ = 4096;
constexpr int NSPL = 6, NDCU = 28;
constexpr int AUX_NT = 2, AUX_SC1 = 16;
constexpr unsigned SENT = 0xFFFFFFFFu, ONE2 = 0x3F803F80u;
constexpr int WS_EPOCH = 0, WS_HB = 4096, WS_HC = WS_HB + 32 * 128, WS_HA = WS_HC + 32 * 128, WS_QG = WS_HA + 8 * 32 * 128;
constexpr int WS_OREC = WS_QG + 8 * 4 * 4 * 256 * 8, OREC_SZ = 1152;
constexpr int WS_HP = WS_OREC + 8 * 24 * OREC_SZ, WS_OP = WS_HP + 8 * 4 * 512 * 2, WS_END = WS_OP + NDCU * 8192;
constexpr int WS_PROF = 2 << 20;
constexpr int L_FLAG = 0, L_MW = 512, L_HINT = 512, L_QS = 1024, L_OA = L_QS + 1024, L_HH = L_OA + 4096, L_SS = L_HH + 512;
constexpr int L_QRED = L_SS + 4096, L_PART = L_QRED + 5120, L_USED = L_PART + 11 * 2048;
constexpr int L_TOTAL = 84 * 1024;
static_assert(L_USED <= L_TOTAL, "lds");
enum { F_QIS = 0, F_QS = 4, F_QPUB = 12, F_QR = 16, F_CL = 24, F_GO = 32, F_SR = 72, F_MW = 80, F_VL = 96, F_OPF = 112 };
constexpr int CODE_BYTES = 15360;
constexpr unsigned SPIN_G = 1u << 17, SPIN_L = 1u << 21;

__device__ __forceinline__ u64 rt() { return __builtin_amdgcn_s_memrealtime(); }
__device__ __forceinline__ rsrc_t mk(const void* p, unsigned bytes) { return __builtin_amdgcn_make_buffer_rsrc((void*)p, 0, bytes, 0x00020000); }
__device__ __forceinline__ f32x4 mfma(v4u a, v4u b, f32x4 c) { return __builtin_amdgcn_mfma_f32_16x16x32_bf16(__builtin_bit_cast(bf16x8, a), __builtin_bit_cast(bf16x8, b), c, 0, 0, 0); }
__device__ __forceinline__ unsigned pk(float lo, float hi) { return __builtin_bit_cast(unsigned, __builtin_convertvector(f32x2{lo, hi}, bf16x2)); }
__device__ __forceinline__ float blo(unsigned v) { return __builtin_bit_cast(float, v << 16); }
__device__ __forceinline__ float bhi(unsigned v) { return __builtin_bit_cast(float, v & 0xffff0000u); }
__device__ __forceinline__ unsigned plo(unsigned a, unsigned b) { return __builtin_amdgcn_perm(b, a, 0x05040100u); }
__device__ __forceinline__ unsigned phi(unsigned a, unsigned b) { return __builtin_amdgcn_perm(b, a, 0x07060302u); }
__device__ __forceinline__ unsigned nosent(unsigned v) { return v < 0xFFFFFFFEu ? v : 0xFFFFFFFEu; }
template <int C> __device__ __forceinline__ float dpp(float x) { return __builtin_bit_cast(float, __builtin_amdgcn_mov_dpp(__builtin_bit_cast(int, x), C, 0xf, 0xf, true)); }
template <int C> __device__ __forceinline__ unsigned dppu(unsigned x) { return (unsigned)__builtin_amdgcn_mov_dpp((int)x, C, 0xf, 0xf, true); }
constexpr int XOR1 = 0xB1, XOR2 = 0x4E, ROR4 = 0x124, ROR8 = 0x128;
__device__ __forceinline__ float x16_sum(float x) { auto s = __builtin_amdgcn_permlane16_swap(__float_as_uint(x), __float_as_uint(x), false, false); return __uint_as_float(s[0]) + __uint_as_float(s[1]); }
__device__ __forceinline__ float x32_sum(float x) { auto s = __builtin_amdgcn_permlane32_swap(__float_as_uint(x), __float_as_uint(x), false, false); return __uint_as_float(s[0]) + __uint_as_float(s[1]); }
__device__ __forceinline__ float x16_max(float x) { auto s = __builtin_amdgcn_permlane16_swap(__float_as_uint(x), __float_as_uint(x), false, false); return fmaxf(__uint_as_float(s[0]), __uint_as_float(s[1])); }
__device__ __forceinline__ float x32_max(float x) { auto s = __builtin_amdgcn_permlane32_swap(__float_as_uint(x), __float_as_uint(x), false, false); return fmaxf(__uint_as_float(s[0]), __uint_as_float(s[1])); }
__device__ __forceinline__ float row_sum(float x) { x += dpp<XOR1>(x); x += dpp<XOR2>(x); x += dpp<ROR4>(x); x += dpp<ROR8>(x); return x; }
__device__ __forceinline__ float row_max(float x) { x = fmaxf(x, dpp<XOR1>(x)); x = fmaxf(x, dpp<XOR2>(x)); x = fmaxf(x, dpp<ROR4>(x)); x = fmaxf(x, dpp<ROR8>(x)); return x; }
__device__ __forceinline__ float dot2(unsigned a, unsigned b, float c) { return __builtin_amdgcn_fdot2_f32_bf16(__builtin_bit_cast(bf16x2, a), __builtin_bit_cast(bf16x2, b), c, false); }
#define CBAR() asm volatile("" ::: "memory")
#define SCHED() __builtin_amdgcn_sched_barrier(0)
#define DRAIN() do { asm volatile("s_waitcnt vmcnt(0)" ::: "memory"); __builtin_amdgcn_s_waitcnt(0x0F70); } while (0)
#define PINV(x) asm volatile("" : "+v"(x))
#define PINS(x) asm volatile("" : "+s"(x))
#define WGBAR() do { SCHED(); asm volatile("s_waitcnt lgkmcnt(0)" ::: "memory"); __builtin_amdgcn_s_barrier(); CBAR(); SCHED(); } while (0)
#define GSPIN _Pragma("nounroll") for (unsigned sp = 0; sp < SPIN_G; ++sp)
#define LD32(o, so) __builtin_amdgcn_raw_buffer_load_b32(rws, (o), (so), AUX_SC1)
#define LD64(o, so) __builtin_amdgcn_raw_buffer_load_b64(rws, (o), (so), AUX_SC1)
#define LD128(o, so) __builtin_amdgcn_raw_buffer_load_b128(rws, (o), (so), AUX_SC1)
#define ST64(v, o) __builtin_amdgcn_raw_buffer_store_b64((v), rws, (o), 0, AUX_SC1)
#define ST128(v, o) __builtin_amdgcn_raw_buffer_store_b128((v), rws, (o), 0, AUX_SC1)
__device__ __forceinline__ bool okv(v4u r) { return max(max(r.x, r.y), max(r.z, r.w)) != SENT; }
__device__ __forceinline__ void lds_wait(volatile LAS unsigned* f, unsigned tg) {
  SCHED();
#pragma nounroll
  for (unsigned s = 0; s < SPIN_L; ++s) { if (__all(*f == tg)) break; }
  CBAR();
  SCHED();
}
__device__ __forceinline__ void lds_set(volatile LAS unsigned* f, unsigned tg) { CBAR(); *f = tg; CBAR(); }
__device__ __forceinline__ void until(u64 t0, unsigned ticks) { while (rt() - t0 < ticks) __builtin_amdgcn_s_sleep(1); }
__device__ __forceinline__ v2u hint(unsigned taglo, unsigned taghi) { return v2u{taglo | ((unsigned)rt() & 0xFFFFu), taghi}; }
__device__ __forceinline__ bool hint_ok(v2u h, unsigned taglo, unsigned taghi) { return (h.y == taghi) & ((h.x ^ taglo) < 65536u); }
__device__ __forceinline__ void wait_age(unsigned t16, unsigned dly) {
#pragma nounroll
  for (unsigned s = 0; s < 4096u; ++s) { if (__all((((unsigned)rt() - t16) & 0xFFFFu) >= dly)) break; }
}
__device__ __forceinline__ void watch(rsrc_t rws, unsigned ho, unsigned taglo, unsigned taghi, LAS v2u* slot, unsigned tg) {
  GSPIN {
    CBAR();
    const v2u h = LD64(ho, 0);
    const bool ok = hint_ok(h, taglo, taghi);
    if (ok) *slot = v2u{tg, h.x};
    CBAR();
    if (__all(ok)) break;
  }
}
__device__ __forceinline__ void watch8(rsrc_t rws, unsigned ho, unsigned taglo, unsigned taghi, unsigned dly, volatile LAS unsigned* go, unsigned tg, int lane) {
  v2u h;
  unsigned seen = 0, done = 0;
#pragma nounroll
  for (unsigned sp = 0; sp < SPIN_G && done != 0x11111111u; ++sp) {
    if (seen != ~0u) {
      CBAR();
      h = LD64(ho, 0);
      seen = (unsigned)__builtin_amdgcn_ballot_w64(hint_ok(h, taglo, taghi));
    }
    unsigned m = (unsigned)__builtin_amdgcn_ballot_w64((((unsigned)rt() - h.x) & 0xFFFFu) >= dly) & seen;
    m &= m >> 1; m &= m >> 2; m &= 0x11111111u;
    if (m != done) { if ((m >> (4 * (lane & 7))) & 1u) go[lane & 7] = tg; done = m; }
  }
  CBAR();
}
__device__ __forceinline__ u64 load_tag(const unsigned char* ws) {
  u64 e;
  asm volatile("s_load_dwordx2 %0, %1, 0x0\n\ts_waitcnt lgkmcnt(0)" : "=s"(e) : "s"(ws) : "memory");
  return (e + 1ull) & 0xFFFFFFFFFFFFull;
}
__device__ __forceinline__ void code_fetch(int part) {
  u64 pc0;
  asm volatile("s_getpc_b64 %0" : "=s"(pc0));
  const u64 a0 = (pc0 & ~127ull) + (u64)(part * 512 < CODE_BYTES - 512 ? part * 512 : CODE_BYTES - 512);
  unsigned t0, t1, t2, t3, t4, t5, t6, t7;
  asm volatile("s_load_dword %0, %8, 0x0\n\ts_load_dword %1, %8, 0x40\n\ts_load_dword %2, %8, 0x80\n\ts_load_dword %3, %8, 0xc0\n\t"
               "s_load_dword %4, %8, 0x100\n\ts_load_dword %5, %8, 0x140\n\ts_load_dword %6, %8, 0x180\n\ts_load_dword %7, %8, 0x1c0\n\ts_waitcnt lgkmcnt(0)"
               : "=&s"(t0), "=&s"(t1), "=&s"(t2), "=&s"(t3), "=&s"(t4), "=&s"(t5), "=&s"(t6), "=&s"(t7) : "s"(a0) : "memory");
}
__device__ __forceinline__ v4i mkd(const void* p, unsigned bytes) { return v4i{(int)(u64)p, (int)((u64)p >> 32) & 0xFFFF, (int)bytes, 0x00020000}; }
template <int OFF> __device__ __forceinline__ void ldnt(v4u& d, unsigned vo, v4i rs, unsigned so) {
  asm volatile("buffer_load_dwordx4 %0, %1, %2, %3 offen offset:%4 nt" : "=v"(d) : "v"(vo), "s"(rs), "s"(so), "i"(OFF) : "memory");
}
template <int LO, int HI> __device__ __forceinline__ void ldtile(v4u* d, unsigned vo, v4i rs, unsigned so, unsigned so2) {
  if constexpr (LO < HI) { ldnt<(LO & 3) * 1024>(d[LO], vo, rs, LO < 4 ? so : so2); ldtile<LO + 1, HI>(d, vo, rs, so, so2); }
}
__device__ __forceinline__ void ldnt2(v2u& d, unsigned vo, v4i rs, unsigned so) {
  asm volatile("buffer_load_dwordx2 %0, %1, %2, %3 offen nt" : "=v"(d) : "v"(vo), "s"(rs), "s"(so) : "memory");
}
__device__ __forceinline__ void ldp4(v4u& d, unsigned vo, v4i rs) {
  asm volatile("buffer_load_dwordx4 %0, %1, %2, 0 offen" : "=v"(d) : "v"(vo), "s"(rs) : "memory");
}
#define VMWAIT2(n, a, b) asm volatile("s_waitcnt vmcnt(%2)" : "+v"(a), "+v"(b) : "i"(n) : "memory")
__device__ __forceinline__ void ask8(unsigned (&hp)[8], unsigned go, unsigned tg, unsigned ho, v4i rs) {
  unsigned t, done, n, cnt;
#define ASK1(j, off) "s_bitcmp1_b32 %10, " #j "\n\ts_cbranch_scc0 2" #j "f\n\ts_movk_i32 %11, " #off "\n\tbuffer_load_dword %" #j ", %14, %15, %11 offen sc1\n2" #j ":\n\t"
  asm volatile("s_mov_b32 %9, 0\n\ts_mov_b32 %11, 0x200000\n"
               "1:\n\tds_read_b32 %8, %12\n\ts_sub_u32 %11, %11, 1\n\ts_cbranch_scc1 3f\n\ts_waitcnt lgkmcnt(0)\n\t"
               "v_cmp_eq_u32 vcc, %13, %8\n\ts_andn2_b32 %10, vcc_lo, %9\n\ts_and_b32 %10, %10, 0xff\n\ts_cbranch_scc0 1b\n\t"
               ASK1(0, 0) ASK1(1, 0x1000) ASK1(2, 0x2000) ASK1(3, 0x3000) ASK1(4, 0x4000) ASK1(5, 0x5000) ASK1(6, 0x6000) ASK1(7, 0x7000)
               "s_mov_b32 %11, 0x200000\n\ts_or_b32 %9, %9, %10\n\ts_cmp_lg_u32 %9, 0xff\n\ts_cbranch_scc1 1b\n"
               "3:\n\ts_waitcnt vmcnt(0) lgkmcnt(0)"
               : "+v"(hp[0]), "+v"(hp[1]), "+v"(hp[2]), "+v"(hp[3]), "+v"(hp[4]), "+v"(hp[5]), "+v"(hp[6]), "+v"(hp[7]), "=&v"(t), "=&s"(done), "=&s"(n), "=&s"(cnt)
               : "v"(go), "s"(tg), "v"(ho), "s"(rs) : "vcc", "scc", "memory");
#undef ASK1
}
__device__ __forceinline__ f32x2 ln_stats(v4u hv) {
  const float c0 = blo((unsigned)__builtin_amdgcn_readfirstlane(hv[0]));
  float t[8];
#pragma unroll
  for (int d = 0; d < 4; ++d) { t[2 * d] = blo(hv[d]) - c0; t[2 * d + 1] = bhi(hv[d]) - c0; }
  float s1 = ((t[0] + t[1]) + (t[2] + t[3])) + ((t[4] + t[5]) + (t[6] + t[7]));
  float s2 = (fmaf(t[1], t[1], t[0] * t[0]) + fmaf(t[3], t[3], t[2] * t[2])) + (fmaf(t[5], t[5], t[4] * t[4]) + fmaf(t[7], t[7], t[6] * t[6]));
  s1 += dpp<XOR1>(s1); s2 += dpp<XOR1>(s2);
  s1 += dpp<XOR2>(s1); s2 += dpp<XOR2>(s2);
  s1 += dpp<ROR4>(s1); s2 += dpp<ROR4>(s2);
  s1 += dpp<ROR8>(s1); s2 += dpp<ROR8>(s2);
  s1 = x16_sum(s1); s2 = x16_sum(s2);
  s1 = x32_sum(s1); s2 = x32_sum(s2);
  const float d = s1 * (1.f / DM), var = fmaxf(fmaf(-d, d, s2 * (1.f / DM)), 0.f);
  return f32x2{c0 + d, __builtin_amdgcn_rsqf(var + 1e-5f)};
}
#define STAMP(k) do { if (PROF == 1) { __builtin_amdgcn_sched_barrier(0); ts[k] = (unsigned)rt(); __builtin_amdgcn_sched_barrier(0); } } while (0)
#define STAMPE(k) do { __builtin_amdgcn_sched_barrier(0); ts[k] = (unsigned)rt(); __builtin_amdgcn_sched_barrier(0); } while (0)
#define LDP(u, k) __hip_atomic_load((GAS unsigned*)(ws + WS_PROF + (size_t)(u) * 64 + (k) * 4), __ATOMIC_RELAXED, __HIP_MEMORY_SCOPE_AGENT)
#define PDUMP() do { until(tstart, 2500); ts[10] = (unsigned)__builtin_amdgcn_s_getreg(20 | (3 << 11)) & 7u; if (lane == 0) { _Pragma("unroll") for (int k_ = 0; k_ < 12; ++k_) __hip_atomic_store((GAS unsigned*)(ws + WS_PROF + (((size_t)c * 16 + w) * 16 + k_) * 4), ts[k_], __ATOMIC_RELAXED, __HIP_MEMORY_SCOPE_AGENT); } \
  if (c == 0 && w == 11) { until(tstart, 4000); int mn = 0, mx = 0; \
    for (int u = lane; u < 4096; u += 64) mn = min(mn, (int)(LDP(u, 0) - (unsigned)tstart)); \
    for (int m_ = 32; m_ >= 1; m_ >>= 1) mn = min(mn, __shfl_xor(mn, m_)); \
    for (int u = lane; u < 4096; u += 64) { const int ww = u & 15; const unsigned tk_ = LDP(u, 1), xc_ = LDP(u, 10); \
      if (ww == 12 && (int)(tk_ - (unsigned)tstart) - mn > 150) mx |= 1 << (2 ? xc_ : ((u >> 4) & 7)); } \
    for (int m_ = 32; m_ >= 1; m_ >>= 1) mx |= __shfl_xor(mx, m_); \
    mn = __builtin_amdgcn_readfirstlane(mn); mx = __builtin_amdgcn_readfirstlane(mx); \
    if (2 == 2) mx = LDP(12, 10); \
    until(tstart, (unsigned)(7000 + mn + 100 * mx)); } } while (0)
#define RETRY() ts[11]++

__device__ __forceinline__ void tr4(const v2u* s, v4u* o) {
#pragma unroll
  for (int m = 0; m < 4; ++m) {
    o[0][m] = plo(s[2 * m].x, s[2 * m + 1].x); o[1][m] = phi(s[2 * m].x, s[2 * m + 1].x);
    o[2][m] = plo(s[2 * m].y, s[2 * m + 1].y); o[3][m] = phi(s[2 * m].y, s[2 * m + 1].y);
  }
#pragma unroll
  for (int e = 0; e < 4; ++e) PINV(o[e]);
}
__device__ __forceinline__ void tr8(const v4u* s, v4u* o) {
#pragma unroll
  for (int m = 0; m < 4; ++m)
#pragma unroll
    for (int d = 0; d < 4; ++d) { o[2 * d][m] = plo(s[2 * m][d], s[2 * m + 1][d]); o[2 * d + 1][m] = phi(s[2 * m][d], s[2 * m + 1][d]); }
#pragma unroll
  for (int e = 0; e < 8; ++e) PINV(o[e]);
}

__global__ void __launch_bounds__(1024) fused_decode(const u16* __restrict__ x, const u16* __restrict__ kc, const u16* __restrict__ vc,
                                                     const u16* __restrict__ wq, const u16* __restrict__ wo, const u16* __restrict__ wg,
                                                     const u16* __restrict__ wu, const u16* __restrict__ wd, u16* __restrict__ out, unsigned char* ws) {
  const u64 tstart = rt();
  unsigned ts[12];
#pragma unroll
  for (int k_ = 0; k_ < 12; ++k_) ts[k_] = 0;
  ts[0] = (unsigned)tstart;
  SCHED();
  __shared__ __attribute__((aligned(16))) unsigned char smem[L_TOTAL];
  LAS unsigned char* L = (LAS unsigned char*)smem;
  volatile LAS unsigned* FL = (volatile LAS unsigned*)(L + L_FLAG);
  const int lane = threadIdx.x & 63;
  int w = (__builtin_amdgcn_readfirstlane(threadIdx.x >> 6) + 11) & 15;
  const int c = blockIdx.x, g = c & 7, i = c >> 3;
  if (w == 11) code_fetch(i);
  const int gq = lane >> 4, r16 = lane & 15;
  const int b = (i * 43) >> 8, s = i - b * NSPL;
  constexpr int ntile = 11;
  const int row0 = 344 * s - (s > 4 ? 8 : 0);
  const int cw = w - 12, rq = i & 3, cq = i >> 2, R0 = 128 * rq + 32 * cw;
  asm volatile("" :: "s"(x), "s"(kc), "s"(vc), "s"(wq), "s"(wo), "s"(wg), "s"(wu), "s"(wd), "s"(out), "s"(ws) : "memory");
  v4u kf[8], vf[8], xa, xr;
  v2u wsrc[8];
  const unsigned kvbase = ((unsigned)(b * HKV + g) * SK + (SK - WIN) + row0 + 16u * w) * 256u, kvbase2 = kvbase + ntile * 4096u;
  const v4i rk = mkd(kc, NB * HKV * SK * 256u), rv = mkd(vc, NB * HKV * SK * 256u);
  if (w >= 12) {
    __builtin_amdgcn_s_setprio(3);
    const v4i rw = mkd(wq, DM * NQ * 2u), rxd = mkd(x, NB * DM * 2u);
    const unsigned o = (unsigned)(8 * gq) * (NQ * 2) + r16 * 8, so = R0 * (NQ * 2) + (512 * g + 64 * cq) * 2;
#pragma unroll
    for (int j = 0; j < 8; ++j) ldnt2(wsrc[j], o, rw, so + j * (NQ * 2));
    ldp4(xa, (r16 >> 2) * 1024 + (R0 + 8 * gq) * 2, rxd);
    ldp4(xr, cw * 1024 + lane * 16, rxd);
  } else if (i < 24 && w < ntile) {
    __builtin_amdgcn_s_setprio(2);
    until(rt(), TK0);
    ldtile<0, KPRE>(kf, r16 * 64 + gq * 16, rk, kvbase, kvbase2);
  }
  STAMP(1);
  PINS(w);
  const rsrc_t rws = mk(ws, WS_END), rx = mk(x, NB * DM * 2u);
  const f32x4 Z4 = {0.f, 0.f, 0.f, 0.f};
  const v4u SV = {SENT, SENT, SENT, SENT};
  const unsigned orec = WS_OREC + (g * 24 + i) * OREC_SZ;

  if (__builtin_expect(w >= 12, 1)) {
    const unsigned tg = (unsigned)load_tag(ws);
    asm volatile("s_waitcnt vmcnt(0)" : "+v"(wsrc[0]), "+v"(wsrc[1]), "+v"(wsrc[2]), "+v"(wsrc[3]), "+v"(wsrc[4]), "+v"(wsrc[5]), "+v"(wsrc[6]), "+v"(wsrc[7]), "+v"(xa), "+v"(xr) :: "memory");
    {
      v4u bt[4];
      tr4(wsrc, bt);
      if ((lane & 3) == 1) xa = v4u{ONE2, ONE2, ONE2, ONE2};
      f32x4 qp, cs;
#pragma unroll
      for (int e = 0; e < 4; ++e) { const f32x4 d = mfma(xa, bt[e], Z4); qp[e] = d[0]; cs[e] = d[1]; }
      *(LAS f32x4*)(L + L_QRED + cw * 1024 + lane * 16) = qp;
      if (lane < 16) *(LAS f32x4*)(L + L_QRED + 4096 + cw * 256 + lane * 16) = cs;
    }
    lds_set(FL + F_QR + cw, tg);
    f32x2 xst = ln_stats(xr);
    PINV(xst);
    STAMP(2);
    lds_wait(FL + F_QR + (lane & 3), tg);
    {
      float a[4], cs[4];
#pragma unroll
      for (int j = 0; j < 4; ++j) { a[j] = *(const LAS float*)(L + L_QRED + j * 1024 + cw * 256 + lane * 4); cs[j] = *(const LAS float*)(L + L_QRED + 4096 + j * 256 + lane * 4); }
      const float q = xst.y * (((a[0] + a[1]) + (a[2] + a[3])) - xst.x * ((cs[0] + cs[1]) + (cs[2] + cs[3])));
      const unsigned q2 = pk(q, dpp<XOR1>(q));
      if ((lane & 1) == 0) ST64((v2u{q2, tg}), WS_QG + (((g * NB + cw) * 4 + rq) * 256 + 32 * cq) * 8 + lane * 4);
    }
    lds_set(FL + F_QPUB + cw, tg);
    STAMP(3);
    if (i < 24) {
      until(rt(), QDELTA);
      const unsigned qo = WS_QG + ((g * NB + b) * 4 * 256 + cw * 64 + lane) * 8;
      v2u qg[4];
      GSPIN {
        CBAR();
#pragma unroll
        for (int j = 0; j < 4; ++j) qg[j] = LD64(qo, j * 2048);
        SCHED();
        lds_set(FL + F_QIS + cw, tg);
        SCHED();
        if (__all((qg[0].y == tg) & (qg[1].y == tg) & (qg[2].y == tg) & (qg[3].y == tg))) break;
        RETRY();
      }
      const float q0 = (blo(qg[0].x) + blo(qg[1].x)) + (blo(qg[2].x) + blo(qg[3].x)), q1 = (bhi(qg[0].x) + bhi(qg[1].x)) + (bhi(qg[2].x) + bhi(qg[3].x));
      *(LAS unsigned*)(L + L_QS + (cw * 64 + lane) * 4) = pk(q0, q1);
      lds_set(FL + F_QS + cw, tg);
      STAMP(4);
      PDUMP();
      return;
    }
    __builtin_amdgcn_s_setprio(0);
  } else if (i < 24) {
    if (w < ntile) {
      const unsigned tg = (unsigned)load_tag(ws);
      const int nval = w < 10 ? 8 : s < 4 ? 6 : 4;
      const unsigned oob6 = nval < 6 ? 0x80000000u : 0u, oob8 = nval < 8 ? 0x80000000u : 0u;
      static_assert(KPRE == 4, "");
      if (KMID >= 1) { asm volatile("s_waitcnt vmcnt(%0)" :: "i"(KLEFT) : "memory"); ldtile<4, 6>(kf, (r16 * 64 + gq * 16) | oob6, rk, kvbase, kvbase2); }
      if (KMID >= 2) { asm volatile("s_waitcnt vmcnt(%0)" :: "i"(KLEFT) : "memory"); ldtile<6, 8>(kf, (r16 * 64 + gq * 16) | oob8, rk, kvbase, kvbase2); }
      lds_wait(FL + F_QPUB + (lane & 3), tg);
      if (KMID < 1) ldtile<4, 6>(kf, (r16 * 64 + gq * 16) | oob6, rk, kvbase, kvbase2);
      if (KMID < 2) ldtile<6, 8>(kf, (r16 * 64 + gq * 16) | oob8, rk, kvbase, kvbase2);
      if (VPRE) ldtile<0, VPRE>(vf, lane * 16, rv, kvbase, kvbase2);
      __builtin_amdgcn_s_setprio(0);
      lds_wait(FL + F_QIS + (lane & 3), tg);
      ldtile<VPRE, 4>(vf, lane * 16, rv, kvbase, kvbase2);
      ldtile<4, 6>(vf, (lane * 16) | oob6, rv, kvbase, kvbase2);
      ldtile<6, 8>(vf, (lane * 16) | oob8, rv, kvbase, kvbase2);
      STAMP(2);
      lds_wait(FL + (lane < 4 ? F_QS + lane : F_CL), tg);
      asm volatile("s_waitcnt vmcnt(8)" : "+v"(kf[0]), "+v"(kf[1]), "+v"(kf[2]), "+v"(kf[3]), "+v"(kf[4]), "+v"(kf[5]), "+v"(kf[6]), "+v"(kf[7]) :: "memory");
      STAMP(3);
      const v4u qf = *(const LAS v4u*)(L + L_QS + r16 * 64 + gq * 16);
      float sc[8];
#pragma unroll
      for (int t = 0; t < 8; ++t) {
        const f32x4 d = mfma(kf[t], qf, Z4);
        sc[t] = (dpp<0x00>(d[0]) + dpp<0x55>(d[1])) + (dpp<0xAA>(d[2]) + dpp<0xFF>(d[3]));
        if (t >= 4 && t >= nval) sc[t] = -__builtin_inff();
      }
      float m = fmaxf(fmaxf(fmaxf(sc[0], sc[1]), fmaxf(sc[2], sc[3])), fmaxf(fmaxf(sc[4], sc[5]), fmaxf(sc[6], sc[7])));
      m = x32_max(x16_max(m));
      constexpr float C2 = 0.08838834764831845f * 1.4426950408889634f;
      const float ms = m * C2;
      float p[8], l = 0.f;
#pragma unroll
      for (int t = 0; t < 8; ++t) { p[t] = __builtin_amdgcn_exp2f(fmaf(sc[t], C2, -ms)); l += p[t]; }
      l = x32_sum(x16_sum(l));
      if ((lane & 0x33) == 0) *(LAS f32x2*)(L + L_MW + ((lane >> 2) * 12 + w) * 8) = f32x2{ms, l};
      lds_set(FL + F_MW + w, tg);
      v4u pf = {pk(p[0], p[1]), pk(p[2], p[3]), pk(p[4], p[5]), pk(p[6], p[7])};
      unsigned pa = L_PART + w * 2048 + gq * 512 + r16 * 32;
      PINV(pf); PINV(pa);
      STAMP(4);
      v4u vt[8];
#pragma unroll
      for (int mm = 0; mm < 4; ++mm) {
        if (mm == 0) VMWAIT2(6, vf[0], vf[1]);
        if (mm == 1) VMWAIT2(4, vf[2], vf[3]);
        if (mm == 2) { lds_set(FL + F_VL + w, tg); VMWAIT2(2, vf[4], vf[5]); }
        if (mm == 3) { VMWAIT2(0, vf[6], vf[7]); STAMP(5); }
#pragma unroll
        for (int d = 0; d < 4; ++d) { vt[2 * d][mm] = plo(vf[2 * mm][d], vf[2 * mm + 1][d]); vt[2 * d + 1][mm] = phi(vf[2 * mm][d], vf[2 * mm + 1][d]); }
        if (mm < 3) {
#pragma unroll
          for (int e = 0; e < 8; ++e) PINV(vt[e]);
        }
        SCHED();
      }
      float o[8];
#pragma unroll
      for (int e = 0; e < 8; ++e) o[e] = mfma(pf, vt[e], Z4)[0];
      *(LAS f32x4*)(L + pa) = f32x4{o[0], o[1], o[2], o[3]};
      *(LAS f32x4*)(L + pa + 16) = f32x4{o[4], o[5], o[6], o[7]};
      lds_set(FL + F_OPF + w, tg);
      STAMP(6);
      if (w < 4) {
        const int jl = r16 < ntile ? r16 : 0;
        lds_wait(FL + F_MW + jl, tg);
        const f32x2 ml = *(const LAS f32x2*)(L + L_MW + (w * 12 + jl) * 8);
        const float mg = row_max(ml.x);
        const float wt = r16 < ntile ? __builtin_amdgcn_exp2f(ml.x - mg) : 0.f;
        const float lt = row_sum(wt * ml.y);
        float wj[11];
#pragma unroll
        for (int j = 0; j < 11; ++j) { wj[j] = __builtin_bit_cast(float, __builtin_amdgcn_readlane(__builtin_bit_cast(int, wt), j)); PINS(wj[j]); }
        if (lane == 0) ST64((v2u{nosent(__float_as_uint(mg)), nosent(__float_as_uint(lt))}), orec + 1024 + w * 8);
        unsigned ra = L_PART + w * 512 + lane * 8, so = orec + w * 256 + lane * 4;
        PINV(ra); PINV(so);
        lds_wait(FL + F_OPF + jl, tg);
        f32x2 pv[11];
#pragma unroll
        for (int j = 0; j < 11; ++j) pv[j] = *(const LAS f32x2*)(L + ra + j * 2048);
        SCHED();
        f32x2 a = pv[0] * wj[0], a2 = pv[1] * wj[1];
#pragma unroll
        for (int j = 2; j < 10; j += 2) { a += pv[j] * wj[j]; a2 += pv[j + 1] * wj[j + 1]; }
        a += pv[10] * wj[10];
        a += a2;
        const unsigned v = nosent(pk(a.x, a.y));
        const v4u o4 = {dppu<0x00>(v), dppu<0x55>(v), dppu<0xAA>(v), dppu<0xFF>(v)};
        if ((lane & 3) == 0) ST128(o4, so);
        STAMP(7);
      }
    } else if (w == 11) {
      __builtin_amdgcn_s_setprio(3);
      ST128(SV, orec + lane * 16);
      if (lane < 8) ST128(SV, orec + 1024 + lane * 16);
      const u64 tag = load_tag(ws);
      const unsigned taghi = (unsigned)(tag >> 16), taglo = (unsigned)(tag << 16), tg = (unsigned)tag;
      const bool isR = i < 8;
      const int ln = g * 8 + i, rb = ln >> 4, pg = lane >> 3, q4 = lane & 7, col = 32 * (ln & 15) + 4 * q4;
      v2u xv = {0u, 0u};
      if (isR) xv = __builtin_amdgcn_raw_buffer_load_b64(rx, (rb * DM + col) * 2, 0, 0);
      DRAIN();
      lds_set(FL + F_CL, tg);
      STAMP(3);
      lds_wait(FL + F_VL + (lane < ntile ? lane : 0), tg);
      if (lane == 0) ST64(hint(taglo, taghi), WS_HA + (g * 32 + i) * 128);
      STAMPE(4);
      if (isR) {
        const rsrc_t ro = mk(out, NB * DM * 2u);
        unsigned pf = __builtin_amdgcn_raw_buffer_load_b32(ro, 0, 0, AUX_SC1);
        asm volatile("" :: "v"(pf));
        v2u hb;
        GSPIN {
          CBAR();
          hb = LD64(WS_HB + ((lane & 7) * 4 + ((ln >> 2) & 3)) * 128, 0);
          if (__all(hint_ok(hb, taglo, taghi))) break;
          __builtin_amdgcn_s_sleep(2);
        }
        STAMP(5);
        wait_age(hb.x, DLY3 + 20);
        v2u hv;
        GSPIN {
          CBAR();
          hv = LD64(WS_HP + (pg * NB + rb) * 1024 + col * 2, 0);
          if (__all(max(hv.x, hv.y) != SENT)) break;
        }
        STAMP(6);
        float h[4] = {blo(hv.x), bhi(hv.x), blo(hv.y), bhi(hv.y)};
#pragma unroll
        for (int d = 0; d < 4; ++d) { h[d] += dpp<ROR8>(h[d]); h[d] = x32_sum(x16_sum(h[d])); }
        {
          const unsigned a0 = pk(h[0], h[1]), a1 = pk(h[2], h[3]);
          const unsigned h0 = pk(blo(xv.x) + blo(a0), bhi(xv.x) + bhi(a0)), h1 = pk(blo(xv.y) + blo(a1), bhi(xv.y) + bhi(a1));
          h[0] = blo(h0); h[1] = bhi(h0); h[2] = blo(h1); h[3] = bhi(h1);
        }
        unsigned po = WS_OP + pg * 8192 + ln * 128 + q4 * 16, po3 = pg < 4 ? po + 24 * 8192 : po, oo = (rb * DM + col) * 2;
        PINV(po); PINV(po3); PINV(oo); PINV(h[0]); PINV(h[1]); PINV(h[2]); PINV(h[3]);
        v2u hc;
        GSPIN {
          CBAR();
          hc = LD64(WS_HC + (lane < NDCU ? lane : 0) * 128, 0);
          if (__all(hint_ok(hc, taglo, taghi))) break;
        }
        wait_age(hc.x, DLY5);
        STAMP(7);
        v4u pv[4];
        GSPIN {
          CBAR();
#pragma unroll
          for (int t = 0; t < 3; ++t) pv[t] = LD128(po, t * 8 * 8192);
          pv[3] = LD128(po3, 0);
          if (__all(okv(pv[0]) & okv(pv[1]) & okv(pv[2]) & okv(pv[3]))) break;
          RETRY();
        }
        STAMP(8);
        float y[4];
#pragma unroll
        for (int d = 0; d < 4; ++d) {
          float t = (__uint_as_float(pv[0][d]) + __uint_as_float(pv[1][d])) + (__uint_as_float(pv[2][d]) + (pg < 4 ? __uint_as_float(pv[3][d]) : 0.f));
          t += dpp<ROR8>(t);
          y[d] = x32_sum(x16_sum(t));
        }
        const unsigned y0 = pk(y[0], y[1]), y1 = pk(y[2], y[3]);
        if (lane < 8) __builtin_amdgcn_raw_buffer_store_b64(v2u{pk(h[0] + blo(y0), h[1] + bhi(y0)), pk(h[2] + blo(y1), h[3] + bhi(y1))}, ro, oo, 0, AUX_SC1);
        if (ln == 0 && lane == 0) ST64((v2u{(unsigned)tag, (unsigned)(tag >> 32)}), WS_EPOCH);
        STAMPE(9);
      }
    }
    PDUMP();
    return;
  }

  const int jq = i - 24;
  const bool isC = jq < 4;
  const int dI = g + 8 * (jq - 4);
  if (!isC && dI >= NDCU) { PDUMP(); return; }
  const u64 tag = load_tag(ws);
  const unsigned taghi = (unsigned)(tag >> 16), taglo = (unsigned)(tag << 16), tg = (unsigned)tag;
#define GATHER_H() do { \
    unsigned klo = 0x00003F80u, khi = 0x3F800000u, ho = WS_HP + w * 256 + lane * 4, oa = L_OA + w * 256 + lane * 4; \
    unsigned hp[8] = {0u, 0u, 0u, 0u, 0u, 0u, 0u, 0u}; \
    float x0 = blo(xp), x1 = bhi(xp); \
    PINS(klo); PINS(khi); PINV(oa); PINV(x0); PINV(x1); \
    ask8(hp, (unsigned)(size_t)(FL + F_GO + (lane & 7)), tg, ho, mkd(ws, WS_END)); \
    if (!__all(max(max(max(hp[0], hp[1]), max(hp[2], hp[3])), max(max(hp[4], hp[5]), max(hp[6], hp[7]))) != SENT)) { \
      GSPIN { \
        RETRY(); \
        CBAR(); \
        _Pragma("unroll") for (int j = 0; j < 8; ++j) hp[j] = LD32(ho, j * 4096); \
        if (__all(max(max(max(hp[0], hp[1]), max(hp[2], hp[3])), max(max(hp[4], hp[5]), max(hp[6], hp[7]))) != SENT)) break; \
      } \
      DRAIN(); \
    } \
    STAMP(7); \
    float a0 = 0.f, a1 = 0.f, a2 = 0.f, a3 = 0.f; \
    _Pragma("unroll") for (int j = 0; j < 4; ++j) { a0 = dot2(hp[j], klo, a0); a1 = dot2(hp[j], khi, a1); a2 = dot2(hp[4 + j], klo, a2); a3 = dot2(hp[4 + j], khi, a3); } \
    const unsigned t0 = pk(a0 + a2, a1 + a3); \
    *(LAS unsigned*)(L + oa) = pk(x0 + blo(t0), x1 + bhi(t0)); \
  } while (0)
  unsigned xp = 0;
  if (!isC) xp = __builtin_amdgcn_raw_buffer_load_b32(rx, w * 256 + lane * 4, 0, 0);
  if (w < 8) {
    const unsigned stride = isC ? DM * 2 : FF * 2;
    const GAS unsigned char* p = isC ? (const GAS unsigned char*)wo + (size_t)(512 * g) * (DM * 2) + (128 * jq + 8 * r16) * 2
                                     : (const GAS unsigned char*)(r16 < 8 ? wg : wu) + (64 * dI + 8 * (r16 & 7)) * 2;
    p += (size_t)(64 * w + 8 * gq) * stride;
    unsigned aa = L_OA + (r16 >> 2) * 1024 + w * 128 + gq * 16, pa = L_PART + w * 2048 + gq * 512 + r16 * 32;
    PINV(aa); PINV(pa);
    v4u src[16], bt[16];
    until(tstart, isC ? TC0 : TD0);
#pragma unroll
    for (int j = 0; j < WPRE; ++j) src[j] = __builtin_nontemporal_load((const GAS v4u*)(p + ((j >> 3) * 32 + (j & 7)) * stride));
    lds_wait(FL + F_QPUB + (lane & 3), tg);
#pragma unroll
    for (int j = WPRE; j < 16; ++j) src[j] = __builtin_nontemporal_load((const GAS v4u*)(p + ((j >> 3) * 32 + (j & 7)) * stride));
    SCHED();
    STAMP(4);
    tr8(src, bt);
    tr8(src + 8, bt + 8);
    if (!isC) {
      const v4u ones = {ONE2, ONE2, ONE2, ONE2};
      float cs[8];
#pragma unroll
      for (int e = 0; e < 8; ++e) cs[e] = mfma(ones, bt[8 + e], mfma(ones, bt[e], Z4))[0];
      if (lane < 16) {
        *(LAS f32x4*)(L + L_SS + w * 512 + lane * 32) = f32x4{cs[0], cs[1], cs[2], cs[3]};
        *(LAS f32x4*)(L + L_SS + w * 512 + lane * 32 + 16) = f32x4{cs[4], cs[5], cs[6], cs[7]};
      }
      lds_set(FL + F_SR + w, tg);
    }
    DRAIN();
    STAMP(5);
    if (w == 0) {
      if (isC) {
        const int hl = lane < 24 ? lane : 0;
        watch(rws, WS_HA + (g * 32 + hl) * 128, taglo, taghi, (LAS v2u*)(L + L_HINT + hl * 8), tg);
      } else {
        watch8(rws, WS_HB + (lane & 31) * 128, taglo, taghi, DLY3, FL + F_GO, tg, lane);
      }
      STAMP(10);
      DRAIN();
    } else if (w == 1 && isC) {
      lds_wait(FL + F_SR + (lane & 7), tg);
      if (lane == 0) ST64(hint(taglo, taghi), WS_HB + (g * 4 + jq) * 128);
    }
    if (!isC) GATHER_H();
    WGBAR();
    STAMP(8);
    {
      const v4u a0 = *(const LAS v4u*)(L + aa), a1 = *(const LAS v4u*)(L + aa + 64);
#pragma unroll
      for (int h = 0; h < 2; ++h) {
        f32x4 acc[4];
#pragma unroll
        for (int e = 0; e < 4; ++e) acc[e] = mfma(a0, bt[4 * h + e], Z4);
#pragma unroll
        for (int e = 0; e < 4; ++e) acc[e] = mfma(a1, bt[8 + 4 * h + e], acc[e]);
        *(LAS f32x4*)(L + pa + 16 * h) = f32x4{acc[0][0], acc[1][0], acc[2][0], acc[3][0]};
      }
    }
    STAMP(9);
    WGBAR();
    if (!isC) WGBAR();
  } else if (isC) {
    const int v = w & 7, bb = v >> 1, head = 2 * (v & 1) + (lane >> 5), sg = gq & 1;
    unsigned ra = L_PART + (w & 3) * 512 + lane * 8, so = WS_HP + (g * NB + (w & 3)) * 1024 + jq * 256 + lane * 4;
    unsigned rd = WS_OREC + (g * 24 + bb * NSPL + sg) * OREC_SZ + head * 256 + r16 * 16, rs = WS_OREC + (g * 24 + bb * NSPL + sg) * OREC_SZ + 1024 + head * 8;
    unsigned oa = L_OA + bb * 1024 + head * 256 + r16 * 16 + sg * 8;
    PINV(ra); PINV(so); PINV(rd); PINV(rs); PINV(oa);
    if (w == 15) ST128(SV, WS_HP + (g * NB + gq) * 1024 + jq * 256 + r16 * 16);
    DRAIN();
    STAMP(5);
    {
      v2u e;
#pragma nounroll
      for (unsigned sp = 0; sp < SPIN_L; ++sp) { e = *(volatile LAS v2u*)(L + L_HINT + (bb * NSPL + (lane < NSPL ? lane : 0)) * 8); if (__all(e.x == tg)) break; }
      wait_age(e.y, DLY2);
    }
    STAMP(6);
    GSPIN {
      v4u od[3];
      v2u st[3];
      CBAR();
#pragma unroll
      for (int u = 0; u < 3; ++u) st[u] = LD64(rs, 2 * u * OREC_SZ);
      SCHED();
#pragma unroll
      for (int u = 0; u < 3; ++u) od[u] = LD128(rd, 2 * u * OREC_SZ);
      SCHED();
      lds_set(FL + F_SR + v, tg);
      SCHED();
      unsigned bad = max(max(max(st[0].x, st[0].y), max(st[1].x, st[1].y)), max(st[2].x, st[2].y));
      const float m0 = __uint_as_float(st[0].x), m1 = __uint_as_float(st[1].x), m2 = __uint_as_float(st[2].x);
      const float mg = x16_max(fmaxf(fmaxf(m0, m1), m2));
      float wt[3] = {__builtin_amdgcn_exp2f(m0 - mg), __builtin_amdgcn_exp2f(m1 - mg), __builtin_amdgcn_exp2f(m2 - mg)};
      const float inv = __builtin_amdgcn_rcpf(x16_sum(fmaf(wt[0], __uint_as_float(st[0].y), fmaf(wt[1], __uint_as_float(st[1].y), wt[2] * __uint_as_float(st[2].y)))));
#pragma unroll
      for (int u = 0; u < 3; ++u) { wt[u] *= inv; PINV(wt[u]); }
      PINV(bad);
      SCHED();
      f32x2 a[4];
#pragma unroll
      for (int u = 0; u < 3; ++u) {
        f32x2 t[4];
#pragma unroll
        for (int d = 0; d < 4; ++d) { t[d] = f32x2{blo(od[u][d]), bhi(od[u][d])}; PINV(t[d]); }
        bad = max(bad, max(max(od[u].x, od[u].y), max(od[u].z, od[u].w)));
#pragma unroll
        for (int d = 0; d < 4; ++d) a[d] = u == 0 ? t[d] * wt[0] : t[d] * wt[u] + a[d];
      }
      float r[4];
#pragma unroll
      for (int d = 0; d < 4; ++d) {
        const auto sw = __builtin_amdgcn_permlane16_swap(__float_as_uint(a[d >> 1][d & 1]), __float_as_uint(a[2 + (d >> 1)][d & 1]), false, false);
        r[d] = __uint_as_float(sw[0]) + __uint_as_float(sw[1]);
      }
      *(LAS v2u*)(L + oa) = v2u{pk(r[0], r[1]), pk(r[2], r[3])};
      STAMP(7);
      if (__all(bad != SENT)) break;
      RETRY();
    }
    DRAIN();
    STAMP(8);
    WGBAR();
    WGBAR();
    if (w >= 12) {
      f32x2 pv[8];
#pragma unroll
      for (int j = 0; j < 8; ++j) pv[j] = *(const LAS f32x2*)(L + ra + j * 2048);
      SCHED();
      const f32x2 t = ((pv[0] + pv[1]) + (pv[2] + pv[3])) + ((pv[4] + pv[5]) + (pv[6] + pv[7]));
      const unsigned vv = nosent(pk(t.x, t.y));
      const v4u o4 = {dppu<0x00>(vv), dppu<0x55>(vv), dppu<0xAA>(vv), dppu<0xFF>(vv)};
      if ((lane & 3) == 0) ST128(o4, so);
      STAMPE(9);
    }
  } else {
    unsigned ra = L_PART + (w & 3) * 512 + lane * 4, hw = L_HH + (w & 3) * 128 + lane * 2, ha = L_HH + (r16 >> 2) * 128 + gq * 16;
    unsigned so = WS_OP + dI * 8192 + gq * 2048 + (w & 7) * 256 + r16 * 16, sa = L_OA + (w & 3) * 1024 + lane * 16;
    PINV(ra); PINV(hw); PINV(ha); PINV(so); PINV(sa);
    ST128(SV, WS_OP + dI * 8192 + (w - 8) * 1024 + lane * 16);
    until(tstart, TWD);
    const rsrc_t rw = mk(wd, FF * DM * 2u);
    v2u src[16];
    v4u bt[8];
#pragma unroll
    for (int j = 0; j < 16; ++j) src[j] = __builtin_amdgcn_raw_buffer_load_b64(rw, (unsigned)(8 * gq + (j & 3)) * 1024 + (w - 8) * 128 + r16 * 8, (64 * dI + (j >> 3) * 32 + (j & 4)) * 1024, AUX_NT);
    SCHED();
    tr4(src, bt);
    tr4(src + 8, bt + 4);
    float sgs = 0.f, sus = 0.f;
    if (w >= 12) {
      lds_wait(FL + F_SR + (lane & 7), tg);
#pragma unroll
      for (int j = 0; j < 8; ++j) { sgs += *(const LAS float*)(L + L_SS + j * 512 + lane * 4); sus += *(const LAS float*)(L + L_SS + j * 512 + 256 + lane * 4); }
    }
    PINV(sgs); PINV(sus);
    DRAIN();
    STAMP(5);
    GATHER_H();
    WGBAR();
    if (w >= 12) {
      const f32x2 st = ln_stats(*(const LAS v4u*)(L + sa));
      float kg = st.y * -1.4426950408889634f, cg = st.x * sgs, cu = st.x * sus, ku = st.y;
      PINV(kg); PINV(cg); PINV(cu); PINV(ku);
      WGBAR();
      float gp[8], up[8];
#pragma unroll
      for (int j = 0; j < 8; ++j) { gp[j] = *(const LAS float*)(L + ra + j * 2048); up[j] = *(const LAS float*)(L + ra + j * 2048 + 256); }
      SCHED();
      const float gg = ((gp[0] + gp[1]) + (gp[2] + gp[3])) + ((gp[4] + gp[5]) + (gp[6] + gp[7]));
      const float uu = ((up[0] + up[1]) + (up[2] + up[3])) + ((up[4] + up[5]) + (up[6] + up[7]));
      const float hh = ku * (uu - cu) * __builtin_amdgcn_rcpf(1.f + __builtin_amdgcn_exp2f(kg * (gg - cg)));
      *(LAS u16*)(L + hw) = (u16)pk(hh, hh);
    } else {
      if (w == 8 && lane == 0) ST64(hint(taglo, taghi), WS_HC + dI * 128);
      WGBAR();
    }
    WGBAR();
    STAMP(8);
    const v4u a0 = *(const LAS v4u*)(L + ha), a1 = *(const LAS v4u*)(L + ha + 64);
    f32x4 acc[4];
#pragma unroll
    for (int e = 0; e < 4; ++e) acc[e] = mfma(a0, bt[e], Z4);
#pragma unroll
    for (int e = 0; e < 4; ++e) acc[e] = mfma(a1, bt[4 + e], acc[e]);
    ST128((v4u{nosent(__float_as_uint(acc[0][0])), nosent(__float_as_uint(acc[1][0])), nosent(__float_as_uint(acc[2][0])), nosent(__float_as_uint(acc[3][0]))}), so);
    STAMPE(9);
  }
  PDUMP();
}
}

extern "C" void kernel_launch(void* const* d_in, const int* in_sizes, int n_in, void* d_out, int out_size, void* d_ws, size_t ws_size, hipStream_t stream) {
  (void)in_sizes; (void)n_in; (void)out_size; (void)ws_size;
  fused_decode<<<dim3(256), dim3(1024), 0, stream>>>((const u16*)d_in[0], (const u16*)d_in[1], (const u16*)d_in[2], (const u16*)d_in[3], (const u16*)d_in[4],
                                                     (const u16*)d_in[6], (const u16*)d_in[7], (const u16*)d_in[8], (u16*)d_out, (unsigned char*)d_ws);
}
```

```cpp
#include <hip/hip_runtime.h>
#include <stdint.h>

namespace {
typedef unsigned short u16;
typedef unsigned long long u64;
typedef unsigned v4u __attribute__((ext_vector_type(4)));
typedef unsigned v2u __attribute__((ext_vector_type(2)));
typedef int v4i __attribute__((ext_vector_type(4)));
typedef float f32x4 __attribute__((ext_vector_type(4)));
typedef float f32x2 __attribute__((ext_vector_type(2)));
typedef short bf16x8 __attribute__((ext_vector_type(8)));
typedef __bf16 bf16x2 __attribute__((ext_vector_type(2)));
typedef __bf16 bf16v8 __attribute__((ext_vector_type(8)));
typedef __bf16 bf16v16 __attribute__((ext_vector_type(16)));
typedef unsigned v8u __attribute__((ext_vector_type(8)));
#define GAS __attribute__((address_space(1)))
#define LAS __attribute__((address_space(3)))
typedef __amdgpu_buffer_rsrc_t rsrc_t;

constexpr int NB = 4, DM = 512, HKV = 8, SK = 8192, WIN = 2048, FF = 1792, NQ = 4096;
constexpr int NSPL = 6, NDCU = 28;
constexpr unsigned TK0 = 10;
constexpr unsigned QDELTA = 70;
constexpr unsigned TC0 = 240;
constexpr unsigned TD0 = 100;
constexpr unsigned TWD = 450;
constexpr unsigned DLY2 = 60;
constexpr unsigned DLY3 = 86;
constexpr unsigned DLY5 = 78;
constexpr int WPRE = 3;
constexpr int AUX_NT = 2, AUX_SC1 = 16;
constexpr unsigned SENT = 0xFFFFFFFFu, ONE2 = 0x3F803F80u;
constexpr int WS_EPOCH = 0, WS_HB = 4096, WS_HC = WS_HB + 8 * 4096, WS_HA = WS_HC + 8 * 4096,
              WS_QG = WS_HA + 8 * 32 * 128;
constexpr int WS_OREC = WS_QG + 8 * 4 * 4 * 256 * 8, OREC_SZ = 1152;
constexpr int WS_HP = WS_OREC + 8 * 24 * OREC_SZ, WS_OP = WS_HP + 8 * 4 * 512 * 2, WS_END = WS_OP + NDCU * 8192;
constexpr int L_FLAG = 0, L_MW = 512, L_QS = 1024, L_OA = L_QS + 1024, L_HH = L_OA + 4096, L_SS = L_HH + 512;
constexpr int L_QRED = L_SS + 4096, L_PART = L_QRED + 5120, L_USED = L_PART + 11 * 2048;
constexpr int L_TOTAL = 84 * 1024;
static_assert(L_USED + 512 <= L_TOTAL, "lds");
enum { F_QIS = 0, F_QS = 4, F_QPUB = 12, F_QR = 16, F_CL = 24, F_GO = 32, F_SR = 72, F_MW = 80, F_VL = 96, F_OPF = 112 };
constexpr int CODE_BYTES = 15872;
constexpr unsigned SPIN_G = 1u << 17, SPIN_L = 1u << 21;

__device__ __forceinline__ u64 rt() { return __builtin_amdgcn_s_memrealtime(); }
__device__ __forceinline__ rsrc_t mk(const void* p, unsigned bytes) { return __builtin_amdgcn_make_buffer_rsrc((void*)p, 0, bytes, 0x00020000); }
__device__ __forceinline__ f32x4 mfma(v4u a, v4u b, f32x4 c) { return __builtin_amdgcn_mfma_f32_16x16x32_bf16(__builtin_bit_cast(bf16x8, a), __builtin_bit_cast(bf16x8, b), c, 0, 0, 0); }
__device__ __forceinline__ v8u cat(v4u b0, v4u b1) { return __builtin_shufflevector(b0, b1, 0, 1, 2, 3, 4, 5, 6, 7); }
__device__ __forceinline__ f32x4 mfma64(v4u a, v8u b, f32x4 c, int idx) {
  return __builtin_amdgcn_smfmac_f32_16x16x64_bf16(__builtin_bit_cast(bf16v8, a), __builtin_bit_cast(bf16v16, b), c, idx, 0, 0);
}
__device__ __forceinline__ unsigned apos(unsigned d) { return (d & ~7u) | ((d & 1u) << 2) | ((d >> 1) & 3u); }
__device__ __forceinline__ unsigned pk(float lo, float hi) { return __builtin_bit_cast(unsigned, __builtin_convertvector(f32x2{lo, hi}, bf16x2)); }
__device__ __forceinline__ float blo(unsigned v) { return __builtin_bit_cast(float, v << 16); }
__device__ __forceinline__ float bhi(unsigned v) { return __builtin_bit_cast(float, v & 0xffff0000u); }
__device__ __forceinline__ unsigned plo(unsigned a, unsigned b) { return __builtin_amdgcn_perm(b, a, 0x05040100u); }
__device__ __forceinline__ unsigned phi(unsigned a, unsigned b) { return __builtin_amdgcn_perm(b, a, 0x07060302u); }
__device__ __forceinline__ unsigned nosent(unsigned v) { return v < 0xFFFFFFFEu ? v : 0xFFFFFFFEu; }
template <int C> __device__ __forceinline__ float dpp(float x) { return __builtin_bit_cast(float, __builtin_amdgcn_mov_dpp(__builtin_bit_cast(int, x), C, 0xf, 0xf, true)); }
template <int C> __device__ __forceinline__ unsigned dppu(unsigned x) { return (unsigned)__builtin_amdgcn_mov_dpp((int)x, C, 0xf, 0xf, true); }
constexpr int XOR1 = 0xB1, XOR2 = 0x4E, ROR4 = 0x124, ROR8 = 0x128;
__device__ __forceinline__ float x16_sum(float x) { auto s = __builtin_amdgcn_permlane16_swap(__float_as_uint(x), __float_as_uint(x), false, false); return __uint_as_float(s[0]) + __uint_as_float(s[1]); }
__device__ __forceinline__ float x32_sum(float x) { auto s = __builtin_amdgcn_permlane32_swap(__float_as_uint(x), __float_as_uint(x), false, false); return __uint_as_float(s[0]) + __uint_as_float(s[1]); }
__device__ __forceinline__ float x16_max(float x) { auto s = __builtin_amdgcn_permlane16_swap(__float_as_uint(x), __float_as_uint(x), false, false); return fmaxf(__uint_as_float(s[0]), __uint_as_float(s[1])); }
__device__ __forceinline__ float x32_max(float x) { auto s = __builtin_amdgcn_permlane32_swap(__float_as_uint(x), __float_as_uint(x), false, false); return fmaxf(__uint_as_float(s[0]), __uint_as_float(s[1])); }
__device__ __forceinline__ float row_sum(float x) { x += dpp<XOR1>(x); x += dpp<XOR2>(x); x += dpp<ROR4>(x); x += dpp<ROR8>(x); return x; }
__device__ __forceinline__ float row_max(float x) { x = fmaxf(x, dpp<XOR1>(x)); x = fmaxf(x, dpp<XOR2>(x)); x = fmaxf(x, dpp<ROR4>(x)); x = fmaxf(x, dpp<ROR8>(x)); return x; }
__device__ __forceinline__ float dot2(unsigned a, unsigned b, float c) { return __builtin_amdgcn_fdot2_f32_bf16(__builtin_bit_cast(bf16x2, a), __builtin_bit_cast(bf16x2, b), c, false); }
#define CBAR() asm volatile("" ::: "memory")
#define SCHED() __builtin_amdgcn_sched_barrier(0)
#define DRAIN() do { asm volatile("s_waitcnt vmcnt(0)" ::: "memory"); __builtin_amdgcn_s_waitcnt(0x0F70); } while (0)
#define PINV(x) asm volatile("" : "+v"(x))
#define PINS(x) asm volatile("" : "+s"(x))
#define WGBAR() do { SCHED(); asm volatile("s_waitcnt lgkmcnt(0)" ::: "memory"); __builtin_amdgcn_s_barrier(); CBAR(); SCHED(); } while (0)
#define GSPIN _Pragma("nounroll") for (unsigned sp = 0; sp < SPIN_G; ++sp)
#define LD32(o, so) __builtin_amdgcn_raw_buffer_load_b32(rws, (o), (so), AUX_SC1)
#define LD64(o, so) __builtin_amdgcn_raw_buffer_load_b64(rws, (o), (so), AUX_SC1)
#define LD128(o, so) __builtin_amdgcn_raw_buffer_load_b128(rws, (o), (so), AUX_SC1)
#define ST64(v, o) __builtin_amdgcn_raw_buffer_store_b64((v), rws, (o), 0, AUX_SC1)
#define ST128(v, o) __builtin_amdgcn_raw_buffer_store_b128((v), rws, (o), 0, AUX_SC1)
__device__ __forceinline__ bool okv(v4u r) { return max(max(r.x, r.y), max(r.z, r.w)) != SENT; }
__device__ __forceinline__ void lds_wait(volatile LAS unsigned* f) {
  SCHED();
#pragma nounroll
  for (unsigned s = 0; s < SPIN_L; ++s) { if (__all(*f != 0u)) break; }
  CBAR();
  SCHED();
}
__device__ __forceinline__ void lds_set(volatile LAS unsigned* f) { CBAR(); *f = 1u; CBAR(); }
__device__ __forceinline__ void until(u64 t0, unsigned ticks) { while (rt() - t0 < ticks) __builtin_amdgcn_s_sleep(1); }
__device__ __forceinline__ v2u hint(unsigned taglo, unsigned taghi) { return v2u{taglo | ((unsigned)rt() & 0xFFFFu), taghi}; }
__device__ __forceinline__ bool hint_ok(v2u h, unsigned taglo, unsigned taghi) { return (h.y == taghi) & ((h.x ^ taglo) < 65536u); }
__device__ __forceinline__ void wait_age(unsigned t16, unsigned dly) {
#pragma nounroll
  for (unsigned s = 0; s < 4096u; ++s) { if (__all((((unsigned)rt() - t16) & 0xFFFFu) >= dly)) break; }
}
__device__ __forceinline__ void watch(rsrc_t rws, unsigned ho, unsigned taglo, unsigned taghi, volatile LAS unsigned* slot) {
  GSPIN {
    CBAR();
    const v2u h = LD64(ho, 0);
    const bool ok = hint_ok(h, taglo, taghi);
    if (ok) *slot = h.x | 0x10000u;
    CBAR();
    if (__all(ok)) break;
  }
}
__device__ __forceinline__ void watch8(rsrc_t rws, unsigned ho, unsigned taglo, unsigned taghi, unsigned dly, volatile LAS unsigned* go, int lane) {
  v2u h;
  unsigned seen = 0, done = 0;
#pragma nounroll
  for (unsigned sp = 0; sp < SPIN_G && done != 0x11111111u; ++sp) {
    if (seen != ~0u) {
      CBAR();
      h = LD64(ho, 0);
      seen = (unsigned)__builtin_amdgcn_ballot_w64(hint_ok(h, taglo, taghi));
    }
    unsigned m = (unsigned)__builtin_amdgcn_ballot_w64((((unsigned)rt() - h.x) & 0xFFFFu) >= dly) & seen;
    m &= m >> 1; m &= m >> 2; m &= 0x11111111u;
    if (m != done) { if ((m >> (4 * (lane & 7))) & 1u) go[lane & 7] = 1u; done = m; }
  }
  CBAR();
}
__device__ __forceinline__ u64 load_tag(const unsigned char* ws) {
  u64 e;
  asm volatile("s_load_dwordx2 %0, %1, 0x0\n\ts_waitcnt lgkmcnt(0)" : "=s"(e) : "s"(ws) : "memory");
  return (e + 1ull) & 0xFFFFFFFFFFFFull;
}
__device__ __forceinline__ void code_fetch_and_barrier(int part) {
  u64 pc0;
  asm volatile("s_getpc_b64 %0" : "=s"(pc0));
  const u64 a0 = (pc0 & ~127ull) + (u64)(part * 512 < CODE_BYTES - 512 ? part * 512 : CODE_BYTES - 512);
  unsigned t0, t1, t2, t3, t4, t5, t6, t7;
  asm volatile("s_load_dword %0, %8, 0x0\n\ts_load_dword %1, %8, 0x40\n\ts_load_dword %2, %8, 0x80\n\ts_load_dword %3, %8, 0xc0\n\t"
               "s_load_dword %4, %8, 0x100\n\ts_load_dword %5, %8, 0x140\n\ts_load_dword %6, %8, 0x180\n\ts_load_dword %7, %8, 0x1c0\n\t"
               "s_barrier\n\ts_waitcnt lgkmcnt(0)"
               : "=&s"(t0), "=&s"(t1), "=&s"(t2), "=&s"(t3), "=&s"(t4), "=&s"(t5), "=&s"(t6), "=&s"(t7) : "s"(a0) : "memory");
}
__device__ __forceinline__ v4i mkd(const void* p, unsigned bytes) { return v4i{(int)(u64)p, (int)((u64)p >> 32) & 0xFFFF, (int)bytes, 0x00020000}; }
template <int OFF> __device__ __forceinline__ void ldnt(v4u& d, unsigned vo, v4i rs, unsigned so) {
  asm volatile("buffer_load_dwordx4 %0, %1, %2, %3 offen offset:%4 nt" : "=v"(d) : "v"(vo), "s"(rs), "s"(so), "i"(OFF) : "memory");
}
template <int LO, int HI> __device__ __forceinline__ void ldtile(v4u* d, unsigned vo, v4i rs, unsigned so, unsigned so2) {
  if constexpr (LO < HI) { ldnt<(LO & 3) * 1024>(d[LO], vo, rs, LO < 4 ? so : so2); ldtile<LO + 1, HI>(d, vo, rs, so, so2); }
}
__device__ __forceinline__ void ldnt2(v2u& d, unsigned vo, v4i rs, unsigned so) {
  asm volatile("buffer_load_dwordx2 %0, %1, %2, %3 offen nt" : "=v"(d) : "v"(vo), "s"(rs), "s"(so) : "memory");
}
__device__ __forceinline__ void ldp4(v4u& d, unsigned vo, v4i rs) {
  asm volatile("buffer_load_dwordx4 %0, %1, %2, 0 offen" : "=v"(d) : "v"(vo), "s"(rs) : "memory");
}
#define VMWAIT2(n, a, b) asm volatile("s_waitcnt vmcnt(%2)" : "+v"(a), "+v"(b) : "i"(n) : "memory")
__device__ __forceinline__ void ask8(unsigned (&hp)[8], unsigned go, unsigned ho, v4i rs) {
  unsigned t, done, n, cnt;
#define ASK1(j, off) "s_bitcmp1_b32 %10, " #j "\n\ts_cbranch_scc0 2" #j "f\n\ts_movk_i32 %11, " #off "\n\tbuffer_load_dword %" #j ", %13, %14, %11 offen sc1\n2" #j ":\n\t"
  asm volatile("s_mov_b32 %9, 0\n\ts_mov_b32 %11, 0x200000\n"
               "1:\n\tds_read_b32 %8, %12\n\ts_sub_u32 %11, %11, 1\n\ts_cbranch_scc1 3f\n\ts_waitcnt lgkmcnt(0)\n\t"
               "v_cmp_ne_u32 vcc, 0, %8\n\ts_andn2_b32 %10, vcc_lo, %9\n\ts_and_b32 %10, %10, 0xff\n\ts_cbranch_scc0 1b\n\t"
               ASK1(0, 0) ASK1(1, 0x1000) ASK1(2, 0x2000) ASK1(3, 0x3000) ASK1(4, 0x4000) ASK1(5, 0x5000) ASK1(6, 0x6000) ASK1(7, 0x7000)
               "s_mov_b32 %11, 0x200000\n\ts_or_b32 %9, %9, %10\n\ts_cmp_lg_u32 %9, 0xff\n\ts_cbranch_scc1 1b\n"
               "3:\n\ts_waitcnt vmcnt(0) lgkmcnt(0)"
               : "+v"(hp[0]), "+v"(hp[1]), "+v"(hp[2]), "+v"(hp[3]), "+v"(hp[4]), "+v"(hp[5]), "+v"(hp[6]), "+v"(hp[7]), "=&v"(t), "=&s"(done), "=&s"(n), "=&s"(cnt)
               : "v"(go), "v"(ho), "s"(rs) : "vcc", "scc", "memory");
#undef ASK1
}
__device__ __forceinline__ f32x2 ln_stats(v4u hv) {
  const float c0 = blo((unsigned)__builtin_amdgcn_readfirstlane(hv[0]));
  float t[8];
#pragma unroll
  for (int d = 0; d < 4; ++d) { t[2 * d] = blo(hv[d]) - c0; t[2 * d + 1] = bhi(hv[d]) - c0; }
  float s1 = ((t[0] + t[1]) + (t[2] + t[3])) + ((t[4] + t[5]) + (t[6] + t[7]));
  float s2 = (fmaf(t[1], t[1], t[0] * t[0]) + fmaf(t[3], t[3], t[2] * t[2])) + (fmaf(t[5], t[5], t[4] * t[4]) + fmaf(t[7], t[7], t[6] * t[6]));
  s1 += dpp<XOR1>(s1); s2 += dpp<XOR1>(s2);
  s1 += dpp<XOR2>(s1); s2 += dpp<XOR2>(s2);
  s1 += dpp<ROR4>(s1); s2 += dpp<ROR4>(s2);
  s1 += dpp<ROR8>(s1); s2 += dpp<ROR8>(s2);
  s1 = x16_sum(s1); s2 = x16_sum(s2);
  s1 = x32_sum(s1); s2 = x32_sum(s2);
  const float d = s1 * (1.f / DM), var = fmaxf(fmaf(-d, d, s2 * (1.f / DM)), 0.f);
  return f32x2{c0 + d, __builtin_amdgcn_rsqf(var + 1e-5f)};
}

__device__ __forceinline__ void tr4(const v2u* s, v4u* o) {
#pragma unroll
  for (int m = 0; m < 4; ++m) {
    o[0][m] = plo(s[2 * m].x, s[2 * m + 1].x); o[1][m] = phi(s[2 * m].x, s[2 * m + 1].x);
    o[2][m] = plo(s[2 * m].y, s[2 * m + 1].y); o[3][m] = phi(s[2 * m].y, s[2 * m + 1].y);
  }
#pragma unroll
  for (int e = 0; e < 4; ++e) PINV(o[e]);
}
__device__ __forceinline__ void tr8(const v4u* s, v4u* o) {
#pragma unroll
  for (int m = 0; m < 4; ++m)
#pragma unroll
    for (int d = 0; d < 4; ++d) { o[2 * d][m] = plo(s[2 * m][d], s[2 * m + 1][d]); o[2 * d + 1][m] = phi(s[2 * m][d], s[2 * m + 1][d]); }
#pragma unroll
  for (int e = 0; e < 8; ++e) PINV(o[e]);
}

__global__ void __launch_bounds__(1024, 4) fused_decode(const u16* __restrict__ x, const u16* __restrict__ kc, const u16* __restrict__ vc,
                                                     const u16* __restrict__ wq, const u16* __restrict__ wo, const u16* __restrict__ wg,
                                                     const u16* __restrict__ wu, const u16* __restrict__ wd, u16* __restrict__ out, unsigned char* ws) {
  const u64 tstart = rt();
  __shared__ __attribute__((aligned(16))) unsigned char smem[L_TOTAL];
  LAS unsigned char* L = (LAS unsigned char*)smem;
  volatile LAS unsigned* FL = (volatile LAS unsigned*)(L + L_FLAG);
  const int lane = threadIdx.x & 63;
  int w = (__builtin_amdgcn_readfirstlane(threadIdx.x >> 6) + 11) & 15;
  const int c = blockIdx.x, g = c & 7, i = c >> 3;
  if (w == 11) code_fetch_and_barrier(i);
  *(LAS v2u*)(L + (w == 11 ? L_USED : L_FLAG) + lane * 8) = v2u{0u, 0u};
  const int gq = lane >> 4, r16 = lane & 15;
  const int b = (i * 43) >> 8, s = i - b * NSPL;
  constexpr int ntile = 11;
  const int row0 = 344 * s - (s > 4 ? 8 : 0);
  const int cw = w - 12, rq = i & 3, cq = i >> 2, R0 = 128 * rq + 32 * cw;
  asm volatile("" :: "s"(x), "s"(kc), "s"(vc), "s"(wq), "s"(wo), "s"(wg), "s"(wu), "s"(wd), "s"(out), "s"(ws) : "memory");
  v4u kf[8], vf[8], xa, xr;
  v2u wsrc[8];
  const unsigned kvbase = ((unsigned)(b * HKV + g) * SK + (SK - WIN) + row0 + 16u * w) * 256u, kvbase2 = kvbase + ntile * 4096u;
  const v4i rk = mkd(kc, NB * HKV * SK * 256u), rv = mkd(vc, NB * HKV * SK * 256u);
  if (w >= 12) {
    __builtin_amdgcn_s_setprio(3);
    const v4i rw = mkd(wq, DM * NQ * 2u), rxd = mkd(x, NB * DM * 2u);
    const unsigned o = (unsigned)(8 * gq) * (NQ * 2) + r16 * 8, so = R0 * (NQ * 2) + (512 * g + 64 * cq) * 2;
#pragma unroll
    for (int j = 0; j < 8; ++j) ldnt2(wsrc[j], o, rw, so + j * (NQ * 2));
    ldp4(xa, (r16 >> 2) * 1024 + (R0 + 8 * gq) * 2, rxd);
    ldp4(xr, cw * 1024 + lane * 16, rxd);
    WGBAR();
  } else if (w != 11) {
    WGBAR();
    if (i < 24) {
      __builtin_amdgcn_s_setprio(2);
      until(rt(), TK0);
      ldtile<0, 4>(kf, r16 * 64 + gq * 16, rk, kvbase, kvbase2);
    }
  }
  PINS(w);
  const rsrc_t rws = mk(ws, WS_END), rx = mk(x, NB * DM * 2u);
  const f32x4 Z4 = {0.f, 0.f, 0.f, 0.f};
  const v4u SV = {SENT, SENT, SENT, SENT};
  const unsigned orec = WS_OREC + (g * 24 + i) * OREC_SZ;

  if (__builtin_expect(w >= 12, 1)) {
    const unsigned tg = (unsigned)load_tag(ws);
    asm volatile("s_waitcnt vmcnt(0)" : "+v"(wsrc[0]), "+v"(wsrc[1]), "+v"(wsrc[2]), "+v"(wsrc[3]), "+v"(wsrc[4]), "+v"(wsrc[5]), "+v"(wsrc[6]), "+v"(wsrc[7]), "+v"(xa), "+v"(xr) :: "memory");
    {
      v4u bt[4];
      tr4(wsrc, bt);
      if ((lane & 3) == 1) xa = v4u{ONE2, ONE2, ONE2, ONE2};
      f32x4 qp, cs;
#pragma unroll
      for (int e = 0; e < 4; ++e) { const f32x4 d = mfma(xa, bt[e], Z4); qp[e] = d[0]; cs[e] = d[1]; }
      *(LAS f32x4*)(L + L_QRED + cw * 1024 + lane * 16) = qp;
      if (lane < 16) *(LAS f32x4*)(L + L_QRED + 4096 + cw * 256 + lane * 16) = cs;
    }
    lds_set(FL + F_QR + cw);
    f32x2 xst = ln_stats(xr);
    PINV(xst);
    lds_wait(FL + F_QR + (lane & 3));
    {
      float a[4], cs[4];
#pragma unroll
      for (int j = 0; j < 4; ++j) { a[j] = *(const LAS float*)(L + L_QRED + j * 1024 + cw * 256 + lane * 4); cs[j] = *(const LAS float*)(L + L_QRED + 4096 + j * 256 + lane * 4); }
      const float q = xst.y * (((a[0] + a[1]) + (a[2] + a[3])) - xst.x * ((cs[0] + cs[1]) + (cs[2] + cs[3])));
      const unsigned q2 = pk(q, dpp<XOR1>(q));
      if ((lane & 1) == 0) ST64((v2u{q2, tg}), WS_QG + (((g * NB + cw) * 4 + rq) * 256 + 32 * cq) * 8 + lane * 4);
    }
    lds_set(FL + F_QPUB + cw);
    if (i < 24) {
      until(rt(), QDELTA);
      const unsigned qo = WS_QG + ((g * NB + b) * 4 * 256 + cw * 64 + lane) * 8;
      v2u qg[4];
      GSPIN {
        CBAR();
#pragma unroll
        for (int j = 0; j < 4; ++j) qg[j] = LD64(qo, j * 2048);
        SCHED();
        lds_set(FL + F_QIS + cw);
        SCHED();
        if (__all((qg[0].y == tg) & (qg[1].y == tg) & (qg[2].y == tg) & (qg[3].y == tg))) break;
      }
      const float q0 = (blo(qg[0].x) + blo(qg[1].x)) + (blo(qg[2].x) + blo(qg[3].x)), q1 = (bhi(qg[0].x) + bhi(qg[1].x)) + (bhi(qg[2].x) + bhi(qg[3].x));
      *(LAS unsigned*)(L + L_QS + (cw * 64 + lane) * 4) = pk(q0, q1);
      lds_set(FL + F_QS + cw);
      return;
    }
    __builtin_amdgcn_s_setprio(0);
  } else if (i < 24) {
    if (w < ntile) {
      const int nval = w < 10 ? 8 : s < 4 ? 6 : 4;
      const unsigned oob6 = nval < 6 ? 0x80000000u : 0u, oob8 = nval < 8 ? 0x80000000u : 0u;
      lds_wait(FL + F_QPUB + (lane & 3));
      ldtile<4, 6>(kf, (r16 * 64 + gq * 16) | oob6, rk, kvbase, kvbase2);
      ldtile<6, 8>(kf, (r16 * 64 + gq * 16) | oob8, rk, kvbase, kvbase2);
      __builtin_amdgcn_s_setprio(0);
      lds_wait(FL + F_QIS + (lane & 3));
      ldtile<0, 4>(vf, lane * 16, rv, kvbase, kvbase2);
      ldtile<4, 6>(vf, (lane * 16) | oob6, rv, kvbase, kvbase2);
      ldtile<6, 8>(vf, (lane * 16) | oob8, rv, kvbase, kvbase2);
      lds_wait(FL + (lane < 4 ? F_QS + lane : F_CL));
      asm volatile("s_waitcnt vmcnt(8)" : "+v"(kf[0]), "+v"(kf[1]), "+v"(kf[2]), "+v"(kf[3]), "+v"(kf[4]), "+v"(kf[5]), "+v"(kf[6]), "+v"(kf[7]) :: "memory");
      const v4u qf = *(const LAS v4u*)(L + L_QS + r16 * 64 + gq * 16);
      float sc[8];
      f32x4 dd[8];
#pragma unroll
      for (int t = 0; t < 8; ++t) dd[t] = mfma(kf[t], qf, Z4);
      SCHED();
#pragma unroll
      for (int t = 0; t < 8; ++t) {
        const f32x4 d = dd[t];
        sc[t] = (dpp<0x00>(d[0]) + dpp<0x55>(d[1])) + (dpp<0xAA>(d[2]) + dpp<0xFF>(d[3]));
        if (t >= 4 && t >= nval) sc[t] = -__builtin_inff();
      }
      float m = fmaxf(fmaxf(fmaxf(sc[0], sc[1]), fmaxf(sc[2], sc[3])), fmaxf(fmaxf(sc[4], sc[5]), fmaxf(sc[6], sc[7])));
      m = x32_max(x16_max(m));
      constexpr float C2 = 0.08838834764831845f * 1.4426950408889634f;
      const float ms = m * C2;
      float p[8], l = 0.f;
#pragma unroll
      for (int t = 0; t < 8; ++t) { p[t] = __builtin_amdgcn_exp2f(fmaf(sc[t], C2, -ms)); l += p[t]; }
      l = x32_sum(x16_sum(l));
      if ((lane & 0x33) == 0) *(LAS f32x2*)(L + L_MW + ((lane >> 2) * 12 + w) * 8) = f32x2{ms, l};
      lds_set(FL + F_MW + w);
      v4u pf = {pk(p[0], p[1]), pk(p[2], p[3]), pk(p[4], p[5]), pk(p[6], p[7])};
      unsigned pa = L_PART + w * 2048 + gq * 512 + r16 * 32;
      PINV(pf); PINV(pa);
      v4u vt[8];
#pragma unroll
      for (int mm = 0; mm < 4; ++mm) {
        if (mm == 2) lds_set(FL + F_VL + w);
        if (mm == 0) VMWAIT2(6, vf[0], vf[1]);
        if (mm == 1) VMWAIT2(4, vf[2], vf[3]);
        if (mm == 2) VMWAIT2(2, vf[4], vf[5]);
        if (mm == 3) VMWAIT2(0, vf[6], vf[7]);
#pragma unroll
        for (int d = 0; d < 4; ++d) { vt[2 * d][mm] = plo(vf[2 * mm][d], vf[2 * mm + 1][d]); vt[2 * d + 1][mm] = phi(vf[2 * mm][d], vf[2 * mm + 1][d]); }
        if (mm < 3) {
#pragma unroll
          for (int e = 0; e < 8; ++e) PINV(vt[e]);
        }
        SCHED();
      }
      float o[8];
#pragma unroll
      for (int e = 0; e < 8; ++e) o[e] = mfma(pf, vt[e], Z4)[0];
      *(LAS f32x4*)(L + pa) = f32x4{o[0], o[1], o[2], o[3]};
      *(LAS f32x4*)(L + pa + 16) = f32x4{o[4], o[5], o[6], o[7]};
      lds_set(FL + F_OPF + w);
      if (w < 4) {
        const int jl = r16 < ntile ? r16 : 0;
        lds_wait(FL + F_MW + jl);
        const f32x2 ml = *(const LAS f32x2*)(L + L_MW + (w * 12 + jl) * 8);
        const float mg = row_max(ml.x);
        const float wt = r16 < ntile ? __builtin_amdgcn_exp2f(ml.x - mg) : 0.f;
        const float lt = row_sum(wt * ml.y);
        float wj[11];
#pragma unroll
        for (int j = 0; j < 11; ++j) { wj[j] = __builtin_bit_cast(float, __builtin_amdgcn_readlane(__builtin_bit_cast(int, wt), j)); PINS(wj[j]); }
        if (lane == 0) ST64((v2u{nosent(__float_as_uint(mg)), nosent(__float_as_uint(lt))}), orec + 1024 + w * 8);
        unsigned ra = L_PART + w * 512 + lane * 8, so = orec + w * 256 + lane * 4;
        PINV(ra); PINV(so);
        lds_wait(FL + F_OPF + jl);
        f32x2 pv[11];
#pragma unroll
        for (int j = 0; j < 11; ++j) pv[j] = *(const LAS f32x2*)(L + ra + j * 2048);
        SCHED();
        f32x2 a = pv[0] * wj[0], a2 = pv[1] * wj[1];
#pragma unroll
        for (int j = 2; j < 10; j += 2) { a += pv[j] * wj[j]; a2 += pv[j + 1] * wj[j + 1]; }
        a += pv[10] * wj[10];
        a += a2;
        const unsigned v = nosent(pk(a.x, a.y));
        const v4u o4 = {dppu<0x00>(v), dppu<0x55>(v), dppu<0xAA>(v), dppu<0xFF>(v)};
        if ((lane & 3) == 0) ST128(o4, so);
      }
    } else if (w == 11) {
      __builtin_amdgcn_s_setprio(3);
      ST128(SV, orec + lane * 16);
      if (lane < 8) ST128(SV, orec + 1024 + lane * 16);
      const u64 tag = load_tag(ws);
      const unsigned taghi = (unsigned)(tag >> 16), taglo = (unsigned)(tag << 16);
      const bool isR = i < 8;
      const int ln = g * 8 + i, rb = ln >> 4, pg = lane >> 3, q4 = lane & 7, col = 32 * (ln & 15) + 4 * q4;
      v2u xv = {0u, 0u};
      if (isR) xv = __builtin_amdgcn_raw_buffer_load_b64(rx, (rb * DM + col) * 2, 0, 0);
      DRAIN();
      lds_set(FL + F_CL);
      lds_wait(FL + F_VL + (lane < ntile ? lane : 0));
      if (lane == 0) ST64(hint(taglo, taghi), WS_HA + (g * 32 + i) * 128);
      if (isR) {
        const rsrc_t ro = mk(out, NB * DM * 2u);
        unsigned pf = __builtin_amdgcn_raw_buffer_load_b32(ro, 0, 0, AUX_SC1);
        asm volatile("" :: "v"(pf));
        v2u hb;
        GSPIN {
          CBAR();
          hb = LD64(WS_HB + g * 4096 + ((lane & 7) * 4 + ((ln >> 2) & 3)) * 128, 0);
          if (__all(hint_ok(hb, taglo, taghi))) break;
          __builtin_amdgcn_s_sleep(2);
        }
        wait_age(hb.x, DLY3 + 20);
        v2u hv;
        GSPIN {
          CBAR();
          hv = LD64(WS_HP + (pg * NB + rb) * 1024 + col * 2, 0);
          if (__all(max(hv.x, hv.y) != SENT)) break;
        }
        float h[4] = {blo(hv.x), bhi(hv.x), blo(hv.y), bhi(hv.y)};
#pragma unroll
        for (int d = 0; d < 4; ++d) { h[d] += dpp<ROR8>(h[d]); h[d] = x32_sum(x16_sum(h[d])); }
        {
          const unsigned a0 = pk(h[0], h[1]), a1 = pk(h[2], h[3]);
          const unsigned h0 = pk(blo(xv.x) + blo(a0), bhi(xv.x) + bhi(a0)), h1 = pk(blo(xv.y) + blo(a1), bhi(xv.y) + bhi(a1));
          h[0] = blo(h0); h[1] = bhi(h0); h[2] = blo(h1); h[3] = bhi(h1);
        }
        unsigned po = WS_OP + pg * 8192 + ln * 128 + q4 * 16, po3 = pg < 4 ? po + 24 * 8192 : po, oo = (rb * DM + col) * 2;
        PINV(po); PINV(po3); PINV(oo); PINV(h[0]); PINV(h[1]); PINV(h[2]); PINV(h[3]);
        v2u hc;
        GSPIN {
          CBAR();
          hc = LD64(WS_HC + g * 4096 + (lane < NDCU ? lane : 0) * 128, 0);
          if (__all(hint_ok(hc, taglo, taghi))) break;
        }
        wait_age(hc.x, DLY5);
        v4u pv[4];
        GSPIN {
          CBAR();
#pragma unroll
          for (int t = 0; t < 3; ++t) pv[t] = LD128(po, t * 8 * 8192);
          pv[3] = LD128(po3, 0);
          if (__all(okv(pv[0]) & okv(pv[1]) & okv(pv[2]) & okv(pv[3]))) break;
        }
        float y[4];
#pragma unroll
        for (int d = 0; d < 4; ++d) {
          float t = (__uint_as_float(pv[0][d]) + __uint_as_float(pv[1][d])) + (__uint_as_float(pv[2][d]) + (pg < 4 ? __uint_as_float(pv[3][d]) : 0.f));
          t += dpp<ROR8>(t);
          y[d] = x32_sum(x16_sum(t));
        }
        const unsigned y0 = pk(y[0], y[1]), y1 = pk(y[2], y[3]);
        if (lane < 8) __builtin_amdgcn_raw_buffer_store_b64(v2u{pk(h[0] + blo(y0), h[1] + bhi(y0)), pk(h[2] + blo(y1), h[3] + bhi(y1))}, ro, oo, 0, AUX_SC1);
        if (ln == 0 && lane == 0) ST64((v2u{(unsigned)tag, (unsigned)(tag >> 32)}), WS_EPOCH);
      }
    }
    return;
  }

  const int jq = i - 24;
  const bool isC = jq < 4;
  const int dI = g + 8 * (jq - 4);
  if (!isC && dI >= NDCU) return;
  const u64 tag = load_tag(ws);
  const unsigned taghi = (unsigned)(tag >> 16), taglo = (unsigned)(tag << 16);
#define GATHER_H() do { \
    unsigned klo = 0x00003F80u, khi = 0x3F800000u, ho = WS_HP + w * 256 + lane * 4, oa = L_OA + w * 256 + apos(lane) * 4; \
    unsigned hp[8] = {0u, 0u, 0u, 0u, 0u, 0u, 0u, 0u}; \
    float x0 = blo(xp), x1 = bhi(xp); \
    PINS(klo); PINS(khi); PINV(oa); PINV(x0); PINV(x1); \
    ask8(hp, (unsigned)(size_t)(FL + F_GO + (lane & 7)), ho, mkd(ws, WS_END)); \
    if (!__all(max(max(max(hp[0], hp[1]), max(hp[2], hp[3])), max(max(hp[4], hp[5]), max(hp[6], hp[7]))) != SENT)) { \
      GSPIN { \
        CBAR(); \
        _Pragma("unroll") for (int j = 0; j < 8; ++j) hp[j] = LD32(ho, j * 4096); \
        if (__all(max(max(max(hp[0], hp[1]), max(hp[2], hp[3])), max(max(hp[4], hp[5]), max(hp[6], hp[7]))) != SENT)) break; \
      } \
      DRAIN(); \
    } \
    float a0 = 0.f, a1 = 0.f, a2 = 0.f, a3 = 0.f; \
    _Pragma("unroll") for (int j = 0; j < 4; ++j) { a0 = dot2(hp[j], klo, a0); a1 = dot2(hp[j], khi, a1); a2 = dot2(hp[4 + j], klo, a2); a3 = dot2(hp[4 + j], khi, a3); } \
    const unsigned t0 = pk(a0 + a2, a1 + a3); \
    *(LAS unsigned*)(L + oa) = pk(x0 + blo(t0), x1 + bhi(t0)); \
    CBAR(); \
    if (w == 10) ST64(hint(taglo, taghi), WS_HC + (lane & 7) * 4096 + dI * 128); \
  } while (0)
  unsigned xp = 0;
  if (!isC) xp = __builtin_amdgcn_raw_buffer_load_b32(rx, w * 256 + lane * 4, 0, 0);
  if (w < 8) {
    const unsigned stride = isC ? DM * 2 : FF * 2;
    const GAS unsigned char* p = isC ? (const GAS unsigned char*)wo + (size_t)(512 * g) * (DM * 2) + (128 * jq + 8 * r16) * 2
                                     : (const GAS unsigned char*)(r16 < 8 ? wg : wu) + (64 * dI + 8 * (r16 & 7)) * 2;
    p += (size_t)(64 * w + 8 * gq) * stride;
    unsigned aa = L_OA + (r16 >> 2) * 1024 + w * 128 + gq * 32 + (lane & 1) * 16, pa = L_PART + w * 2048 + gq * 512 + r16 * 32;
    int sidx = (lane & 1) ? 0xEEEE : 0x4444;
    PINV(aa); PINV(pa); PINV(sidx);
    v4u src[16], bt[16];
    until(tstart, isC ? TC0 : TD0);
#pragma unroll
    for (int j = 0; j < WPRE; ++j) src[j] = __builtin_nontemporal_load((const GAS v4u*)(p + ((j >> 3) * 32 + (j & 7)) * stride));
    lds_wait(FL + F_QPUB + (lane & 3));
#pragma unroll
    for (int j = WPRE; j < 16; ++j) src[j] = __builtin_nontemporal_load((const GAS v4u*)(p + ((j >> 3) * 32 + (j & 7)) * stride));
    SCHED();
    tr8(src, bt);
    tr8(src + 8, bt + 8);
    if (!isC) {
      const v4u ones = {ONE2, ONE2, ONE2, ONE2};
      float cs[8];
#pragma unroll
      for (int e = 0; e < 8; ++e) cs[e] = mfma(ones, bt[8 + e], mfma(ones, bt[e], Z4))[0];
      if (lane < 16) {
        *(LAS f32x4*)(L + L_SS + w * 512 + lane * 32) = f32x4{cs[0], cs[1], cs[2], cs[3]};
        *(LAS f32x4*)(L + L_SS + w * 512 + lane * 32 + 16) = f32x4{cs[4], cs[5], cs[6], cs[7]};
      }
      lds_set(FL + F_SR + w);
    }
    v8u b8[8];
    f32x4 acc[8];
#pragma unroll
    for (int e = 0; e < 8; ++e) { b8[e] = cat(bt[e], bt[8 + e]); PINV(b8[e]); acc[e] = Z4; PINV(acc[e]); }
    DRAIN();
    if (w == 0) {
      if (isC) {
        const int hl = lane < 24 ? lane : 0;
        watch(rws, WS_HA + (g * 32 + hl) * 128, taglo, taghi, FL + F_GO + hl);
      } else {
        watch8(rws, WS_HB + g * 4096 + (lane & 31) * 128, taglo, taghi, DLY3, FL + F_GO, lane);
      }
      DRAIN();
    } else if (w == 1 && isC) {
      lds_wait(FL + F_SR + (lane & 7));
      if (lane < 8) ST64(hint(taglo, taghi), WS_HB + lane * 4096 + (g * 4 + jq) * 128);
    }
    if (!isC) GATHER_H();
    WGBAR();
    {
      const v4u a0 = *(const LAS v4u*)(L + aa);
#pragma unroll
      for (int e = 0; e < 8; ++e) acc[e] = mfma64(a0, b8[e], acc[e], sidx);
      SCHED();
#pragma unroll
      for (int h = 0; h < 2; ++h)
        *(LAS f32x4*)(L + pa + 16 * h) = f32x4{acc[4 * h][0] + acc[4 * h][1], acc[4 * h + 1][0] + acc[4 * h + 1][1], acc[4 * h + 2][0] + acc[4 * h + 2][1], acc[4 * h + 3][0] + acc[4 * h + 3][1]};
    }
    WGBAR();
    if (!isC) WGBAR();
  } else if (isC) {
    const int v = w & 7, bb = v >> 1, head = 2 * (v & 1) + (lane >> 5), sg = gq & 1;
    unsigned ra = L_PART + (w & 3) * 512 + lane * 8, so = WS_HP + (g * NB + (w & 3)) * 1024 + jq * 256 + lane * 4;
    unsigned rd = WS_OREC + (g * 24 + bb * NSPL + sg) * OREC_SZ + head * 256 + r16 * 16, rs = WS_OREC + (g * 24 + bb * NSPL + sg) * OREC_SZ + 1024 + head * 8;
    unsigned oa = L_OA + bb * 1024 + head * 256 + apos(4 * r16 + 2 * sg) * 4;
    PINV(ra); PINV(so); PINV(rd); PINV(rs); PINV(oa);
    if (w == 15) ST128(SV, WS_HP + (g * NB + gq) * 1024 + jq * 256 + r16 * 16);
    DRAIN();
    {
      unsigned e;
#pragma nounroll
      for (unsigned sp = 0; sp < SPIN_L; ++sp) { e = FL[F_GO + bb * NSPL + (lane < NSPL ? lane : 0)]; if (__all(e != 0u)) break; }
      wait_age(e, DLY2);
    }
    GSPIN {
      v4u od[3];
      v2u st[3];
      CBAR();
#pragma unroll
      for (int u = 0; u < 3; ++u) st[u] = LD64(rs, 2 * u * OREC_SZ);
      SCHED();
#pragma unroll
      for (int u = 0; u < 3; ++u) od[u] = LD128(rd, 2 * u * OREC_SZ);
      SCHED();
      lds_set(FL + F_SR + v);
      SCHED();
      unsigned bad = max(max(max(st[0].x, st[0].y), max(st[1].x, st[1].y)), max(st[2].x, st[2].y));
      const float m0 = __uint_as_float(st[0].x), m1 = __uint_as_float(st[1].x), m2 = __uint_as_float(st[2].x);
      const float mg = x16_max(fmaxf(fmaxf(m0, m1), m2));
      float wt[3] = {__builtin_amdgcn_exp2f(m0 - mg), __builtin_amdgcn_exp2f(m1 - mg), __builtin_amdgcn_exp2f(m2 - mg)};
      const float inv = __builtin_amdgcn_rcpf(x16_sum(fmaf(wt[0], __uint_as_float(st[0].y), fmaf(wt[1], __uint_as_float(st[1].y), wt[2] * __uint_as_float(st[2].y)))));
#pragma unroll
      for (int u = 0; u < 3; ++u) { wt[u] *= inv; PINV(wt[u]); }
      PINV(bad);
      SCHED();
      f32x2 a[4];
#pragma unroll
      for (int u = 0; u < 3; ++u) {
        f32x2 t[4];
#pragma unroll
        for (int d = 0; d < 4; ++d) { t[d] = f32x2{blo(od[u][d]), bhi(od[u][d])}; PINV(t[d]); }
        bad = max(bad, max(max(od[u].x, od[u].y), max(od[u].z, od[u].w)));
#pragma unroll
        for (int d = 0; d < 4; ++d) a[d] = u == 0 ? t[d] * wt[0] : t[d] * wt[u] + a[d];
      }
      float r[4];
#pragma unroll
      for (int d = 0; d < 4; ++d) {
        const auto sw = __builtin_amdgcn_permlane16_swap(__float_as_uint(a[d >> 1][d & 1]), __float_as_uint(a[2 + (d >> 1)][d & 1]), false, false);
        r[d] = __uint_as_float(sw[0]) + __uint_as_float(sw[1]);
      }
      *(LAS unsigned*)(L + oa) = pk(r[0], r[1]);
      *(LAS unsigned*)(L + oa + 16) = pk(r[2], r[3]);
      if (__all(bad != SENT)) break;
    }
    DRAIN();
    WGBAR();
    WGBAR();
    if (w >= 12) {
      f32x2 pv[8];
#pragma unroll
      for (int j = 0; j < 8; ++j) pv[j] = *(const LAS f32x2*)(L + ra + j * 2048);
      SCHED();
      const f32x2 t = ((pv[0] + pv[1]) + (pv[2] + pv[3])) + ((pv[4] + pv[5]) + (pv[6] + pv[7]));
      const unsigned vv = nosent(pk(t.x, t.y));
      const v4u o4 = {dppu<0x00>(vv), dppu<0x55>(vv), dppu<0xAA>(vv), dppu<0xFF>(vv)};
      if ((lane & 3) == 0) ST128(o4, so);
    }
  } else {
    unsigned ra = L_PART + (w & 3) * 512 + lane * 4, hw = L_HH + (w & 3) * 128 + apos(lane >> 1) * 4 + (lane & 1) * 2, ha = L_HH + (r16 >> 2) * 128 + gq * 32 + (lane & 1) * 16;
    int sidx = (lane & 1) ? 0xEEEE : 0x4444;
    PINV(sidx);
    unsigned so = WS_OP + dI * 8192 + gq * 2048 + (w & 7) * 256 + r16 * 16, sa = L_OA + (w & 3) * 1024 + lane * 16;
    PINV(ra); PINV(hw); PINV(ha); PINV(so); PINV(sa);
    ST128(SV, WS_OP + dI * 8192 + (w - 8) * 1024 + lane * 16);
    until(tstart, TWD);
    const rsrc_t rw = mk(wd, FF * DM * 2u);
    v2u src[16];
    v4u bt[8];
#pragma unroll
    for (int j = 0; j < 16; ++j) src[j] = __builtin_amdgcn_raw_buffer_load_b64(rw, (unsigned)(8 * gq + (j & 3)) * 1024 + (w - 8) * 128 + r16 * 8, (64 * dI + (j >> 3) * 32 + (j & 4)) * 1024, AUX_NT);
    SCHED();
    tr4(src, bt);
    tr4(src + 8, bt + 4);
    v8u b8[4];
    f32x4 acc[4];
#pragma unroll
    for (int e = 0; e < 4; ++e) { b8[e] = cat(bt[e], bt[4 + e]); PINV(b8[e]); acc[e] = Z4; PINV(acc[e]); }
    float sgs = 0.f, sus = 0.f;
    if (w >= 12) {
      lds_wait(FL + F_SR + (lane & 7));
#pragma unroll
      for (int j = 0; j < 8; ++j) { sgs += *(const LAS float*)(L + L_SS + j * 512 + lane * 4); sus += *(const LAS float*)(L + L_SS + j * 512 + 256 + lane * 4); }
    }
    PINV(sgs); PINV(sus);
    DRAIN();
    lds_set(FL + F_CL + w - 8);
    if (w == 10) lds_wait(FL + F_CL + (lane & 7));
    GATHER_H();
    WGBAR();
    if (w >= 12) {
      const f32x2 st = ln_stats(*(const LAS v4u*)(L + sa));
      float kg = st.y * -1.4426950408889634f, cg = st.x * sgs, cu = st.x * sus, ku = st.y;
      PINV(kg); PINV(cg); PINV(cu); PINV(ku);
      WGBAR();
      float gp[8], up[8];
#pragma unroll
      for (int j = 0; j < 8; ++j) { gp[j] = *(const LAS float*)(L + ra + j * 2048); up[j] = *(const LAS float*)(L + ra + j * 2048 + 256); }
      SCHED();
      const float gg = ((gp[0] + gp[1]) + (gp[2] + gp[3])) + ((gp[4] + gp[5]) + (gp[6] + gp[7]));
      const float uu = ((up[0] + up[1]) + (up[2] + up[3])) + ((up[4] + up[5]) + (up[6] + up[7]));
      const float hh = ku * (uu - cu) * __builtin_amdgcn_rcpf(1.f + __builtin_amdgcn_exp2f(kg * (gg - cg)));
      *(LAS u16*)(L + hw) = (u16)pk(hh, hh);
    } else {
      WGBAR();
    }
    WGBAR();
    const v4u a0 = *(const LAS v4u*)(L + ha);
#pragma unroll
    for (int e = 0; e < 4; ++e) acc[e] = mfma64(a0, b8[e], acc[e], sidx);
    SCHED();
    ST128((v4u{nosent(__float_as_uint(acc[0][0] + acc[0][1])), nosent(__float_as_uint(acc[1][0] + acc[1][1])),
               nosent(__float_as_uint(acc[2][0] + acc[2][1])), nosent(__float_as_uint(acc[3][0] + acc[3][1]))}), so);
  }
}
}

extern "C" void kernel_launch(void* const* d_in, const int* in_sizes, int n_in, void* d_out, int out_size, void* d_ws, size_t ws_size, hipStream_t stream) {
  (void)in_sizes; (void)n_in; (void)out_size; (void)ws_size;
  fused_decode<<<dim3(256), dim3(1024), 0, stream>>>((const u16*)d_in[0], (const u16*)d_in[1], (const u16*)d_in[2], (const u16*)d_in[3], (const u16*)d_in[4],
                                                     (const u16*)d_in[6], (const u16*)d_in[7], (const u16*)d_in[8], (u16*)d_out, (unsigned char*)d_ws);
}
```

```cpp
#include <hip/hip_runtime.h>
#include <stdint.h>

#define PROF 2
#define TK0 10
#define KPRE 4
#define QDELTA 70
#define TC0 240
#define TD0 100
#define KMID 0
#define KLEFT 2
#define VPRE 0
#define VEV 2
#define WPRE 3
#define TWD 450
#define DLY2 60
#define DLY3 86
#define DLY5 78

namespace {
typedef unsigned short u16;
typedef unsigned long long u64;
typedef unsigned v4u __attribute__((ext_vector_type(4)));
typedef unsigned v2u __attribute__((ext_vector_type(2)));
typedef int v4i __attribute__((ext_vector_type(4)));
typedef float f32x4 __attribute__((ext_vector_type(4)));
typedef float f32x2 __attribute__((ext_vector_type(2)));
typedef short bf16x8 __attribute__((ext_vector_type(8)));
typedef __bf16 bf16x2 __attribute__((ext_vector_type(2)));
typedef __bf16 bf16v8 __attribute__((ext_vector_type(8)));
typedef __bf16 bf16v16 __attribute__((ext_vector_type(16)));
typedef unsigned v8u __attribute__((ext_vector_type(8)));
#define GAS __attribute__((address_space(1)))
#define LAS __attribute__((address_space(3)))
typedef __amdgpu_buffer_rsrc_t rsrc_t;

constexpr int NB = 4, DM = 512, HKV = 8, SK = 8192, WIN = 2048, FF = 1792, NQ = 4096;
constexpr int NSPL = 6, NDCU = 28;
constexpr int AUX_NT = 2, AUX_SC1 = 16;
constexpr unsigned SENT = 0xFFFFFFFFu, ONE2 = 0x3F803F80u;
constexpr int WS_EPOCH = 0, WS_HB = 4096, WS_HC = WS_HB + 8 * 4096, WS_HA = WS_HC + 8 * 4096,
              WS_QG = WS_HA + 8 * 32 * 128;
constexpr int WS_OREC = WS_QG + 8 * 4 * 4 * 256 * 8, OREC_SZ = 1152;
constexpr int WS_HP = WS_OREC + 8 * 24 * OREC_SZ, WS_OP = WS_HP + 8 * 4 * 512 * 2, WS_END = WS_OP + NDCU * 8192;
constexpr int WS_PROF = 2 << 20;
constexpr int L_FLAG = 0, L_MW = 512, L_QS = 1024, L_OA = L_QS + 1024, L_HH = L_OA + 4096, L_SS = L_HH + 512;
constexpr int L_QRED = L_SS + 4096, L_PART = L_QRED + 5120, L_USED = L_PART + 11 * 2048;
constexpr int L_TOTAL = 84 * 1024;
static_assert(L_USED + 512 <= L_TOTAL, "lds");
enum { F_QIS = 0, F_QS = 4, F_QPUB = 12, F_QR = 16, F_CL = 24, F_GO = 32, F_SR = 72, F_MW = 80, F_VL = 96, F_OPF = 112 };
constexpr int CODE_BYTES = 15872;
constexpr unsigned SPIN_G = 1u << 17, SPIN_L = 1u << 21;

__device__ __forceinline__ u64 rt() { return __builtin_amdgcn_s_memrealtime(); }
__device__ __forceinline__ rsrc_t mk(const void* p, unsigned bytes) { return __builtin_amdgcn_make_buffer_rsrc((void*)p, 0, bytes, 0x00020000); }
__device__ __forceinline__ f32x4 mfma(v4u a, v4u b, f32x4 c) { return __builtin_amdgcn_mfma_f32_16x16x32_bf16(__builtin_bit_cast(bf16x8, a), __builtin_bit_cast(bf16x8, b), c, 0, 0, 0); }
__device__ __forceinline__ v8u cat(v4u b0, v4u b1) { return __builtin_shufflevector(b0, b1, 0, 1, 2, 3, 4, 5, 6, 7); }
__device__ __forceinline__ f32x4 mfma64(v4u a, v8u b, f32x4 c, int idx) {
  return __builtin_amdgcn_smfmac_f32_16x16x64_bf16(__builtin_bit_cast(bf16v8, a), __builtin_bit_cast(bf16v16, b), c, idx, 0, 0);
}
__device__ __forceinline__ unsigned apos(unsigned d) { return (d & ~7u) | ((d & 1u) << 2) | ((d >> 1) & 3u); }
__device__ __forceinline__ unsigned pk(float lo, float hi) { return __builtin_bit_cast(unsigned, __builtin_convertvector(f32x2{lo, hi}, bf16x2)); }
__device__ __forceinline__ float blo(unsigned v) { return __builtin_bit_cast(float, v << 16); }
__device__ __forceinline__ float bhi(unsigned v) { return __builtin_bit_cast(float, v & 0xffff0000u); }
__device__ __forceinline__ unsigned plo(unsigned a, unsigned b) { return __builtin_amdgcn_perm(b, a, 0x05040100u); }
__device__ __forceinline__ unsigned phi(unsigned a, unsigned b) { return __builtin_amdgcn_perm(b, a, 0x07060302u); }
__device__ __forceinline__ unsigned nosent(unsigned v) { return v < 0xFFFFFFFEu ? v : 0xFFFFFFFEu; }
template <int C> __device__ __forceinline__ float dpp(float x) { return __builtin_bit_cast(float, __builtin_amdgcn_mov_dpp(__builtin_bit_cast(int, x), C, 0xf, 0xf, true)); }
template <int C> __device__ __forceinline__ unsigned dppu(unsigned x) { return (unsigned)__builtin_amdgcn_mov_dpp((int)x, C, 0xf, 0xf, true); }
constexpr int XOR1 = 0xB1, XOR2 = 0x4E, ROR4 = 0x124, ROR8 = 0x128;
__device__ __forceinline__ float x16_sum(float x) { auto s = __builtin_amdgcn_permlane16_swap(__float_as_uint(x), __float_as_uint(x), false, false); return __uint_as_float(s[0]) + __uint_as_float(s[1]); }
__device__ __forceinline__ float x32_sum(float x) { auto s = __builtin_amdgcn_permlane32_swap(__float_as_uint(x), __float_as_uint(x), false, false); return __uint_as_float(s[0]) + __uint_as_float(s[1]); }
__device__ __forceinline__ float x16_max(float x) { auto s = __builtin_amdgcn_permlane16_swap(__float_as_uint(x), __float_as_uint(x), false, false); return fmaxf(__uint_as_float(s[0]), __uint_as_float(s[1])); }
__device__ __forceinline__ float x32_max(float x) { auto s = __builtin_amdgcn_permlane32_swap(__float_as_uint(x), __float_as_uint(x), false, false); return fmaxf(__uint_as_float(s[0]), __uint_as_float(s[1])); }
__device__ __forceinline__ float row_sum(float x) { x += dpp<XOR1>(x); x += dpp<XOR2>(x); x += dpp<ROR4>(x); x += dpp<ROR8>(x); return x; }
__device__ __forceinline__ float row_max(float x) { x = fmaxf(x, dpp<XOR1>(x)); x = fmaxf(x, dpp<XOR2>(x)); x = fmaxf(x, dpp<ROR4>(x)); x = fmaxf(x, dpp<ROR8>(x)); return x; }
__device__ __forceinline__ float dot2(unsigned a, unsigned b, float c) { return __builtin_amdgcn_fdot2_f32_bf16(__builtin_bit_cast(bf16x2, a), __builtin_bit_cast(bf16x2, b), c, false); }
#define CBAR() asm volatile("" ::: "memory")
#define SCHED() __builtin_amdgcn_sched_barrier(0)
#define DRAIN() do { asm volatile("s_waitcnt vmcnt(0)" ::: "memory"); __builtin_amdgcn_s_waitcnt(0x0F70); } while (0)
#define PINV(x) asm volatile("" : "+v"(x))
#define PINS(x) asm volatile("" : "+s"(x))
#define WGBAR() do { SCHED(); asm volatile("s_waitcnt lgkmcnt(0)" ::: "memory"); __builtin_amdgcn_s_barrier(); CBAR(); SCHED(); } while (0)
#define GSPIN _Pragma("nounroll") for (unsigned sp = 0; sp < SPIN_G; ++sp)
#define LD32(o, so) __builtin_amdgcn_raw_buffer_load_b32(rws, (o), (so), AUX_SC1)
#define LD64(o, so) __builtin_amdgcn_raw_buffer_load_b64(rws, (o), (so), AUX_SC1)
#define LD128(o, so) __builtin_amdgcn_raw_buffer_load_b128(rws, (o), (so), AUX_SC1)
#define ST64(v, o) __builtin_amdgcn_raw_buffer_store_b64((v), rws, (o), 0, AUX_SC1)
#define ST128(v, o) __builtin_amdgcn_raw_buffer_store_b128((v), rws, (o), 0, AUX_SC1)
__device__ __forceinline__ bool okv(v4u r) { return max(max(r.x, r.y), max(r.z, r.w)) != SENT; }
__device__ __forceinline__ void lds_wait(volatile LAS unsigned* f) {
  SCHED();
#pragma nounroll
  for (unsigned s = 0; s < SPIN_L; ++s) { if (__all(*f != 0u)) break; }
  CBAR();
  SCHED();
}
__device__ __forceinline__ void lds_set(volatile LAS unsigned* f) { CBAR(); *f = 1u; CBAR(); }
__device__ __forceinline__ void until(u64 t0, unsigned ticks) { while (rt() - t0 < ticks) __builtin_amdgcn_s_sleep(1); }
__device__ __forceinline__ v2u hint(unsigned taglo, unsigned taghi) { return v2u{taglo | ((unsigned)rt() & 0xFFFFu), taghi}; }
__device__ __forceinline__ bool hint_ok(v2u h, unsigned taglo, unsigned taghi) { return (h.y == taghi) & ((h.x ^ taglo) < 65536u); }
__device__ __forceinline__ void wait_age(unsigned t16, unsigned dly) {
#pragma nounroll
  for (unsigned s = 0; s < 4096u; ++s) { if (__all((((unsigned)rt() - t16) & 0xFFFFu) >= dly)) break; }
}
__device__ __forceinline__ void watch(rsrc_t rws, unsigned ho, unsigned taglo, unsigned taghi, volatile LAS unsigned* slot) {
  GSPIN {
    CBAR();
    const v2u h = LD64(ho, 0);
    const bool ok = hint_ok(h, taglo, taghi);
    if (ok) *slot = h.x | 0x10000u;
    CBAR();
    if (__all(ok)) break;
  }
}
__device__ __forceinline__ void watch8(rsrc_t rws, unsigned ho, unsigned taglo, unsigned taghi, unsigned dly, volatile LAS unsigned* go, int lane) {
  v2u h;
  unsigned seen = 0, done = 0;
#pragma nounroll
  for (unsigned sp = 0; sp < SPIN_G && done != 0x11111111u; ++sp) {
    if (seen != ~0u) {
      CBAR();
      h = LD64(ho, 0);
      seen = (unsigned)__builtin_amdgcn_ballot_w64(hint_ok(h, taglo, taghi));
    }
    unsigned m = (unsigned)__builtin_amdgcn_ballot_w64((((unsigned)rt() - h.x) & 0xFFFFu) >= dly) & seen;
    m &= m >> 1; m &= m >> 2; m &= 0x11111111u;
    if (m != done) { if ((m >> (4 * (lane & 7))) & 1u) go[lane & 7] = 1u; done = m; }
  }
  CBAR();
}
__device__ __forceinline__ u64 load_tag(const unsigned char* ws) {
  u64 e;
  asm volatile("s_load_dwordx2 %0, %1, 0x0\n\ts_waitcnt lgkmcnt(0)" : "=s"(e) : "s"(ws) : "memory");
  return (e + 1ull) & 0xFFFFFFFFFFFFull;
}
__device__ __forceinline__ void code_fetch_and_barrier(int part) {
  u64 pc0;
  asm volatile("s_getpc_b64 %0" : "=s"(pc0));
  const u64 a0 = (pc0 & ~127ull) + (u64)(part * 512 < CODE_BYTES - 512 ? part * 512 : CODE_BYTES - 512);
  unsigned t0, t1, t2, t3, t4, t5, t6, t7;
  asm volatile("s_load_dword %0, %8, 0x0\n\ts_load_dword %1, %8, 0x40\n\ts_load_dword %2, %8, 0x80\n\ts_load_dword %3, %8, 0xc0\n\t"
               "s_load_dword %4, %8, 0x100\n\ts_load_dword %5, %8, 0x140\n\ts_load_dword %6, %8, 0x180\n\ts_load_dword %7, %8, 0x1c0\n\t"
               "s_barrier\n\ts_waitcnt lgkmcnt(0)"
               : "=&s"(t0), "=&s"(t1), "=&s"(t2), "=&s"(t3), "=&s"(t4), "=&s"(t5), "=&s"(t6), "=&s"(t7) : "s"(a0) : "memory");
}
__device__ __forceinline__ v4i mkd(const void* p, unsigned bytes) { return v4i{(int)(u64)p, (int)((u64)p >> 32) & 0xFFFF, (int)bytes, 0x00020000}; }
template <int OFF> __device__ __forceinline__ void ldnt(v4u& d, unsigned vo, v4i rs, unsigned so) {
  asm volatile("buffer_load_dwordx4 %0, %1, %2, %3 offen offset:%4 nt" : "=v"(d) : "v"(vo), "s"(rs), "s"(so), "i"(OFF) : "memory");
}
template <int LO, int HI> __device__ __forceinline__ void ldtile(v4u* d, unsigned vo, v4i rs, unsigned so, unsigned so2) {
  if constexpr (LO < HI) { ldnt<(LO & 3) * 1024>(d[LO], vo, rs, LO < 4 ? so : so2); ldtile<LO + 1, HI>(d, vo, rs, so, so2); }
}
__device__ __forceinline__ void ldnt2(v2u& d, unsigned vo, v4i rs, unsigned so) {
  asm volatile("buffer_load_dwordx2 %0, %1, %2, %3 offen nt" : "=v"(d) : "v"(vo), "s"(rs), "s"(so) : "memory");
}
__device__ __forceinline__ void ldp4(v4u& d, unsigned vo, v4i rs) {
  asm volatile("buffer_load_dwordx4 %0, %1, %2, 0 offen" : "=v"(d) : "v"(vo), "s"(rs) : "memory");
}
#define VMWAIT2(n, a, b) asm volatile("s_waitcnt vmcnt(%2)" : "+v"(a), "+v"(b) : "i"(n) : "memory")
__device__ __forceinline__ void ask8(unsigned (&hp)[8], unsigned go, unsigned ho, v4i rs) {
  unsigned t, done, n, cnt;
#define ASK1(j, off) "s_bitcmp1_b32 %10, " #j "\n\ts_cbranch_scc0 2" #j "f\n\ts_movk_i32 %11, " #off "\n\tbuffer_load_dword %" #j ", %13, %14, %11 offen sc1\n2" #j ":\n\t"
  asm volatile("s_mov_b32 %9, 0\n\ts_mov_b32 %11, 0x200000\n"
               "1:\n\tds_read_b32 %8, %12\n\ts_sub_u32 %11, %11, 1\n\ts_cbranch_scc1 3f\n\ts_waitcnt lgkmcnt(0)\n\t"
               "v_cmp_ne_u32 vcc, 0, %8\n\ts_andn2_b32 %10, vcc_lo, %9\n\ts_and_b32 %10, %10, 0xff\n\ts_cbranch_scc0 1b\n\t"
               ASK1(0, 0) ASK1(1, 0x1000) ASK1(2, 0x2000) ASK1(3, 0x3000) ASK1(4, 0x4000) ASK1(5, 0x5000) ASK1(6, 0x6000) ASK1(7, 0x7000)
               "s_mov_b32 %11, 0x200000\n\ts_or_b32 %9, %9, %10\n\ts_cmp_lg_u32 %9, 0xff\n\ts_cbranch_scc1 1b\n"
               "3:\n\ts_waitcnt vmcnt(0) lgkmcnt(0)"
               : "+v"(hp[0]), "+v"(hp[1]), "+v"(hp[2]), "+v"(hp[3]), "+v"(hp[4]), "+v"(hp[5]), "+v"(hp[6]), "+v"(hp[7]), "=&v"(t), "=&s"(done), "=&s"(n), "=&s"(cnt)
               : "v"(go), "v"(ho), "s"(rs) : "vcc", "scc", "memory");
#undef ASK1
}
__device__ __forceinline__ f32x2 ln_stats(v4u hv) {
  const float c0 = blo((unsigned)__builtin_amdgcn_readfirstlane(hv[0]));
  float t[8];
#pragma unroll
  for (int d = 0; d < 4; ++d) { t[2 * d] = blo(hv[d]) - c0; t[2 * d + 1] = bhi(hv[d]) - c0; }
  float s1 = ((t[0] + t[1]) + (t[2] + t[3])) + ((t[4] + t[5]) + (t[6] + t[7]));
  float s2 = (fmaf(t[1], t[1], t[0] * t[0]) + fmaf(t[3], t[3], t[2] * t[2])) + (fmaf(t[5], t[5], t[4] * t[4]) + fmaf(t[7], t[7], t[6] * t[6]));
  s1 += dpp<XOR1>(s1); s2 += dpp<XOR1>(s2);
  s1 += dpp<XOR2>(s1); s2 += dpp<XOR2>(s2);
  s1 += dpp<ROR4>(s1); s2 += dpp<ROR4>(s2);
  s1 += dpp<ROR8>(s1); s2 += dpp<ROR8>(s2);
  s1 = x16_sum(s1); s2 = x16_sum(s2);
  s1 = x32_sum(s1); s2 = x32_sum(s2);
  const float d = s1 * (1.f / DM), var = fmaxf(fmaf(-d, d, s2 * (1.f / DM)), 0.f);
  return f32x2{c0 + d, __builtin_amdgcn_rsqf(var + 1e-5f)};
}
#define STAMP(k) do { if (PROF == 1) { __builtin_amdgcn_sched_barrier(0); ts[k] = (unsigned)rt(); __builtin_amdgcn_sched_barrier(0); } } while (0)
#define STAMPE(k) do { __builtin_amdgcn_sched_barrier(0); ts[k] = (unsigned)rt(); __builtin_amdgcn_sched_barrier(0); } while (0)
#define LDP(u, k) __hip_atomic_load((GAS unsigned*)(ws + WS_PROF + (size_t)(u) * 64 + (k) * 4), __ATOMIC_RELAXED, __HIP_MEMORY_SCOPE_AGENT)
#define PDUMP() do { until(tstart, 2500); if (lane == 0) { _Pragma("unroll") for (int k_ = 0; k_ < 12; ++k_) __hip_atomic_store((GAS unsigned*)(ws + WS_PROF + (((size_t)c * 16 + w) * 16 + k_) * 4), ts[k_], __ATOMIC_RELAXED, __HIP_MEMORY_SCOPE_AGENT); } \
  if (c == 0 && w == 11) { until(tstart, 4000); int mn = 0, mx = 0; \
    for (int u = lane; u < 4096; u += 64) mn = min(mn, (int)(LDP(u, 0) - (unsigned)tstart)); \
    for (int m_ = 32; m_ >= 1; m_ >>= 1) mn = min(mn, __shfl_xor(mn, m_)); \
    for (int u = lane; u < 4096; u += 64) { const int ww = u & 15, ii = u >> 7; const unsigned tk_ = LDP(u, 11), ta_ = LDP(u, 0); \
      if (ii >= 0 && ii <= 23 && ww >= 11 && ww <= 15 && tk_ != 0u) { const int v_ = (int)tk_ * 10; mx += v_; } } \
    for (int m_ = 32; m_ >= 1; m_ >>= 1) mx += __shfl_xor(mx, m_); \
    mn = __builtin_amdgcn_readfirstlane(mn); mx = __builtin_amdgcn_readfirstlane(mx); \
    mx = mx < 0 ? 0 : mx > 3000 ? 3000 : mx; \
    until(tstart, (unsigned)(7000 + mn + 10 * mx)); } } while (0)
#define RETRY() ts[11]++

__device__ __forceinline__ void tr4(const v2u* s, v4u* o) {
#pragma unroll
  for (int m = 0; m < 4; ++m) {
    o[0][m] = plo(s[2 * m].x, s[2 * m + 1].x); o[1][m] = phi(s[2 * m].x, s[2 * m + 1].x);
    o[2][m] = plo(s[2 * m].y, s[2 * m + 1].y); o[3][m] = phi(s[2 * m].y, s[2 * m + 1].y);
  }
#pragma unroll
  for (int e = 0; e < 4; ++e) PINV(o[e]);
}
__device__ __forceinline__ void tr8(const v4u* s, v4u* o) {
#pragma unroll
  for (int m = 0; m < 4; ++m)
#pragma unroll
    for (int d = 0; d < 4; ++d) { o[2 * d][m] = plo(s[2 * m][d], s[2 * m + 1][d]); o[2 * d + 1][m] = phi(s[2 * m][d], s[2 * m + 1][d]); }
#pragma unroll
  for (int e = 0; e < 8; ++e) PINV(o[e]);
}

__global__ void __launch_bounds__(1024) fused_decode(const u16* __restrict__ x, const u16* __restrict__ kc, const u16* __restrict__ vc,
                                                     const u16* __restrict__ wq, const u16* __restrict__ wo, const u16* __restrict__ wg,
                                                     const u16* __restrict__ wu, const u16* __restrict__ wd, u16* __restrict__ out, unsigned char* ws) {
  const u64 tstart = rt();
  unsigned ts[12];
#pragma unroll
  for (int k_ = 0; k_ < 12; ++k_) ts[k_] = 0;
  ts[0] = (unsigned)tstart;
  SCHED();
  __shared__ __attribute__((aligned(16))) unsigned char smem[L_TOTAL];
  LAS unsigned char* L = (LAS unsigned char*)smem;
  volatile LAS unsigned* FL = (volatile LAS unsigned*)(L + L_FLAG);
  const int lane = threadIdx.x & 63;
  int w = (__builtin_amdgcn_readfirstlane(threadIdx.x >> 6) + 11) & 15;
  const int c = blockIdx.x, g = c & 7, i = c >> 3;
  if (w == 11) code_fetch_and_barrier(i);
  *(LAS v2u*)(L + (w == 11 ? L_USED : L_FLAG) + lane * 8) = v2u{0u, 0u};
  const int gq = lane >> 4, r16 = lane & 15;
  const int b = (i * 43) >> 8, s = i - b * NSPL;
  constexpr int ntile = 11;
  const int row0 = 344 * s - (s > 4 ? 8 : 0);
  const int cw = w - 12, rq = i & 3, cq = i >> 2, R0 = 128 * rq + 32 * cw;
  asm volatile("" :: "s"(x), "s"(kc), "s"(vc), "s"(wq), "s"(wo), "s"(wg), "s"(wu), "s"(wd), "s"(out), "s"(ws) : "memory");
  v4u kf[8], vf[8], xa, xr;
  v2u wsrc[8];
  const unsigned kvbase = ((unsigned)(b * HKV + g) * SK + (SK - WIN) + row0 + 16u * w) * 256u, kvbase2 = kvbase + ntile * 4096u;
  const v4i rk = mkd(kc, NB * HKV * SK * 256u), rv = mkd(vc, NB * HKV * SK * 256u);
  if (w >= 12) {
    __builtin_amdgcn_s_setprio(3);
    const v4i rw = mkd(wq, DM * NQ * 2u), rxd = mkd(x, NB * DM * 2u);
    const unsigned o = (unsigned)(8 * gq) * (NQ * 2) + r16 * 8, so = R0 * (NQ * 2) + (512 * g + 64 * cq) * 2;
#pragma unroll
    for (int j = 0; j < 8; ++j) ldnt2(wsrc[j], o, rw, so + j * (NQ * 2));
    ldp4(xa, (r16 >> 2) * 1024 + (R0 + 8 * gq) * 2, rxd);
    ldp4(xr, cw * 1024 + lane * 16, rxd);
    WGBAR();
  } else if (w != 11) {
    WGBAR();
    if (i < 24) {
      __builtin_amdgcn_s_setprio(2);
      until(rt(), TK0);
      ldtile<0, KPRE>(kf, r16 * 64 + gq * 16, rk, kvbase, kvbase2);
    }
  }
  STAMP(1);
  PINS(w);
  const rsrc_t rws = mk(ws, WS_END), rx = mk(x, NB * DM * 2u);
  const f32x4 Z4 = {0.f, 0.f, 0.f, 0.f};
  const v4u SV = {SENT, SENT, SENT, SENT};
  const unsigned orec = WS_OREC + (g * 24 + i) * OREC_SZ;

  if (__builtin_expect(w >= 12, 1)) {
    const unsigned tg = (unsigned)load_tag(ws);
    asm volatile("s_waitcnt vmcnt(0)" : "+v"(wsrc[0]), "+v"(wsrc[1]), "+v"(wsrc[2]), "+v"(wsrc[3]), "+v"(wsrc[4]), "+v"(wsrc[5]), "+v"(wsrc[6]), "+v"(wsrc[7]), "+v"(xa), "+v"(xr) :: "memory");
    {
      v4u bt[4];
      tr4(wsrc, bt);
      if ((lane & 3) == 1) xa = v4u{ONE2, ONE2, ONE2, ONE2};
      f32x4 qp, cs;
#pragma unroll
      for (int e = 0; e < 4; ++e) { const f32x4 d = mfma(xa, bt[e], Z4); qp[e] = d[0]; cs[e] = d[1]; }
      *(LAS f32x4*)(L + L_QRED + cw * 1024 + lane * 16) = qp;
      if (lane < 16) *(LAS f32x4*)(L + L_QRED + 4096 + cw * 256 + lane * 16) = cs;
    }
    lds_set(FL + F_QR + cw);
    f32x2 xst = ln_stats(xr);
    PINV(xst);
    STAMP(2);
    lds_wait(FL + F_QR + (lane & 3));
    {
      float a[4], cs[4];
#pragma unroll
      for (int j = 0; j < 4; ++j) { a[j] = *(const LAS float*)(L + L_QRED + j * 1024 + cw * 256 + lane * 4); cs[j] = *(const LAS float*)(L + L_QRED + 4096 + j * 256 + lane * 4); }
      const float q = xst.y * (((a[0] + a[1]) + (a[2] + a[3])) - xst.x * ((cs[0] + cs[1]) + (cs[2] + cs[3])));
      const unsigned q2 = pk(q, dpp<XOR1>(q));
      if ((lane & 1) == 0) ST64((v2u{q2, tg}), WS_QG + (((g * NB + cw) * 4 + rq) * 256 + 32 * cq) * 8 + lane * 4);
    }
    lds_set(FL + F_QPUB + cw);
    STAMP(3);
    if (i < 24) {
      until(rt(), QDELTA);
      const unsigned qo = WS_QG + ((g * NB + b) * 4 * 256 + cw * 64 + lane) * 8;
      v2u qg[4];
      GSPIN {
        CBAR();
#pragma unroll
        for (int j = 0; j < 4; ++j) qg[j] = LD64(qo, j * 2048);
        SCHED();
        lds_set(FL + F_QIS + cw);
        SCHED();
        if (__all((qg[0].y == tg) & (qg[1].y == tg) & (qg[2].y == tg) & (qg[3].y == tg))) break;
        RETRY();
      }
      const float q0 = (blo(qg[0].x) + blo(qg[1].x)) + (blo(qg[2].x) + blo(qg[3].x)), q1 = (bhi(qg[0].x) + bhi(qg[1].x)) + (bhi(qg[2].x) + bhi(qg[3].x));
      *(LAS unsigned*)(L + L_QS + (cw * 64 + lane) * 4) = pk(q0, q1);
      lds_set(FL + F_QS + cw);
      STAMP(4);
      PDUMP();
      return;
    }
    __builtin_amdgcn_s_setprio(0);
  } else if (i < 24) {
    if (w < ntile) {
      const int nval = w < 10 ? 8 : s < 4 ? 6 : 4;
      const unsigned oob6 = nval < 6 ? 0x80000000u : 0u, oob8 = nval < 8 ? 0x80000000u : 0u;
      static_assert(KPRE == 4, "");
      if (KMID >= 1) { asm volatile("s_waitcnt vmcnt(%0)" :: "i"(KLEFT) : "memory"); ldtile<4, 6>(kf, (r16 * 64 + gq * 16) | oob6, rk, kvbase, kvbase2); }
      if (KMID >= 2) { asm volatile("s_waitcnt vmcnt(%0)" :: "i"(KLEFT) : "memory"); ldtile<6, 8>(kf, (r16 * 64 + gq * 16) | oob8, rk, kvbase, kvbase2); }
      lds_wait(FL + F_QPUB + (lane & 3));
      if (KMID < 1) ldtile<4, 6>(kf, (r16 * 64 + gq * 16) | oob6, rk, kvbase, kvbase2);
      if (KMID < 2) ldtile<6, 8>(kf, (r16 * 64 + gq * 16) | oob8, rk, kvbase, kvbase2);
      if (VPRE) ldtile<0, VPRE>(vf, lane * 16, rv, kvbase, kvbase2);
      __builtin_amdgcn_s_setprio(0);
      lds_wait(FL + F_QIS + (lane & 3));
      ldtile<VPRE, 4>(vf, lane * 16, rv, kvbase, kvbase2);
      ldtile<4, 6>(vf, (lane * 16) | oob6, rv, kvbase, kvbase2);
      ldtile<6, 8>(vf, (lane * 16) | oob8, rv, kvbase, kvbase2);
      STAMP(2);
      lds_wait(FL + (lane < 4 ? F_QS + lane : F_CL));
      asm volatile("s_waitcnt vmcnt(8)" : "+v"(kf[0]), "+v"(kf[1]), "+v"(kf[2]), "+v"(kf[3]), "+v"(kf[4]), "+v"(kf[5]), "+v"(kf[6]), "+v"(kf[7]) :: "memory");
      STAMP(3);
      const v4u qf = *(const LAS v4u*)(L + L_QS + r16 * 64 + gq * 16);
      float sc[8];
      f32x4 dd[8];
#pragma unroll
      for (int t = 0; t < 8; ++t) dd[t] = mfma(kf[t], qf, Z4);
      SCHED();
#pragma unroll
      for (int t = 0; t < 8; ++t) {
        const f32x4 d = dd[t];
        sc[t] = (dpp<0x00>(d[0]) + dpp<0x55>(d[1])) + (dpp<0xAA>(d[2]) + dpp<0xFF>(d[3]));
        if (t >= 4 && t >= nval) sc[t] = -__builtin_inff();
      }
      float m = fmaxf(fmaxf(fmaxf(sc[0], sc[1]), fmaxf(sc[2], sc[3])), fmaxf(fmaxf(sc[4], sc[5]), fmaxf(sc[6], sc[7])));
      m = x32_max(x16_max(m));
      constexpr float C2 = 0.08838834764831845f * 1.4426950408889634f;
      const float ms = m * C2;
      float p[8], l = 0.f;
#pragma unroll
      for (int t = 0; t < 8; ++t) { p[t] = __builtin_amdgcn_exp2f(fmaf(sc[t], C2, -ms)); l += p[t]; }
      l = x32_sum(x16_sum(l));
      if ((lane & 0x33) == 0) *(LAS f32x2*)(L + L_MW + ((lane >> 2) * 12 + w) * 8) = f32x2{ms, l};
      lds_set(FL + F_MW + w);
      v4u pf = {pk(p[0], p[1]), pk(p[2], p[3]), pk(p[4], p[5]), pk(p[6], p[7])};
      unsigned pa = L_PART + w * 2048 + gq * 512 + r16 * 32;
      PINV(pf); PINV(pa);
      STAMP(4);
      v4u vt[8];
#pragma unroll
      for (int mm = 0; mm < 4; ++mm) {
        if (mm == VEV) lds_set(FL + F_VL + w);
        if (mm == 0) VMWAIT2(6, vf[0], vf[1]);
        if (mm == 1) VMWAIT2(4, vf[2], vf[3]);
        if (mm == 2) VMWAIT2(2, vf[4], vf[5]);
        if (mm == 3) { VMWAIT2(0, vf[6], vf[7]); STAMP(5); }
#pragma unroll
        for (int d = 0; d < 4; ++d) { vt[2 * d][mm] = plo(vf[2 * mm][d], vf[2 * mm + 1][d]); vt[2 * d + 1][mm] = phi(vf[2 * mm][d], vf[2 * mm + 1][d]); }
        if (mm < 3) {
#pragma unroll
          for (int e = 0; e < 8; ++e) PINV(vt[e]);
        }
        SCHED();
      }
      float o[8];
#pragma unroll
      for (int e = 0; e < 8; ++e) o[e] = mfma(pf, vt[e], Z4)[0];
      *(LAS f32x4*)(L + pa) = f32x4{o[0], o[1], o[2], o[3]};
      *(LAS f32x4*)(L + pa + 16) = f32x4{o[4], o[5], o[6], o[7]};
      lds_set(FL + F_OPF + w);
      STAMP(6);
      if (w < 4) {
        const int jl = r16 < ntile ? r16 : 0;
        lds_wait(FL + F_MW + jl);
        const f32x2 ml = *(const LAS f32x2*)(L + L_MW + (w * 12 + jl) * 8);
        const float mg = row_max(ml.x);
        const float wt = r16 < ntile ? __builtin_amdgcn_exp2f(ml.x - mg) : 0.f;
        const float lt = row_sum(wt * ml.y);
        float wj[11];
#pragma unroll
        for (int j = 0; j < 11; ++j) { wj[j] = __builtin_bit_cast(float, __builtin_amdgcn_readlane(__builtin_bit_cast(int, wt), j)); PINS(wj[j]); }
        if (lane == 0) ST64((v2u{nosent(__float_as_uint(mg)), nosent(__float_as_uint(lt))}), orec + 1024 + w * 8);
        unsigned ra = L_PART + w * 512 + lane * 8, so = orec + w * 256 + lane * 4;
        PINV(ra); PINV(so);
        lds_wait(FL + F_OPF + jl);
        f32x2 pv[11];
#pragma unroll
        for (int j = 0; j < 11; ++j) pv[j] = *(const LAS f32x2*)(L + ra + j * 2048);
        SCHED();
        f32x2 a = pv[0] * wj[0], a2 = pv[1] * wj[1];
#pragma unroll
        for (int j = 2; j < 10; j += 2) { a += pv[j] * wj[j]; a2 += pv[j + 1] * wj[j + 1]; }
        a += pv[10] * wj[10];
        a += a2;
        const unsigned v = nosent(pk(a.x, a.y));
        const v4u o4 = {dppu<0x00>(v), dppu<0x55>(v), dppu<0xAA>(v), dppu<0xFF>(v)};
        if ((lane & 3) == 0) ST128(o4, so);
        STAMP(7);
      }
    } else if (w == 11) {
      __builtin_amdgcn_s_setprio(3);
      ST128(SV, orec + lane * 16);
      if (lane < 8) ST128(SV, orec + 1024 + lane * 16);
      const u64 tag = load_tag(ws);
      const unsigned taghi = (unsigned)(tag >> 16), taglo = (unsigned)(tag << 16);
      const bool isR = i < 8;
      const int ln = g * 8 + i, rb = ln >> 4, pg = lane >> 3, q4 = lane & 7, col = 32 * (ln & 15) + 4 * q4;
      v2u xv = {0u, 0u};
      if (isR) xv = __builtin_amdgcn_raw_buffer_load_b64(rx, (rb * DM + col) * 2, 0, 0);
      DRAIN();
      lds_set(FL + F_CL);
      STAMP(3);
      lds_wait(FL + F_VL + (lane < ntile ? lane : 0));
      if (lane == 0) ST64(hint(taglo, taghi), WS_HA + (g * 32 + i) * 128);
      STAMPE(4);
      if (isR) {
        const rsrc_t ro = mk(out, NB * DM * 2u);
        unsigned pf = __builtin_amdgcn_raw_buffer_load_b32(ro, 0, 0, AUX_SC1);
        asm volatile("" :: "v"(pf));
        v2u hb;
        GSPIN {
          CBAR();
          hb = LD64(WS_HB + g * 4096 + ((lane & 7) * 4 + ((ln >> 2) & 3)) * 128, 0);
          if (__all(hint_ok(hb, taglo, taghi))) break;
          __builtin_amdgcn_s_sleep(2);
        }
        STAMP(5);
        wait_age(hb.x, DLY3 + 20);
        v2u hv;
        GSPIN {
          CBAR();
          hv = LD64(WS_HP + (pg * NB + rb) * 1024 + col * 2, 0);
          if (__all(max(hv.x, hv.y) != SENT)) break;
        }
        STAMP(6);
        float h[4] = {blo(hv.x), bhi(hv.x), blo(hv.y), bhi(hv.y)};
#pragma unroll
        for (int d = 0; d < 4; ++d) { h[d] += dpp<ROR8>(h[d]); h[d] = x32_sum(x16_sum(h[d])); }
        {
          const unsigned a0 = pk(h[0], h[1]), a1 = pk(h[2], h[3]);
          const unsigned h0 = pk(blo(xv.x) + blo(a0), bhi(xv.x) + bhi(a0)), h1 = pk(blo(xv.y) + blo(a1), bhi(xv.y) + bhi(a1));
          h[0] = blo(h0); h[1] = bhi(h0); h[2] = blo(h1); h[3] = bhi(h1);
        }
        unsigned po = WS_OP + pg * 8192 + ln * 128 + q4 * 16, po3 = pg < 4 ? po + 24 * 8192 : po, oo = (rb * DM + col) * 2;
        PINV(po); PINV(po3); PINV(oo); PINV(h[0]); PINV(h[1]); PINV(h[2]); PINV(h[3]);
        v2u hc;
        GSPIN {
          CBAR();
          hc = LD64(WS_HC + g * 4096 + (lane < NDCU ? lane : 0) * 128, 0);
          if (__all(hint_ok(hc, taglo, taghi))) break;
        }
        wait_age(hc.x, DLY5);
        STAMP(7);
        v4u pv[4];
        GSPIN {
          CBAR();
#pragma unroll
          for (int t = 0; t < 3; ++t) pv[t] = LD128(po, t * 8 * 8192);
          pv[3] = LD128(po3, 0);
          if (__all(okv(pv[0]) & okv(pv[1]) & okv(pv[2]) & okv(pv[3]))) break;
          RETRY();
        }
        STAMP(8);
        float y[4];
#pragma unroll
        for (int d = 0; d < 4; ++d) {
          float t = (__uint_as_float(pv[0][d]) + __uint_as_float(pv[1][d])) + (__uint_as_float(pv[2][d]) + (pg < 4 ? __uint_as_float(pv[3][d]) : 0.f));
          t += dpp<ROR8>(t);
          y[d] = x32_sum(x16_sum(t));
        }
        const unsigned y0 = pk(y[0], y[1]), y1 = pk(y[2], y[3]);
        if (lane < 8) __builtin_amdgcn_raw_buffer_store_b64(v2u{pk(h[0] + blo(y0), h[1] + bhi(y0)), pk(h[2] + blo(y1), h[3] + bhi(y1))}, ro, oo, 0, AUX_SC1);
        if (ln == 0 && lane == 0) ST64((v2u{(unsigned)tag, (unsigned)(tag >> 32)}), WS_EPOCH);
        STAMPE(9);
      }
    }
    PDUMP();
    return;
  }

  const int jq = i - 24;
  const bool isC = jq < 4;
  const int dI = g + 8 * (jq - 4);
  if (!isC && dI >= NDCU) { PDUMP(); return; }
  const u64 tag = load_tag(ws);
  const unsigned taghi = (unsigned)(tag >> 16), taglo = (unsigned)(tag << 16);
#define GATHER_H() do { \
    unsigned klo = 0x00003F80u, khi = 0x3F800000u, ho = WS_HP + w * 256 + lane * 4, oa = L_OA + w * 256 + apos(lane) * 4; \
    unsigned hp[8] = {0u, 0u, 0u, 0u, 0u, 0u, 0u, 0u}; \
    float x0 = blo(xp), x1 = bhi(xp); \
    PINS(klo); PINS(khi); PINV(oa); PINV(x0); PINV(x1); \
    ask8(hp, (unsigned)(size_t)(FL + F_GO + (lane & 7)), ho, mkd(ws, WS_END)); \
    if (!__all(max(max(max(hp[0], hp[1]), max(hp[2], hp[3])), max(max(hp[4], hp[5]), max(hp[6], hp[7]))) != SENT)) { \
      GSPIN { \
        RETRY(); \
        CBAR(); \
        _Pragma("unroll") for (int j = 0; j < 8; ++j) hp[j] = LD32(ho, j * 4096); \
        if (__all(max(max(max(hp[0], hp[1]), max(hp[2], hp[3])), max(max(hp[4], hp[5]), max(hp[6], hp[7]))) != SENT)) break; \
      } \
      DRAIN(); \
    } \
    STAMP(7); \
    float a0 = 0.f, a1 = 0.f, a2 = 0.f, a3 = 0.f; \
    _Pragma("unroll") for (int j = 0; j < 4; ++j) { a0 = dot2(hp[j], klo, a0); a1 = dot2(hp[j], khi, a1); a2 = dot2(hp[4 + j], klo, a2); a3 = dot2(hp[4 + j], khi, a3); } \
    const unsigned t0 = pk(a0 + a2, a1 + a3); \
    *(LAS unsigned*)(L + oa) = pk(x0 + blo(t0), x1 + bhi(t0)); \
    CBAR(); \
    if (w == 10) ST64(hint(taglo, taghi), WS_HC + (lane & 7) * 4096 + dI * 128); \
  } while (0)
  unsigned xp = 0;
  if (!isC) xp = __builtin_amdgcn_raw_buffer_load_b32(rx, w * 256 + lane * 4, 0, 0);
  if (w < 8) {
    const unsigned stride = isC ? DM * 2 : FF * 2;
    const GAS unsigned char* p = isC ? (const GAS unsigned char*)wo + (size_t)(512 * g) * (DM * 2) + (128 * jq + 8 * r16) * 2
                                     : (const GAS unsigned char*)(r16 < 8 ? wg : wu) + (64 * dI + 8 * (r16 & 7)) * 2;
    p += (size_t)(64 * w + 8 * gq) * stride;
    unsigned aa = L_OA + (r16 >> 2) * 1024 + w * 128 + gq * 32 + (lane & 1) * 16, pa = L_PART + w * 2048 + gq * 512 + r16 * 32;
    int sidx = (lane & 1) ? 0xEEEE : 0x4444;
    PINV(aa); PINV(pa); PINV(sidx);
    v4u src[16], bt[16];
    until(tstart, isC ? TC0 : TD0);
#pragma unroll
    for (int j = 0; j < WPRE; ++j) src[j] = __builtin_nontemporal_load((const GAS v4u*)(p + ((j >> 3) * 32 + (j & 7)) * stride));
    lds_wait(FL + F_QPUB + (lane & 3));
#pragma unroll
    for (int j = WPRE; j < 16; ++j) src[j] = __builtin_nontemporal_load((const GAS v4u*)(p + ((j >> 3) * 32 + (j & 7)) * stride));
    SCHED();
    STAMP(4);
    tr8(src, bt);
    tr8(src + 8, bt + 8);
    if (!isC) {
      const v4u ones = {ONE2, ONE2, ONE2, ONE2};
      float cs[8];
#pragma unroll
      for (int e = 0; e < 8; ++e) cs[e] = mfma(ones, bt[8 + e], mfma(ones, bt[e], Z4))[0];
      if (lane < 16) {
        *(LAS f32x4*)(L + L_SS + w * 512 + lane * 32) = f32x4{cs[0], cs[1], cs[2], cs[3]};
        *(LAS f32x4*)(L + L_SS + w * 512 + lane * 32 + 16) = f32x4{cs[4], cs[5], cs[6], cs[7]};
      }
      lds_set(FL + F_SR + w);
    }
    v8u b8[8];
    f32x4 acc[8];
#pragma unroll
    for (int e = 0; e < 8; ++e) { b8[e] = cat(bt[e], bt[8 + e]); PINV(b8[e]); acc[e] = Z4; PINV(acc[e]); }
    DRAIN();
    STAMP(5);
    if (w == 0) {
      if (isC) {
        const int hl = lane < 24 ? lane : 0;
        watch(rws, WS_HA + (g * 32 + hl) * 128, taglo, taghi, FL + F_GO + hl);
      } else {
        watch8(rws, WS_HB + g * 4096 + (lane & 31) * 128, taglo, taghi, DLY3, FL + F_GO, lane);
      }
      STAMP(10);
      DRAIN();
    } else if (w == 1 && isC) {
      lds_wait(FL + F_SR + (lane & 7));
      if (lane < 8) ST64(hint(taglo, taghi), WS_HB + lane * 4096 + (g * 4 + jq) * 128);
    }
    if (!isC) GATHER_H();
    WGBAR();
    STAMP(8);
    {
      const v4u a0 = *(const LAS v4u*)(L + aa);
#pragma unroll
      for (int e = 0; e < 8; ++e) acc[e] = mfma64(a0, b8[e], acc[e], sidx);
      SCHED();
#pragma unroll
      for (int h = 0; h < 2; ++h)
        *(LAS f32x4*)(L + pa + 16 * h) = f32x4{acc[4 * h][0] + acc[4 * h][1], acc[4 * h + 1][0] + acc[4 * h + 1][1], acc[4 * h + 2][0] + acc[4 * h + 2][1], acc[4 * h + 3][0] + acc[4 * h + 3][1]};
    }
    STAMP(9);
    WGBAR();
    if (!isC) WGBAR();
  } else if (isC) {
    const int v = w & 7, bb = v >> 1, head = 2 * (v & 1) + (lane >> 5), sg = gq & 1;
    unsigned ra = L_PART + (w & 3) * 512 + lane * 8, so = WS_HP + (g * NB + (w & 3)) * 1024 + jq * 256 + lane * 4;
    unsigned rd = WS_OREC + (g * 24 + bb * NSPL + sg) * OREC_SZ + head * 256 + r16 * 16, rs = WS_OREC + (g * 24 + bb * NSPL + sg) * OREC_SZ + 1024 + head * 8;
    unsigned oa = L_OA + bb * 1024 + head * 256 + apos(4 * r16 + 2 * sg) * 4;
    PINV(ra); PINV(so); PINV(rd); PINV(rs); PINV(oa);
    if (w == 15) ST128(SV, WS_HP + (g * NB + gq) * 1024 + jq * 256 + r16 * 16);
    DRAIN();
    STAMP(5);
    {
      unsigned e;
#pragma nounroll
      for (unsigned sp = 0; sp < SPIN_L; ++sp) { e = FL[F_GO + bb * NSPL + (lane < NSPL ? lane : 0)]; if (__all(e != 0u)) break; }
      wait_age(e, DLY2);
    }
    STAMP(6);
    GSPIN {
      v4u od[3];
      v2u st[3];
      CBAR();
#pragma unroll
      for (int u = 0; u < 3; ++u) st[u] = LD64(rs, 2 * u * OREC_SZ);
      SCHED();
#pragma unroll
      for (int u = 0; u < 3; ++u) od[u] = LD128(rd, 2 * u * OREC_SZ);
      SCHED();
      lds_set(FL + F_SR + v);
      SCHED();
      unsigned bad = max(max(max(st[0].x, st[0].y), max(st[1].x, st[1].y)), max(st[2].x, st[2].y));
      const float m0 = __uint_as_float(st[0].x), m1 = __uint_as_float(st[1].x), m2 = __uint_as_float(st[2].x);
      const float mg = x16_max(fmaxf(fmaxf(m0, m1), m2));
      float wt[3] = {__builtin_amdgcn_exp2f(m0 - mg), __builtin_amdgcn_exp2f(m1 - mg), __builtin_amdgcn_exp2f(m2 - mg)};
      const float inv = __builtin_amdgcn_rcpf(x16_sum(fmaf(wt[0], __uint_as_float(st[0].y), fmaf(wt[1], __uint_as_float(st[1].y), wt[2] * __uint_as_float(st[2].y)))));
#pragma unroll
      for (int u = 0; u < 3; ++u) { wt[u] *= inv; PINV(wt[u]); }
      PINV(bad);
      SCHED();
      f32x2 a[4];
#pragma unroll
      for (int u = 0; u < 3; ++u) {
        f32x2 t[4];
#pragma unroll
        for (int d = 0; d < 4; ++d) { t[d] = f32x2{blo(od[u][d]), bhi(od[u][d])}; PINV(t[d]); }
        bad = max(bad, max(max(od[u].x, od[u].y), max(od[u].z, od[u].w)));
#pragma unroll
        for (int d = 0; d < 4; ++d) a[d] = u == 0 ? t[d] * wt[0] : t[d] * wt[u] + a[d];
      }
      float r[4];
#pragma unroll
      for (int d = 0; d < 4; ++d) {
        const auto sw = __builtin_amdgcn_permlane16_swap(__float_as_uint(a[d >> 1][d & 1]), __float_as_uint(a[2 + (d >> 1)][d & 1]), false, false);
        r[d] = __uint_as_float(sw[0]) + __uint_as_float(sw[1]);
      }
      *(LAS unsigned*)(L + oa) = pk(r[0], r[1]);
      *(LAS unsigned*)(L + oa + 16) = pk(r[2], r[3]);
      STAMP(7);
      if (__all(bad != SENT)) break;
      RETRY();
    }
    DRAIN();
    STAMP(8);
    WGBAR();
    WGBAR();
    if (w >= 12) {
      f32x2 pv[8];
#pragma unroll
      for (int j = 0; j < 8; ++j) pv[j] = *(const LAS f32x2*)(L + ra + j * 2048);
      SCHED();
      const f32x2 t = ((pv[0] + pv[1]) + (pv[2] + pv[3])) + ((pv[4] + pv[5]) + (pv[6] + pv[7]));
      const unsigned vv = nosent(pk(t.x, t.y));
      const v4u o4 = {dppu<0x00>(vv), dppu<0x55>(vv), dppu<0xAA>(vv), dppu<0xFF>(vv)};
      if ((lane & 3) == 0) ST128(o4, so);
      STAMPE(9);
    }
  } else {
    unsigned ra = L_PART + (w & 3) * 512 + lane * 4, hw = L_HH + (w & 3) * 128 + apos(lane >> 1) * 4 + (lane & 1) * 2, ha = L_HH + (r16 >> 2) * 128 + gq * 32 + (lane & 1) * 16;
    int sidx = (lane & 1) ? 0xEEEE : 0x4444;
    PINV(sidx);
    unsigned so = WS_OP + dI * 8192 + gq * 2048 + (w & 7) * 256 + r16 * 16, sa = L_OA + (w & 3) * 1024 + lane * 16;
    PINV(ra); PINV(hw); PINV(ha); PINV(so); PINV(sa);
    ST128(SV, WS_OP + dI * 8192 + (w - 8) * 1024 + lane * 16);
    until(tstart, TWD);
    const rsrc_t rw = mk(wd, FF * DM * 2u);
    v2u src[16];
    v4u bt[8];
#pragma unroll
    for (int j = 0; j < 16; ++j) src[j] = __builtin_amdgcn_raw_buffer_load_b64(rw, (unsigned)(8 * gq + (j & 3)) * 1024 + (w - 8) * 128 + r16 * 8, (64 * dI + (j >> 3) * 32 + (j & 4)) * 1024, AUX_NT);
    SCHED();
    tr4(src, bt);
    tr4(src + 8, bt + 4);
    v8u b8[4];
    f32x4 acc[4];
#pragma unroll
    for (int e = 0; e < 4; ++e) { b8[e] = cat(bt[e], bt[4 + e]); PINV(b8[e]); acc[e] = Z4; PINV(acc[e]); }
    float sgs = 0.f, sus = 0.f;
    if (w >= 12) {
      lds_wait(FL + F_SR + (lane & 7));
#pragma unroll
      for (int j = 0; j < 8; ++j) { sgs += *(const LAS float*)(L + L_SS + j * 512 + lane * 4); sus += *(const LAS float*)(L + L_SS + j * 512 + 256 + lane * 4); }
    }
    PINV(sgs); PINV(sus);
    DRAIN();
    lds_set(FL + F_CL + w - 8);
    if (w == 10) lds_wait(FL + F_CL + (lane & 7));
    STAMP(5);
    GATHER_H();
    WGBAR();
    if (w >= 12) {
      const f32x2 st = ln_stats(*(const LAS v4u*)(L + sa));
      float kg = st.y * -1.4426950408889634f, cg = st.x * sgs, cu = st.x * sus, ku = st.y;
      PINV(kg); PINV(cg); PINV(cu); PINV(ku);
      WGBAR();
      float gp[8], up[8];
#pragma unroll
      for (int j = 0; j < 8; ++j) { gp[j] = *(const LAS float*)(L + ra + j * 2048); up[j] = *(const LAS float*)(L + ra + j * 2048 + 256); }
      SCHED();
      const float gg = ((gp[0] + gp[1]) + (gp[2] + gp[3])) + ((gp[4] + gp[5]) + (gp[6] + gp[7]));
      const float uu = ((up[0] + up[1]) + (up[2] + up[3])) + ((up[4] + up[5]) + (up[6] + up[7]));
      const float hh = ku * (uu - cu) * __builtin_amdgcn_rcpf(1.f + __builtin_amdgcn_exp2f(kg * (gg - cg)));
      *(LAS u16*)(L + hw) = (u16)pk(hh, hh);
    } else {
      WGBAR();
    }
    WGBAR();
    STAMP(8);
    const v4u a0 = *(const LAS v4u*)(L + ha);
#pragma unroll
    for (int e = 0; e < 4; ++e) acc[e] = mfma64(a0, b8[e], acc[e], sidx);
    SCHED();
    ST128((v4u{nosent(__float_as_uint(acc[0][0] + acc[0][1])), nosent(__float_as_uint(acc[1][0] + acc[1][1])),
               nosent(__float_as_uint(acc[2][0] + acc[2][1])), nosent(__float_as_uint(acc[3][0] + acc[3][1]))}), so);
    STAMPE(9);
  }
  PDUMP();
}
}

extern "C" void kernel_launch(void* const* d_in, const int* in_sizes, int n_in, void* d_out, int out_size, void* d_ws, size_t ws_size, hipStream_t stream) {
  (void)in_sizes; (void)n_in; (void)out_size; (void)ws_size;
  fused_decode<<<dim3(256), dim3(1024), 0, stream>>>((const u16*)d_in[0], (const u16*)d_in[1], (const u16*)d_in[2], (const u16*)d_in[3], (const u16*)d_in[4],
                                                     (const u16*)d_in[6], (const u16*)d_in[7], (const u16*)d_in[8], (u16*)d_out, (unsigned char*)d_ws);
}
```

```cpp
#include <hip/hip_runtime.h>
#include <stdint.h>

namespace {
typedef unsigned short u16;
typedef unsigned long long u64;
typedef unsigned v4u __attribute__((ext_vector_type(4)));
typedef unsigned v2u __attribute__((ext_vector_type(2)));
typedef int v4i __attribute__((ext_vector_type(4)));
typedef float f32x4 __attribute__((ext_vector_type(4)));
typedef float f32x2 __attribute__((ext_vector_type(2)));
typedef short bf16x8 __attribute__((ext_vector_type(8)));
typedef __bf16 bf16x2 __attribute__((ext_vector_type(2)));
typedef __bf16 bf16v8 __attribute__((ext_vector_type(8)));
typedef __bf16 bf16v16 __attribute__((ext_vector_type(16)));
typedef unsigned v8u __attribute__((ext_vector_type(8)));
#define GAS __attribute__((address_space(1)))
#define LAS __attribute__((address_space(3)))
typedef __amdgpu_buffer_rsrc_t rsrc_t;

constexpr int NB = 4, DM = 512, HKV = 8, SK = 8192, WIN = 2048, FF = 1792, NQ = 4096;
constexpr int NSPL = 6, NDCU = 28;
constexpr unsigned TK0 = 10;
constexpr unsigned QDELTA = 70;
constexpr unsigned TC0 = 240;
constexpr unsigned TD0 = 100;
constexpr unsigned TWD = 450;
constexpr unsigned DLY2 = 60;
constexpr unsigned DLY3 = 86;
constexpr unsigned DLY5 = 78;
constexpr int WPRE = 3;
constexpr int AUX_NT = 2, AUX_SC1 = 16;
constexpr unsigned SENT = 0xFFFFFFFFu, ONE2 = 0x3F803F80u;
constexpr int WS_EPOCH = 0, WS_HB = 4096, WS_HC = WS_HB + 8 * 4096, WS_HA = WS_HC + 8 * 4096,
              WS_QG = WS_HA + 8 * 32 * 128;
constexpr int WS_OREC = WS_QG + 8 * 4 * 4 * 256 * 8, OREC_SZ = 1152;
constexpr int WS_HP = WS_OREC + 8 * 24 * OREC_SZ, WS_OP = WS_HP + 8 * 4 * 512 * 2, WS_END = WS_OP + NDCU * 8192;
constexpr int L_FLAG = 0, L_MW = 512, L_QS = 1024, L_OA = L_QS + 1024, L_HH = L_OA + 4096, L_SS = L_HH + 512;
constexpr int L_QRED = L_SS + 4096, L_PART = L_QRED + 5120, L_USED = L_PART + 11 * 2048;
constexpr int L_TOTAL = 84 * 1024;
static_assert(L_USED + 512 <= L_TOTAL, "lds");
enum { F_QIS = 0, F_QS = 4, F_QPUB = 12, F_QR = 16, F_CL = 24, F_GO = 32, F_SR = 72, F_MW = 80, F_VL = 96, F_OPF = 112 };
constexpr int CODE_BYTES = 14000;
constexpr unsigned SPIN_G = 1u << 17, SPIN_L = 1u << 21;

__device__ __forceinline__ u64 rt() { return __builtin_amdgcn_s_memrealtime(); }
__device__ __forceinline__ rsrc_t mk(const void* p, unsigned bytes) { return __builtin_amdgcn_make_buffer_rsrc((void*)p, 0, bytes, 0x00020000); }
__device__ __forceinline__ f32x4 mfma(v4u a, v4u b, f32x4 c) { return __builtin_amdgcn_mfma_f32_16x16x32_bf16(__builtin_bit_cast(bf16x8, a), __builtin_bit_cast(bf16x8, b), c, 0, 0, 0); }
__device__ __forceinline__ v8u cat(v4u b0, v4u b1) { return __builtin_shufflevector(b0, b1, 0, 1, 2, 3, 4, 5, 6, 7); }
__device__ __forceinline__ f32x4 mfma64(v4u a, v8u b, f32x4 c, int idx) {
  return __builtin_amdgcn_smfmac_f32_16x16x64_bf16(__builtin_bit_cast(bf16v8, a), __builtin_bit_cast(bf16v16, b), c, idx, 0, 0);
}
__device__ __forceinline__ unsigned apos(unsigned d) { return (d & ~7u) | ((d & 1u) << 2) | ((d >> 1) & 3u); }
__device__ __forceinline__ unsigned pk(float lo, float hi) { return __builtin_bit_cast(unsigned, __builtin_convertvector(f32x2{lo, hi}, bf16x2)); }
__device__ __forceinline__ float blo(unsigned v) { return __builtin_bit_cast(float, v << 16); }
__device__ __forceinline__ float bhi(unsigned v) { return __builtin_bit_cast(float, v & 0xffff0000u); }
__device__ __forceinline__ unsigned plo(unsigned a, unsigned b) { return __builtin_amdgcn_perm(b, a, 0x05040100u); }
__device__ __forceinline__ unsigned phi(unsigned a, unsigned b) { return __builtin_amdgcn_perm(b, a, 0x07060302u); }
__device__ __forceinline__ unsigned nosent(unsigned v) { return v < 0xFFFFFFFEu ? v : 0xFFFFFFFEu; }
template <int C> __device__ __forceinline__ float dpp(float x) { return __builtin_bit_cast(float, __builtin_amdgcn_mov_dpp(__builtin_bit_cast(int, x), C, 0xf, 0xf, true)); }
template <int C> __device__ __forceinline__ unsigned dppu(unsigned x) { return (unsigned)__builtin_amdgcn_mov_dpp((int)x, C, 0xf, 0xf, true); }
constexpr int XOR1 = 0xB1, XOR2 = 0x4E, ROR4 = 0x124, ROR8 = 0x128;
__device__ __forceinline__ float x16_sum(float x) { auto s = __builtin_amdgcn_permlane16_swap(__float_as_uint(x), __float_as_uint(x), false, false); return __uint_as_float(s[0]) + __uint_as_float(s[1]); }
__device__ __forceinline__ float x32_sum(float x) { auto s = __builtin_amdgcn_permlane32_swap(__float_as_uint(x), __float_as_uint(x), false, false); return __uint_as_float(s[0]) + __uint_as_float(s[1]); }
__device__ __forceinline__ float x16_max(float x) { auto s = __builtin_amdgcn_permlane16_swap(__float_as_uint(x), __float_as_uint(x), false, false); return fmaxf(__uint_as_float(s[0]), __uint_as_float(s[1])); }
__device__ __forceinline__ float x32_max(float x) { auto s = __builtin_amdgcn_permlane32_swap(__float_as_uint(x), __float_as_uint(x), false, false); return fmaxf(__uint_as_float(s[0]), __uint_as_float(s[1])); }
__device__ __forceinline__ float row_sum(float x) { x += dpp<XOR1>(x); x += dpp<XOR2>(x); x += dpp<ROR4>(x); x += dpp<ROR8>(x); return x; }
__device__ __forceinline__ float row_max(float x) { x = fmaxf(x, dpp<XOR1>(x)); x = fmaxf(x, dpp<XOR2>(x)); x = fmaxf(x, dpp<ROR4>(x)); x = fmaxf(x, dpp<ROR8>(x)); return x; }
__device__ __forceinline__ float dot2(unsigned a, unsigned b, float c) { return __builtin_amdgcn_fdot2_f32_bf16(__builtin_bit_cast(bf16x2, a), __builtin_bit_cast(bf16x2, b), c, false); }
#define CBAR() asm volatile("" ::: "memory")
#define SCHED() __builtin_amdgcn_sched_barrier(0)
#define DRAIN() do { asm volatile("s_waitcnt vmcnt(0)" ::: "memory"); __builtin_amdgcn_s_waitcnt(0x0F70); } while (0)
#define PINV(x) asm volatile("" : "+v"(x))
#define PINS(x) asm volatile("" : "+s"(x))
#define WGBAR() do { SCHED(); asm volatile("s_waitcnt lgkmcnt(0)" ::: "memory"); __builtin_amdgcn_s_barrier(); CBAR(); SCHED(); } while (0)
#define GSPIN _Pragma("nounroll") for (unsigned sp = 0; sp < SPIN_G; ++sp)
#define LD32(o, so) __builtin_amdgcn_raw_buffer_load_b32(rws, (o), (so), AUX_SC1)
#define LD64(o, so) __builtin_amdgcn_raw_buffer_load_b64(rws, (o), (so), AUX_SC1)
#define LD128(o, so) __builtin_amdgcn_raw_buffer_load_b128(rws, (o), (so), AUX_SC1)
#define ST64(v, o) __builtin_amdgcn_raw_buffer_store_b64((v), rws, (o), 0, AUX_SC1)
#define ST128(v, o) __builtin_amdgcn_raw_buffer_store_b128((v), rws, (o), 0, AUX_SC1)
__device__ __forceinline__ bool okv(v4u r) { return max(max(r.x, r.y), max(r.z, r.w)) != SENT; }
__device__ __forceinline__ void lds_wait(volatile LAS unsigned* f) {
  SCHED();
#pragma nounroll
  for (unsigned s = 0; s < SPIN_L; ++s) { if (__all(*f != 0u)) break; }
  CBAR();
  SCHED();
}
__device__ __forceinline__ void lds_set(volatile LAS unsigned* f) { CBAR(); *f = 1u; CBAR(); }
__device__ __forceinline__ void until(u64 t0, unsigned ticks) { while (rt() - t0 < ticks) __builtin_amdgcn_s_sleep(1); }
__device__ __forceinline__ v2u hint(unsigned taglo, unsigned taghi) { return v2u{taglo | ((unsigned)rt() & 0xFFFFu), taghi}; }
__device__ __forceinline__ bool hint_ok(v2u h, unsigned taglo, unsigned taghi) { return (h.y == taghi) & ((h.x ^ taglo) < 65536u); }
__device__ __forceinline__ void wait_age(unsigned t16, unsigned dly) {
#pragma nounroll
  for (unsigned s = 0; s < 4096u; ++s) { if (__all((((unsigned)rt() - t16) & 0xFFFFu) >= dly)) break; }
}
__device__ __forceinline__ void watch(rsrc_t rws, unsigned ho, unsigned taglo, unsigned taghi, volatile LAS unsigned* slot) {
  GSPIN {
    CBAR();
    const v2u h = LD64(ho, 0);
    const bool ok = hint_ok(h, taglo, taghi);
    if (ok) *slot = h.x | 0x10000u;
    CBAR();
    if (__all(ok)) break;
  }
}
__device__ __forceinline__ void watch8(rsrc_t rws, unsigned ho, unsigned taglo, unsigned taghi, unsigned dly, volatile LAS unsigned* go, int lane) {
  v2u h;
  unsigned seen = 0, done = 0;
#pragma nounroll
  for (unsigned sp = 0; sp < SPIN_G && done != 0x11111111u; ++sp) {
    if (seen != ~0u) {
      CBAR();
      h = LD64(ho, 0);
      seen = (unsigned)__builtin_amdgcn_ballot_w64(hint_ok(h, taglo, taghi));
    }
    unsigned m = (unsigned)__builtin_amdgcn_ballot_w64((((unsigned)rt() - h.x) & 0xFFFFu) >= dly) & seen;
    m &= m >> 1; m &= m >> 2; m &= 0x11111111u;
    if (m != done) { if ((m >> (4 * (lane & 7))) & 1u) go[lane & 7] = 1u; done = m; }
  }
  CBAR();
}
__device__ __forceinline__ u64 load_tag(const unsigned char* ws) {
  u64 e;
  asm volatile("s_load_dwordx2 %0, %1, 0x0\n\ts_waitcnt lgkmcnt(0)" : "=s"(e) : "s"(ws) : "memory");
  return (e + 1ull) & 0xFFFFFFFFFFFFull;
}
__device__ __forceinline__ void code_fetch_and_barrier(int hw_wave, int part) {
  const unsigned off = part * 512 < CODE_BYTES - 512 ? part * 512 : CODE_BYTES - 512;
  u64 a;
  unsigned t0, t1, t2, t3, t4, t5, t6, t7;
  asm volatile("s_cmp_lg_u32 %9, 0\n\ts_cbranch_scc1 1f\n\t"
               "s_getpc_b64 %8\n\t"
               "s_load_dword %0, %8, %10\n\ts_load_dword %1, %8, %10 offset:0x40\n\ts_load_dword %2, %8, %10 offset:0x80\n\ts_load_dword %3, %8, %10 offset:0xc0\n\t"
               "s_load_dword %4, %8, %10 offset:0x100\n\ts_load_dword %5, %8, %10 offset:0x140\n\ts_load_dword %6, %8, %10 offset:0x180\n\ts_load_dword %7, %8, %10 offset:0x1c0\n\t"
               "s_barrier\n\ts_waitcnt lgkmcnt(0)\n1:"
               : "=&s"(t0), "=&s"(t1), "=&s"(t2), "=&s"(t3), "=&s"(t4), "=&s"(t5), "=&s"(t6), "=&s"(t7), "=&s"(a) : "s"(hw_wave), "s"(off) : "scc", "memory");
}
__device__ __forceinline__ v4i mkd(const void* p, unsigned bytes) { return v4i{(int)(u64)p, (int)((u64)p >> 32) & 0xFFFF, (int)bytes, 0x00020000}; }
template <int OFF> __device__ __forceinline__ void ldnt(v4u& d, unsigned vo, v4i rs, unsigned so) {
  asm volatile("buffer_load_dwordx4 %0, %1, %2, %3 offen offset:%4 nt" : "=v"(d) : "v"(vo), "s"(rs), "s"(so), "i"(OFF) : "memory");
}
template <int LO, int HI> __device__ __forceinline__ void ldtile(v4u* d, unsigned vo, v4i rs, unsigned so, unsigned so2) {
  if constexpr (LO < HI) { ldnt<(LO & 3) * 1024>(d[LO], vo, rs, LO < 4 ? so : so2); ldtile<LO + 1, HI>(d, vo, rs, so, so2); }
}
__device__ __forceinline__ void ldnt2(v2u& d, unsigned vo, v4i rs, unsigned so) {
  asm volatile("buffer_load_dwordx2 %0, %1, %2, %3 offen nt" : "=v"(d) : "v"(vo), "s"(rs), "s"(so) : "memory");
}
__device__ __forceinline__ void ldp4(v4u& d, unsigned vo, v4i rs) {
  asm volatile("buffer_load_dwordx4 %0, %1, %2, 0 offen" : "=v"(d) : "v"(vo), "s"(rs) : "memory");
}
#define VMWAIT2(n, a, b) asm volatile("s_waitcnt vmcnt(%2)" : "+v"(a), "+v"(b) : "i"(n) : "memory")
__device__ __forceinline__ void ask8(unsigned (&hp)[8], unsigned go, unsigned ho, v4i rs) {
  unsigned t, done, n, cnt;
#define ASK1(j, off) "s_bitcmp1_b32 %10, " #j "\n\ts_cbranch_scc0 2" #j "f\n\ts_movk_i32 %11, " #off "\n\tbuffer_load_dword %" #j ", %13, %14, %11 offen sc1\n2" #j ":\n\t"
  asm volatile("s_mov_b32 %9, 0\n\ts_mov_b32 %11, 0x200000\n"
               "1:\n\tds_read_b32 %8, %12\n\ts_sub_u32 %11, %11, 1\n\ts_cbranch_scc1 3f\n\ts_waitcnt lgkmcnt(0)\n\t"
               "v_cmp_ne_u32 vcc, 0, %8\n\ts_andn2_b32 %10, vcc_lo, %9\n\ts_and_b32 %10, %10, 0xff\n\ts_cbranch_scc0 1b\n\t"
               ASK1(0, 0) ASK1(1, 0x1000) ASK1(2, 0x2000) ASK1(3, 0x3000) ASK1(4, 0x4000) ASK1(5, 0x5000) ASK1(6, 0x6000) ASK1(7, 0x7000)
               "s_mov_b32 %11, 0x200000\n\ts_or_b32 %9, %9, %10\n\ts_cmp_lg_u32 %9, 0xff\n\ts_cbranch_scc1 1b\n"
               "3:\n\ts_waitcnt vmcnt(0) lgkmcnt(0)"
               : "+v"(hp[0]), "+v"(hp[1]), "+v"(hp[2]), "+v"(hp[3]), "+v"(hp[4]), "+v"(hp[5]), "+v"(hp[6]), "+v"(hp[7]), "=&v"(t), "=&s"(done), "=&s"(n), "=&s"(cnt)
               : "v"(go), "v"(ho), "s"(rs) : "vcc", "scc", "memory");
#undef ASK1
}
__device__ __forceinline__ f32x2 ln_stats(v4u hv) {
  const float c0 = blo((unsigned)__builtin_amdgcn_readfirstlane(hv[0]));
  float t[8];
#pragma unroll
  for (int d = 0; d < 4; ++d) { t[2 * d] = blo(hv[d]) - c0; t[2 * d + 1] = bhi(hv[d]) - c0; }
  float s1 = ((t[0] + t[1]) + (t[2] + t[3])) + ((t[4] + t[5]) + (t[6] + t[7]));
  float s2 = (fmaf(t[1], t[1], t[0] * t[0]) + fmaf(t[3], t[3], t[2] * t[2])) + (fmaf(t[5], t[5], t[4] * t[4]) + fmaf(t[7], t[7], t[6] * t[6]));
  s1 += dpp<XOR1>(s1); s2 += dpp<XOR1>(s2);
  s1 += dpp<XOR2>(s1); s2 += dpp<XOR2>(s2);
  s1 += dpp<ROR4>(s1); s2 += dpp<ROR4>(s2);
  s1 += dpp<ROR8>(s1); s2 += dpp<ROR8>(s2);
  s1 = x16_sum(s1); s2 = x16_sum(s2);
  s1 = x32_sum(s1); s2 = x32_sum(s2);
  const float d = s1 * (1.f / DM), var = fmaxf(fmaf(-d, d, s2 * (1.f / DM)), 0.f);
  return f32x2{c0 + d, __builtin_amdgcn_rsqf(var + 1e-5f)};
}

__device__ __forceinline__ void tr4(const v2u* s, v4u* o) {
#pragma unroll
  for (int m = 0; m < 4; ++m) {
    o[0][m] = plo(s[2 * m].x, s[2 * m + 1].x); o[1][m] = phi(s[2 * m].x, s[2 * m + 1].x);
    o[2][m] = plo(s[2 * m].y, s[2 * m + 1].y); o[3][m] = phi(s[2 * m].y, s[2 * m + 1].y);
  }
#pragma unroll
  for (int e = 0; e < 4; ++e) PINV(o[e]);
}
__device__ __forceinline__ void tr8(const v4u* s, v4u* o) {
#pragma unroll
  for (int m = 0; m < 4; ++m)
#pragma unroll
    for (int d = 0; d < 4; ++d) { o[2 * d][m] = plo(s[2 * m][d], s[2 * m + 1][d]); o[2 * d + 1][m] = phi(s[2 * m][d], s[2 * m + 1][d]); }
#pragma unroll
  for (int e = 0; e < 8; ++e) PINV(o[e]);
}

__global__ void __launch_bounds__(1024, 4) fused_decode(const u16* __restrict__ x, const u16* __restrict__ kc, const u16* __restrict__ vc,
                                                     const u16* __restrict__ wq, const u16* __restrict__ wo, const u16* __restrict__ wg,
                                                     const u16* __restrict__ wu, const u16* __restrict__ wd, u16* __restrict__ out, unsigned char* ws) {
  const u64 tstart = rt();
  code_fetch_and_barrier(__builtin_amdgcn_readfirstlane(threadIdx.x >> 6), blockIdx.x >> 3);
  __shared__ __attribute__((aligned(16))) unsigned char smem[L_TOTAL];
  LAS unsigned char* L = (LAS unsigned char*)smem;
  volatile LAS unsigned* FL = (volatile LAS unsigned*)(L + L_FLAG);
  const int lane = threadIdx.x & 63;
  int w = (__builtin_amdgcn_readfirstlane(threadIdx.x >> 6) + 11) & 15;
  const int c = blockIdx.x, g = c & 7, i = c >> 3;
  asm volatile("" :: "s"(x), "s"(kc), "s"(vc), "s"(wq), "s"(wo), "s"(wg), "s"(wu), "s"(wd), "s"(out), "s"(ws) : "memory");
  *(LAS v2u*)(L + (w == 11 ? L_USED : L_FLAG) + lane * 8) = v2u{0u, 0u};
  const int gq = lane >> 4, r16 = lane & 15;
  const int b = (i * 43) >> 8, s = i - b * NSPL;
  constexpr int ntile = 11;
  const int row0 = 344 * s - (s > 4 ? 8 : 0);
  const int cw = w - 12, rq = i & 3, cq = i >> 2, R0 = 128 * rq + 32 * cw;
  v4u kf[8], vf[8], xa, xr;
  v2u wsrc[8];
  const unsigned kvbase = ((unsigned)(b * HKV + g) * SK + (SK - WIN) + row0 + 16u * w) * 256u, kvbase2 = kvbase + ntile * 4096u;
  const v4i rk = mkd(kc, NB * HKV * SK * 256u), rv = mkd(vc, NB * HKV * SK * 256u);
  if (w >= 12) {
    __builtin_amdgcn_s_setprio(3);
    const v4i rw = mkd(wq, DM * NQ * 2u), rxd = mkd(x, NB * DM * 2u);
    const unsigned o = (unsigned)(8 * gq) * (NQ * 2) + r16 * 8, so = R0 * (NQ * 2) + (512 * g + 64 * cq) * 2;
#pragma unroll
    for (int j = 0; j < 8; ++j) ldnt2(wsrc[j], o, rw, so + j * (NQ * 2));
    ldp4(xa, (r16 >> 2) * 1024 + (R0 + 8 * gq) * 2, rxd);
    ldp4(xr, cw * 1024 + lane * 16, rxd);
    WGBAR();
  } else if (w != 11) {
    WGBAR();
    if (i < 24) {
      __builtin_amdgcn_s_setprio(2);
      until(rt(), TK0);
      ldtile<0, 4>(kf, r16 * 64 + gq * 16, rk, kvbase, kvbase2);
    }
  }
  PINS(w);
  const rsrc_t rws = mk(ws, WS_END), rx = mk(x, NB * DM * 2u);
  const f32x4 Z4 = {0.f, 0.f, 0.f, 0.f};
  const v4u SV = {SENT, SENT, SENT, SENT};
  const unsigned orec = WS_OREC + (g * 24 + i) * OREC_SZ;

  if (__builtin_expect(w >= 12, 1)) {
    const unsigned tg = (unsigned)load_tag(ws);
    asm volatile("s_waitcnt vmcnt(0)" : "+v"(wsrc[0]), "+v"(wsrc[1]), "+v"(wsrc[2]), "+v"(wsrc[3]), "+v"(wsrc[4]), "+v"(wsrc[5]), "+v"(wsrc[6]), "+v"(wsrc[7]), "+v"(xa), "+v"(xr) :: "memory");
    {
      v4u bt[4];
      tr4(wsrc, bt);
      if ((lane & 3) == 1) xa = v4u{ONE2, ONE2, ONE2, ONE2};
      f32x4 qp, cs;
#pragma unroll
      for (int e = 0; e < 4; ++e) { const f32x4 d = mfma(xa, bt[e], Z4); qp[e] = d[0]; cs[e] = d[1]; }
      *(LAS f32x4*)(L + L_QRED + cw * 1024 + lane * 16) = qp;
      if (lane < 16) *(LAS f32x4*)(L + L_QRED + 4096 + cw * 256 + lane * 16) = cs;
    }
    lds_set(FL + F_QR + cw);
    f32x2 xst = ln_stats(xr);
    PINV(xst);
    lds_wait(FL + F_QR + (lane & 3));
    {
      float a[4], cs[4];
#pragma unroll
      for (int j = 0; j < 4; ++j) { a[j] = *(const LAS float*)(L + L_QRED + j * 1024 + cw * 256 + lane * 4); cs[j] = *(const LAS float*)(L + L_QRED + 4096 + j * 256 + lane * 4); }
      const float q = xst.y * (((a[0] + a[1]) + (a[2] + a[3])) - xst.x * ((cs[0] + cs[1]) + (cs[2] + cs[3])));
      const unsigned q2 = pk(q, dpp<XOR1>(q));
      if ((lane & 1) == 0) ST64((v2u{q2, tg}), WS_QG + (((g * NB + cw) * 4 + rq) * 256 + 32 * cq) * 8 + lane * 4);
    }
    lds_set(FL + F_QPUB + cw);
    if (i < 24) {
      until(rt(), QDELTA);
      const unsigned qo = WS_QG + ((g * NB + b) * 4 * 256 + cw * 64 + lane) * 8;
      v2u qg[4];
      GSPIN {
        CBAR();
#pragma unroll
        for (int j = 0; j < 4; ++j) qg[j] = LD64(qo, j * 2048);
        SCHED();
        lds_set(FL + F_QIS + cw);
        SCHED();
        if (__all((qg[0].y == tg) & (qg[1].y == tg) & (qg[2].y == tg) & (qg[3].y == tg))) break;
      }
      const float q0 = (blo(qg[0].x) + blo(qg[1].x)) + (blo(qg[2].x) + blo(qg[3].x)), q1 = (bhi(qg[0].x) + bhi(qg[1].x)) + (bhi(qg[2].x) + bhi(qg[3].x));
      *(LAS unsigned*)(L + L_QS + (cw * 64 + lane) * 4) = pk(q0, q1);
      lds_set(FL + F_QS + cw);
      return;
    }
    __builtin_amdgcn_s_setprio(0);
  } else if (i < 24) {
    if (w < ntile) {
      const int nval = w < 10 ? 8 : s < 4 ? 6 : 4;
      const unsigned oob6 = nval < 6 ? 0x80000000u : 0u, oob8 = nval < 8 ? 0x80000000u : 0u;
      lds_wait(FL + F_QPUB + (lane & 3));
      ldtile<4, 6>(kf, (r16 * 64 + gq * 16) | oob6, rk, kvbase, kvbase2);
      ldtile<6, 8>(kf, (r16 * 64 + gq * 16) | oob8, rk, kvbase, kvbase2);
      __builtin_amdgcn_s_setprio(0);
      lds_wait(FL + F_QIS + (lane & 3));
      ldtile<0, 4>(vf, lane * 16, rv, kvbase, kvbase2);
      ldtile<4, 6>(vf, (lane * 16) | oob6, rv, kvbase, kvbase2);
      ldtile<6, 8>(vf, (lane * 16) | oob8, rv, kvbase, kvbase2);
      lds_wait(FL + (lane < 4 ? F_QS + lane : F_CL));
      asm volatile("s_waitcnt vmcnt(8)" : "+v"(kf[0]), "+v"(kf[1]), "+v"(kf[2]), "+v"(kf[3]), "+v"(kf[4]), "+v"(kf[5]), "+v"(kf[6]), "+v"(kf[7]) :: "memory");
      const v4u qf = *(const LAS v4u*)(L + L_QS + r16 * 64 + gq * 16);
      float sc[8];
      f32x4 dd[8];
#pragma unroll
      for (int t = 0; t < 8; ++t) dd[t] = mfma(kf[t], qf, Z4);
      SCHED();
#pragma unroll
      for (int t = 0; t < 8; ++t) {
        const f32x4 d = dd[t];
        sc[t] = (dpp<0x00>(d[0]) + dpp<0x55>(d[1])) + (dpp<0xAA>(d[2]) + dpp<0xFF>(d[3]));
        if (t >= 4 && t >= nval) sc[t] = -__builtin_inff();
      }
      float m = fmaxf(fmaxf(fmaxf(sc[0], sc[1]), fmaxf(sc[2], sc[3])), fmaxf(fmaxf(sc[4], sc[5]), fmaxf(sc[6], sc[7])));
      m = x32_max(x16_max(m));
      constexpr float C2 = 0.08838834764831845f * 1.4426950408889634f;
      const float ms = m * C2;
      float p[8], l = 0.f;
#pragma unroll
      for (int t = 0; t < 8; ++t) { p[t] = __builtin_amdgcn_exp2f(fmaf(sc[t], C2, -ms)); l += p[t]; }
      l = x32_sum(x16_sum(l));
      if ((lane & 0x33) == 0) *(LAS f32x2*)(L + L_MW + ((lane >> 2) * 12 + w) * 8) = f32x2{ms, l};
      lds_set(FL + F_MW + w);
      v4u pf = {pk(p[0], p[1]), pk(p[2], p[3]), pk(p[4], p[5]), pk(p[6], p[7])};
      unsigned pa = L_PART + w * 2048 + gq * 512 + r16 * 32;
      PINV(pf); PINV(pa);
      v4u vt[8];
#pragma unroll
      for (int mm = 0; mm < 4; ++mm) {
        if (mm == 2) lds_set(FL + F_VL + w);
        if (mm == 0) VMWAIT2(6, vf[0], vf[1]);
        if (mm == 1) VMWAIT2(4, vf[2], vf[3]);
        if (mm == 2) VMWAIT2(2, vf[4], vf[5]);
        if (mm == 3) VMWAIT2(0, vf[6], vf[7]);
#pragma unroll
        for (int d = 0; d < 4; ++d) { vt[2 * d][mm] = plo(vf[2 * mm][d], vf[2 * mm + 1][d]); vt[2 * d + 1][mm] = phi(vf[2 * mm][d], vf[2 * mm + 1][d]); }
        if (mm < 3) {
#pragma unroll
          for (int e = 0; e < 8; ++e) PINV(vt[e]);
        }
        SCHED();
      }
      float o[8];
#pragma unroll
      for (int e = 0; e < 8; ++e) o[e] = mfma(pf, vt[e], Z4)[0];
      *(LAS f32x4*)(L + pa) = f32x4{o[0], o[1], o[2], o[3]};
      *(LAS f32x4*)(L + pa + 16) = f32x4{o[4], o[5], o[6], o[7]};
      lds_set(FL + F_OPF + w);
      if (w < 4) {
        const int jl = r16 < ntile ? r16 : 0;
        lds_wait(FL + F_MW + jl);
        const f32x2 ml = *(const LAS f32x2*)(L + L_MW + (w * 12 + jl) * 8);
        const float mg = row_max(ml.x);
        const float wt = r16 < ntile ? __builtin_amdgcn_exp2f(ml.x - mg) : 0.f;
        const float lt = row_sum(wt * ml.y);
        float wj[11];
#pragma unroll
        for (int j = 0; j < 11; ++j) { wj[j] = __builtin_bit_cast(float, __builtin_amdgcn_readlane(__builtin_bit_cast(int, wt), j)); PINS(wj[j]); }
        if (lane == 0) ST64((v2u{nosent(__float_as_uint(mg)), nosent(__float_as_uint(lt))}), orec + 1024 + w * 8);
        unsigned ra = L_PART + w * 512 + lane * 8, so = orec + w * 256 + lane * 4;
        PINV(ra); PINV(so);
        lds_wait(FL + F_OPF + jl);
        f32x2 pv[11];
#pragma unroll
        for (int j = 0; j < 11; ++j) pv[j] = *(const LAS f32x2*)(L + ra + j * 2048);
        SCHED();
        f32x2 a = pv[0] * wj[0], a2 = pv[1] * wj[1];
#pragma unroll
        for (int j = 2; j < 10; j += 2) { a += pv[j] * wj[j]; a2 += pv[j + 1] * wj[j + 1]; }
        a += pv[10] * wj[10];
        a += a2;
        const unsigned v = nosent(pk(a.x, a.y));
        const v4u o4 = {dppu<0x00>(v), dppu<0x55>(v), dppu<0xAA>(v), dppu<0xFF>(v)};
        if ((lane & 3) == 0) ST128(o4, so);
      }
    } else if (w == 11) {
      __builtin_amdgcn_s_setprio(3);
      ST128(SV, orec + lane * 16);
      if (lane < 8) ST128(SV, orec + 1024 + lane * 16);
      const u64 tag = load_tag(ws);
      const unsigned taghi = (unsigned)(tag >> 16), taglo = (unsigned)(tag << 16);
      const bool isR = i < 8;
      const int ln = g * 8 + i, rb = ln >> 4, pg = lane >> 3, q4 = lane & 7, col = 32 * (ln & 15) + 4 * q4;
      v2u xv = {0u, 0u};
      if (isR) xv = __builtin_amdgcn_raw_buffer_load_b64(rx, (rb * DM + col) * 2, 0, 0);
      DRAIN();
      lds_set(FL + F_CL);
      lds_wait(FL + F_VL + (lane < ntile ? lane : 0));
      if (lane == 0) ST64(hint(taglo, taghi), WS_HA + (g * 32 + i) * 128);
      if (isR) {
        const rsrc_t ro = mk(out, NB * DM * 2u);
        unsigned pf = __builtin_amdgcn_raw_buffer_load_b32(ro, 0, 0, AUX_SC1);
        asm volatile("" :: "v"(pf));
        v2u hb;
        GSPIN {
          CBAR();
          hb = LD64(WS_HB + g * 4096 + ((lane & 7) * 4 + ((ln >> 2) & 3)) * 128, 0);
          if (__all(hint_ok(hb, taglo, taghi))) break;
          __builtin_amdgcn_s_sleep(2);
        }
        wait_age(hb.x, DLY3 + 20);
        v2u hv;
        GSPIN {
          CBAR();
          hv = LD64(WS_HP + (pg * NB + rb) * 1024 + col * 2, 0);
          if (__all(max(hv.x, hv.y) != SENT)) break;
        }
        float h[4] = {blo(hv.x), bhi(hv.x), blo(hv.y), bhi(hv.y)};
#pragma unroll
        for (int d = 0; d < 4; ++d) { h[d] += dpp<ROR8>(h[d]); h[d] = x32_sum(x16_sum(h[d])); }
        {
          const unsigned a0 = pk(h[0], h[1]), a1 = pk(h[2], h[3]);
          const unsigned h0 = pk(blo(xv.x) + blo(a0), bhi(xv.x) + bhi(a0)), h1 = pk(blo(xv.y) + blo(a1), bhi(xv.y) + bhi(a1));
          h[0] = blo(h0); h[1] = bhi(h0); h[2] = blo(h1); h[3] = bhi(h1);
        }
        unsigned po = WS_OP + pg * 8192 + ln * 128 + q4 * 16, po3 = pg < 4 ? po + 24 * 8192 : po, oo = (rb * DM + col) * 2;
        PINV(po); PINV(po3); PINV(oo); PINV(h[0]); PINV(h[1]); PINV(h[2]); PINV(h[3]);
        v2u hc;
        GSPIN {
          CBAR();
          hc = LD64(WS_HC + g * 4096 + (lane < NDCU ? lane : 0) * 128, 0);
          if (__all(hint_ok(hc, taglo, taghi))) break;
        }
        wait_age(hc.x, DLY5);
        v4u pv[4];
        GSPIN {
          CBAR();
#pragma unroll
          for (int t = 0; t < 3; ++t) pv[t] = LD128(po, t * 8 * 8192);
          pv[3] = LD128(po3, 0);
          if (__all(okv(pv[0]) & okv(pv[1]) & okv(pv[2]) & okv(pv[3]))) break;
        }
        float y[4];
#pragma unroll
        for (int d = 0; d < 4; ++d) {
          float t = (__uint_as_float(pv[0][d]) + __uint_as_float(pv[1][d])) + (__uint_as_float(pv[2][d]) + (pg < 4 ? __uint_as_float(pv[3][d]) : 0.f));
          t += dpp<ROR8>(t);
          y[d] = x32_sum(x16_sum(t));
        }
        const unsigned y0 = pk(y[0], y[1]), y1 = pk(y[2], y[3]);
        if (lane < 8) __builtin_amdgcn_raw_buffer_store_b64(v2u{pk(h[0] + blo(y0), h[1] + bhi(y0)), pk(h[2] + blo(y1), h[3] + bhi(y1))}, ro, oo, 0, AUX_SC1);
        if (ln == 0 && lane == 0) ST64((v2u{(unsigned)tag, (unsigned)(tag >> 32)}), WS_EPOCH);
      }
    }
    return;
  }

  const int jq = i - 24;
  const bool isC = jq < 4;
  const int dI = g + 8 * (jq - 4);
  if (!isC && dI >= NDCU) return;
  const u64 tag = load_tag(ws);
  const unsigned taghi = (unsigned)(tag >> 16), taglo = (unsigned)(tag << 16);
#define GATHER_H() do { \
    unsigned klo = 0x00003F80u, khi = 0x3F800000u, ho = WS_HP + w * 256 + lane * 4, oa = L_OA + w * 256 + apos(lane) * 4; \
    unsigned hp[8] = {0u, 0u, 0u, 0u, 0u, 0u, 0u, 0u}; \
    float x0 = blo(xp), x1 = bhi(xp); \
    PINS(klo); PINS(khi); PINV(oa); PINV(x0); PINV(x1); \
    ask8(hp, (unsigned)(size_t)(FL + F_GO + (lane & 7)), ho, mkd(ws, WS_END)); \
    if (!__all(max(max(max(hp[0], hp[1]), max(hp[2], hp[3])), max(max(hp[4], hp[5]), max(hp[6], hp[7]))) != SENT)) { \
      GSPIN { \
        CBAR(); \
        _Pragma("unroll") for (int j = 0; j < 8; ++j) hp[j] = LD32(ho, j * 4096); \
        if (__all(max(max(max(hp[0], hp[1]), max(hp[2], hp[3])), max(max(hp[4], hp[5]), max(hp[6], hp[7]))) != SENT)) break; \
      } \
      DRAIN(); \
    } \
    float a0 = 0.f, a1 = 0.f, a2 = 0.f, a3 = 0.f; \
    _Pragma("unroll") for (int j = 0; j < 4; ++j) { a0 = dot2(hp[j], klo, a0); a1 = dot2(hp[j], khi, a1); a2 = dot2(hp[4 + j], klo, a2); a3 = dot2(hp[4 + j], khi, a3); } \
    const unsigned t0 = pk(a0 + a2, a1 + a3); \
    *(LAS unsigned*)(L + oa) = pk(x0 + blo(t0), x1 + bhi(t0)); \
    CBAR(); \
    if (w == 10) ST64(hint(taglo, taghi), WS_HC + (lane & 7) * 4096 + dI * 128); \
  } while (0)
  unsigned xp = 0;
  if (!isC) xp = __builtin_amdgcn_raw_buffer_load_b32(rx, w * 256 + lane * 4, 0, 0);
  if (w < 8) {
    const unsigned stride = isC ? DM * 2 : FF * 2;
    const GAS unsigned char* p = isC ? (const GAS unsigned char*)wo + (size_t)(512 * g) * (DM * 2) + (128 * jq + 8 * r16) * 2
                                     : (const GAS unsigned char*)(r16 < 8 ? wg : wu) + (64 * dI + 8 * (r16 & 7)) * 2;
    p += (size_t)(64 * w + 8 * gq) * stride;
    unsigned aa = L_OA + (r16 >> 2) * 1024 + w * 128 + gq * 32 + (lane & 1) * 16, pa = L_PART + w * 2048 + gq * 512 + r16 * 32;
    int sidx = (lane & 1) ? 0xEEEE : 0x4444;
    PINV(aa); PINV(pa); PINV(sidx);
    v4u src[16], bt[16];
    until(tstart, isC ? TC0 : TD0);
#pragma unroll
    for (int j = 0; j < WPRE; ++j) src[j] = __builtin_nontemporal_load((const GAS v4u*)(p + ((j >> 3) * 32 + (j & 7)) * stride));
    lds_wait(FL + F_QPUB + (lane & 3));
#pragma unroll
    for (int j = WPRE; j < 16; ++j) src[j] = __builtin_nontemporal_load((const GAS v4u*)(p + ((j >> 3) * 32 + (j & 7)) * stride));
    SCHED();
    tr8(src, bt);
    tr8(src + 8, bt + 8);
    if (!isC) {
      const v4u ones = {ONE2, ONE2, ONE2, ONE2};
      float cs[8];
#pragma unroll
      for (int e = 0; e < 8; ++e) cs[e] = mfma(ones, bt[8 + e], mfma(ones, bt[e], Z4))[0];
      if (lane < 16) {
        *(LAS f32x4*)(L + L_SS + w * 512 + lane * 32) = f32x4{cs[0], cs[1], cs[2], cs[3]};
        *(LAS f32x4*)(L + L_SS + w * 512 + lane * 32 + 16) = f32x4{cs[4], cs[5], cs[6], cs[7]};
      }
      lds_set(FL + F_SR + w);
    }
    v8u b8[8];
    f32x4 acc[8];
#pragma unroll
    for (int e = 0; e < 8; ++e) { b8[e] = cat(bt[e], bt[8 + e]); PINV(b8[e]); acc[e] = Z4; PINV(acc[e]); }
    DRAIN();
    if (w == 0) {
      if (isC) {
        const int hl = lane < 24 ? lane : 0;
        watch(rws, WS_HA + (g * 32 + hl) * 128, taglo, taghi, FL + F_GO + hl);
      } else {
        watch8(rws, WS_HB + g * 4096 + (lane & 31) * 128, taglo, taghi, DLY3, FL + F_GO, lane);
      }
      DRAIN();
    } else if (w == 1 && isC) {
      lds_wait(FL + F_SR + (lane & 7));
      if (lane < 8) ST64(hint(taglo, taghi), WS_HB + lane * 4096 + (g * 4 + jq) * 128);
    }
    if (!isC) GATHER_H();
    WGBAR();
    {
      const v4u a0 = *(const LAS v4u*)(L + aa);
#pragma unroll
      for (int e = 0; e < 8; ++e) acc[e] = mfma64(a0, b8[e], acc[e], sidx);
      SCHED();
#pragma unroll
      for (int h = 0; h < 2; ++h)
        *(LAS f32x4*)(L + pa + 16 * h) = f32x4{acc[4 * h][0] + acc[4 * h][1], acc[4 * h + 1][0] + acc[4 * h + 1][1], acc[4 * h + 2][0] + acc[4 * h + 2][1], acc[4 * h + 3][0] + acc[4 * h + 3][1]};
    }
    WGBAR();
    if (!isC) WGBAR();
  } else if (isC) {
    const int v = w & 7, bb = v >> 1, head = 2 * (v & 1) + (lane >> 5), sg = gq & 1;
    unsigned ra = L_PART + (w & 3) * 512 + lane * 8, so = WS_HP + (g * NB + (w & 3)) * 1024 + jq * 256 + lane * 4;
    unsigned rd = WS_OREC + (g * 24 + bb * NSPL + sg) * OREC_SZ + head * 256 + r16 * 16, rs = WS_OREC + (g * 24 + bb * NSPL + sg) * OREC_SZ + 1024 + head * 8;
    unsigned oa = L_OA + bb * 1024 + head * 256 + apos(4 * r16 + 2 * sg) * 4;
    PINV(ra); PINV(so); PINV(rd); PINV(rs); PINV(oa);
    if (w == 15) ST128(SV, WS_HP + (g * NB + gq) * 1024 + jq * 256 + r16 * 16);
    DRAIN();
    {
      unsigned e;
#pragma nounroll
      for (unsigned sp = 0; sp < SPIN_L; ++sp) { e = FL[F_GO + bb * NSPL + (lane < NSPL ? lane : 0)]; if (__all(e != 0u)) break; }
      wait_age(e, DLY2);
    }
    GSPIN {
      v4u od[3];
      v2u st[3];
      CBAR();
#pragma unroll
      for (int u = 0; u < 3; ++u) st[u] = LD64(rs, 2 * u * OREC_SZ);
      SCHED();
#pragma unroll
      for (int u = 0; u < 3; ++u) od[u] = LD128(rd, 2 * u * OREC_SZ);
      SCHED();
      lds_set(FL + F_SR + v);
      SCHED();
      unsigned bad = max(max(max(st[0].x, st[0].y), max(st[1].x, st[1].y)), max(st[2].x, st[2].y));
      const float m0 = __uint_as_float(st[0].x), m1 = __uint_as_float(st[1].x), m2 = __uint_as_float(st[2].x);
      const float mg = x16_max(fmaxf(fmaxf(m0, m1), m2));
      float wt[3] = {__builtin_amdgcn_exp2f(m0 - mg), __builtin_amdgcn_exp2f(m1 - mg), __builtin_amdgcn_exp2f(m2 - mg)};
      const float inv = __builtin_amdgcn_rcpf(x16_sum(fmaf(wt[0], __uint_as_float(st[0].y), fmaf(wt[1], __uint_as_float(st[1].y), wt[2] * __uint_as_float(st[2].y)))));
#pragma unroll
      for (int u = 0; u < 3; ++u) { wt[u] *= inv; PINV(wt[u]); }
      PINV(bad);
      SCHED();
      f32x2 a[4];
#pragma unroll
      for (int u = 0; u < 3; ++u) {
        f32x2 t[4];
#pragma unroll
        for (int d = 0; d < 4; ++d) { t[d] = f32x2{blo(od[u][d]), bhi(od[u][d])}; PINV(t[d]); }
        bad = max(bad, max(max(od[u].x, od[u].y), max(od[u].z, od[u].w)));
#pragma unroll
        for (int d = 0; d < 4; ++d) a[d] = u == 0 ? t[d] * wt[0] : t[d] * wt[u] + a[d];
      }
      float r[4];
#pragma unroll
      for (int d = 0; d < 4; ++d) {
        const auto sw = __builtin_amdgcn_permlane16_swap(__float_as_uint(a[d >> 1][d & 1]), __float_as_uint(a[2 + (d >> 1)][d & 1]), false, false);
        r[d] = __uint_as_float(sw[0]) + __uint_as_float(sw[1]);
      }
      *(LAS unsigned*)(L + oa) = pk(r[0], r[1]);
      *(LAS unsigned*)(L + oa + 16) = pk(r[2], r[3]);
      if (__all(bad != SENT)) break;
    }
    DRAIN();
    WGBAR();
    WGBAR();
    if (w >= 12) {
      f32x2 pv[8];
#pragma unroll
      for (int j = 0; j < 8; ++j) pv[j] = *(const LAS f32x2*)(L + ra + j * 2048);
      SCHED();
      const f32x2 t = ((pv[0] + pv[1]) + (pv[2] + pv[3])) + ((pv[4] + pv[5]) + (pv[6] + pv[7]));
      const unsigned vv = nosent(pk(t.x, t.y));
      const v4u o4 = {dppu<0x00>(vv), dppu<0x55>(vv), dppu<0xAA>(vv), dppu<0xFF>(vv)};
      if ((lane & 3) == 0) ST128(o4, so);
    }
  } else {
    unsigned ra = L_PART + (w & 3) * 512 + lane * 4, hw = L_HH + (w & 3) * 128 + apos(lane >> 1) * 4 + (lane & 1) * 2, ha = L_HH + (r16 >> 2) * 128 + gq * 32 + (lane & 1) * 16;
    int sidx = (lane & 1) ? 0xEEEE : 0x4444;
    PINV(sidx);
    unsigned so = WS_OP + dI * 8192 + gq * 2048 + (w & 7) * 256 + r16 * 16, sa = L_OA + (w & 3) * 1024 + lane * 16;
    PINV(ra); PINV(hw); PINV(ha); PINV(so); PINV(sa);
    ST128(SV, WS_OP + dI * 8192 + (w - 8) * 1024 + lane * 16);
    until(tstart, TWD);
    const rsrc_t rw = mk(wd, FF * DM * 2u);
    v2u src[16];
    v4u bt[8];
#pragma unroll
    for (int j = 0; j < 16; ++j) src[j] = __builtin_amdgcn_raw_buffer_load_b64(rw, (unsigned)(8 * gq + (j & 3)) * 1024 + (w - 8) * 128 + r16 * 8, (64 * dI + (j >> 3) * 32 + (j & 4)) * 1024, AUX_NT);
    SCHED();
    tr4(src, bt);
    tr4(src + 8, bt + 4);
    v8u b8[4];
    f32x4 acc[4];
#pragma unroll
    for (int e = 0; e < 4; ++e) { b8[e] = cat(bt[e], bt[4 + e]); PINV(b8[e]); acc[e] = Z4; PINV(acc[e]); }
    float sgs = 0.f, sus = 0.f;
    if (w >= 12) {
      lds_wait(FL + F_SR + (lane & 7));
#pragma unroll
      for (int j = 0; j < 8; ++j) { sgs += *(const LAS float*)(L + L_SS + j * 512 + lane * 4); sus += *(const LAS float*)(L + L_SS + j * 512 + 256 + lane * 4); }
    }
    PINV(sgs); PINV(sus);
    DRAIN();
    lds_set(FL + F_CL + w - 8);
    if (w == 10) lds_wait(FL + F_CL + (lane & 7));
    GATHER_H();
    WGBAR();
    if (w >= 12) {
      const f32x2 st = ln_stats(*(const LAS v4u*)(L + sa));
      float kg = st.y * -1.4426950408889634f, cg = st.x * sgs, cu = st.x * sus, ku = st.y;
      PINV(kg); PINV(cg); PINV(cu); PINV(ku);
      WGBAR();
      float gp[8], up[8];
#pragma unroll
      for (int j = 0; j < 8; ++j) { gp[j] = *(const LAS float*)(L + ra + j * 2048); up[j] = *(const LAS float*)(L + ra + j * 2048 + 256); }
      SCHED();
      const float gg = ((gp[0] + gp[1]) + (gp[2] + gp[3])) + ((gp[4] + gp[5]) + (gp[6] + gp[7]));
      const float uu = ((up[0] + up[1]) + (up[2] + up[3])) + ((up[4] + up[5]) + (up[6] + up[7]));
      const float hh = ku * (uu - cu) * __builtin_amdgcn_rcpf(1.f + __builtin_amdgcn_exp2f(kg * (gg - cg)));
      *(LAS u16*)(L + hw) = (u16)pk(hh, hh);
    } else {
      WGBAR();
    }
    WGBAR();
    const v4u a0 = *(const LAS v4u*)(L + ha);
#pragma unroll
    for (int e = 0; e < 4; ++e) acc[e] = mfma64(a0, b8[e], acc[e], sidx);
    SCHED();
    ST128((v4u{nosent(__float_as_uint(acc[0][0] + acc[0][1])), nosent(__float_as_uint(acc[1][0] + acc[1][1])),
               nosent(__float_as_uint(acc[2][0] + acc[2][1])), nosent(__float_as_uint(acc[3][0] + acc[3][1]))}), so);
  }
}
}

extern "C" void kernel_launch(void* const* d_in, const int* in_sizes, int n_in, void* d_out, int out_size, void* d_ws, size_t ws_size, hipStream_t stream) {
  (void)in_sizes; (void)n_in; (void)out_size; (void)ws_size;
  fused_decode<<<dim3(256), dim3(1024), 0, stream>>>((const u16*)d_in[0], (const u16*)d_in[1], (const u16*)d_in[2], (const u16*)d_in[3], (const u16*)d_in[4],
                                                     (const u16*)d_in[6], (const u16*)d_in[7], (const u16*)d_in[8], (u16*)d_out, (unsigned char*)d_ws);
}
```
